```python
import torch

def _softmax(x, axis=-1):
    return torch.softmax(x, dim=axis)
def _gelu(x):
    return torch.nn.functional.gelu(x, approximate="tanh")
def _silu(x):
    return torch.nn.functional.silu(x)
def _sigmoid(x):
    return torch.sigmoid(x)
def _layernorm(x, axis=-1, eps=1e-5):
    m = x.mean(dim=axis, keepdim=True)
    v = x.var(dim=axis, keepdim=True, unbiased=False)
    return (x - m) / torch.sqrt(v + eps)
def _scatter_add(tgt, idx, src, axis=0):
    return tgt.clone().index_add_(axis, idx, src)
def _segment_sum(src, seg, n):
    out = torch.zeros((n,) + src.shape[1:], dtype=src.dtype)
    return out.index_add_(0, seg, src)
def _pad(x, pw):
    flat = tuple(v for pair in reversed(pw) for v in pair)
    return torch.nn.functional.pad(x, flat)

kernel_name = 'gated_mix__compressed_kv_cla__ssm_conv_h3__prompt'

def setup_inputs():

    _w = (lambda a: (lambda t: t.to(torch.bfloat16) if t.is_floating_point() else t)(torch.as_tensor(a)))
    def _A_setup():
        B, H, Sq, Sk, D, M = 1, 32, 2048, 2048, 128, 640
        Dc = 4 * D
        return {
            "x": _w((lambda *s: torch.randn(*s, dtype=torch.bfloat16) * 0.05)(B, Sq, M)),
            "c_kv": _w((lambda *s: torch.randn(*s, dtype=torch.bfloat16) * 0.4)(B, Sk, Dc)),
            "w_q": _w((lambda *s: torch.randn(*s, dtype=torch.bfloat16) * 6.4)(M, H, D) * (M ** -0.5)),
            "w_uk": _w((lambda *s: torch.randn(*s, dtype=torch.bfloat16) * 6.4)(Dc, H, D) * (Dc ** -0.5)),
            "w_uv": _w((lambda *s: torch.randn(*s, dtype=torch.bfloat16) * 0.4)(Dc, H, D) * (Dc ** -0.5)),
            "w_o": _w((lambda *s: torch.randn(*s, dtype=torch.bfloat16) * 0.4)(H, D, M) * ((H * D) ** -0.5)),
        }
    _A_in = _A_setup()

    def _B_setup():
        B, S, D = 1, 2048, 640
        ks = 4
        return {
            "q": _w((lambda *s: torch.randn(*s, dtype=torch.bfloat16) * 0.05)(B, S, D)),
            "k": _w((lambda *s: torch.randn(*s, dtype=torch.bfloat16) * 0.4)(B, S, D)),
            "v": _w((lambda *s: torch.randn(*s, dtype=torch.bfloat16) * 0.4)(B, S, D)),
            "shift_k": _w((lambda *s: torch.randn(*s, dtype=torch.bfloat16) * 0.4)(D, ks) * 0.5),
            "ssm_k": _w((lambda *s: torch.randn(*s, dtype=torch.bfloat16) * 0.8)(D, S) * (1.0 / S ** 0.5)),
            "ssm_D": _w((lambda *s: torch.randn(*s, dtype=torch.bfloat16) * 0.4)(D)),
        }
    _B_in = _B_setup()

    return {
        "A_x": _A_in["x"],
        "A_c_kv": _A_in["c_kv"],
        "A_w_q": _A_in["w_q"],
        "A_w_uk": _A_in["w_uk"],
        "A_w_uv": _A_in["w_uv"],
        "A_w_o": _A_in["w_o"],
        "B_q": _B_in["q"],
        "B_k": _B_in["k"],
        "B_v": _B_in["v"],
        "B_shift_k": _B_in["shift_k"],
        "B_ssm_k": _B_in["ssm_k"],
        "B_ssm_D": _B_in["ssm_D"],
        "gate_w": _w((lambda *s: torch.randn(*s, dtype=torch.bfloat16) * 0.05)(640) * 16.0),
    }

def reference(A_x, A_c_kv, A_w_q, A_w_uk, A_w_uv, A_w_o, B_q, B_k, B_v, B_shift_k, B_ssm_k, B_ssm_D, gate_w):
    def _A_ref(x, c_kv, w_q, w_uk, w_uv, w_o):
        B, Sq, M = x.shape[0], x.shape[1], x.shape[2]
        Sk = c_kv.shape[1]
        H, D = w_q.shape[1], w_q.shape[2]
        q = torch.einsum("bqm,mhd->bhqd", x, w_q)
        k = torch.einsum("bkc,chd->bhkd", c_kv, w_uk)
        v = torch.einsum("bkc,chd->bhkd", c_kv, w_uv)
        scores = torch.einsum("bhqd,bhkd->bhqk", q, k) * (D ** -0.5)
        off = Sk - Sq
        qi = torch.arange(Sq)[:, None]
        kj = torch.arange(Sk)[None, :]
        cmask = kj <= (qi + off)
        scores = torch.where(cmask[None, None], scores, -1e9)
        p = _softmax(scores, axis=-1)
        ctx = torch.einsum("bhqk,bhkd->bhqd", p, v)
        return torch.einsum("bhqd,hdm->bqm", ctx, w_o)

    def _B_ref(q, k, v, shift_k, ssm_k, ssm_D):
        B, S, D = q.shape
        ks = shift_k.shape[-1]
        k_pad = _pad(k, ((0, 0), (ks - 1, 0), (0, 0)))
        k_shift = torch.zeros_like(k)
        for t in range(ks):
            k_shift = k_shift + k_pad[:, t:t + S, :] * shift_k[:, ks - 1 - t][None, None, :]
        kv = k_shift * v
        L = 2 * S
        kv_pad = _pad(kv, ((0, 0), (0, S), (0, 0)))
        kf_pad = _pad(ssm_k, ((0, 0), (0, S)))
        kv_f = (lambda x, axes=(-2,-1): torch.fft.rfft2(x.float(), dim=axes))(kv_pad[:, None, :, :], axes=(1, 2))
        kf_f = (lambda x, axes=(-2,-1): torch.fft.rfft2(x.float(), dim=axes))(kf_pad[:, None, :], axes=(1, 2))
        kf_f = (lambda x, *d: x.permute(*d))(kf_f, 1, 2, 0)
        y_f = kv_f * kf_f[None, :, :, :]
        y = (lambda x, s, axes=(-2,-1): torch.fft.irfft2(x, s=s, dim=axes).to(torch.bfloat16))(y_f, (1, L), axes=(1, 2))[:, 0, :S, :]
        y = y + kv * ssm_D[None, None, :]
        return q * y

    x = A_x
    g = _sigmoid((x * gate_w).sum(-1, keepdims=True))
    a = _A_ref(x, A_c_kv, A_w_q, A_w_uk, A_w_uv, A_w_o)
    b = _B_ref(x, B_k, B_v, B_shift_k, B_ssm_k, B_ssm_D)
    return g * a + (1 - g) * b
```

```cpp
#include <hip/hip_runtime.h>
#include <hip/hip_bf16.h>
#include <stdint.h>

typedef unsigned short u16;
__device__ __forceinline__ float bf2f(u16 b) { return __uint_as_float(((unsigned)b) << 16); }
__device__ __forceinline__ u16 f2bf(float f) { unsigned u = __float_as_uint(f); u += 0x7FFFu + ((u >> 16) & 1u); return (u16)(u >> 16); }
__device__ __forceinline__ float rb(float f) { return bf2f(f2bf(f)); }

constexpr int S = 2048, H = 32, D = 128, M = 640, DC = 512, HD = H * D;

__global__ void naive_gemm(const u16* A, const u16* B, u16* C, int Mm, int N, int K) {
    __shared__ float As[16][17], Bs[16][17];
    int tx = threadIdx.x, ty = threadIdx.y;
    int n = blockIdx.x * 16 + tx, m = blockIdx.y * 16 + ty;
    float acc = 0.f;
    for (int k0 = 0; k0 < K; k0 += 16) {
        As[ty][tx] = bf2f(A[(size_t)m * K + k0 + tx]);
        Bs[ty][tx] = bf2f(B[(size_t)(k0 + ty) * N + n]);
        __syncthreads();
        for (int k = 0; k < 16; ++k) acc += As[ty][k] * Bs[k][tx];
        __syncthreads();
    }
    C[(size_t)m * N + n] = f2bf(acc);
}

__global__ void naive_attn(const u16* Q, const u16* K, const u16* V, u16* ctx) {
    __shared__ float qs[D];
    __shared__ float p[S];
    __shared__ float red[128];
    int qi = blockIdx.x, h = blockIdx.y, t = threadIdx.x;
    qs[t] = bf2f(Q[(size_t)qi * HD + h * D + t]);
    __syncthreads();
    float mx = -1e30f;
    for (int j = t; j <= qi; j += 128) {
        const u16* kr = K + (size_t)j * HD + h * D;
        float s = 0.f;
        for (int d = 0; d < D; ++d) s += qs[d] * bf2f(kr[d]);
        s = rb(rb(s) * 0.08838834764831845f);
        p[j] = s; mx = fmaxf(mx, s);
    }
    red[t] = mx; __syncthreads();
    for (int o = 64; o > 0; o >>= 1) { if (t < o) red[t] = fmaxf(red[t], red[t + o]); __syncthreads(); }
    mx = red[0]; __syncthreads();
    float sum = 0.f;
    for (int j = t; j <= qi; j += 128) { float e = expf(p[j] - mx); p[j] = e; sum += e; }
    red[t] = sum; __syncthreads();
    for (int o = 64; o > 0; o >>= 1) { if (t < o) red[t] += red[t + o]; __syncthreads(); }
    sum = red[0];
    float inv = 1.f / sum;
    float acc = 0.f;
    for (int j = 0; j <= qi; ++j) acc += rb(p[j] * inv) * bf2f(V[(size_t)j * HD + h * D + t]);
    ctx[(size_t)qi * HD + h * D + t] = f2bf(acc);
}

__global__ void naive_kv(const u16* k, const u16* v, const u16* shift_k, u16* kv) {
    int c = blockIdx.x * 128 + threadIdx.x, s = blockIdx.y;
    float ks = 0.f;
    for (int t = 0; t < 4; ++t) {
        int si = s + t - 3;
        float kp = si >= 0 ? bf2f(k[(size_t)si * M + c]) : 0.f;
        ks = rb(ks + rb(kp * bf2f(shift_k[c * 4 + 3 - t])));
    }
    kv[(size_t)s * M + c] = f2bf(ks * bf2f(v[(size_t)s * M + c]));
}

__global__ void naive_final(const u16* x, const u16* a, const u16* q, const u16* kv, const u16* ssm_k, const u16* ssm_D, const u16* gate_w, u16* out) {
    __shared__ float red[1024];
    int s = blockIdx.x, c = threadIdx.x;
    red[c] = rb(bf2f(x[(size_t)s * M + c]) * bf2f(gate_w[c]));
    if (c < 1024 - 640) red[640 + c] = 0.f;
    __syncthreads();
    for (int o = 512; o > 0; o >>= 1) { if (c < o) red[c] += red[c + o]; if (o == 512 && c + 0 < 512 && false) {} __syncthreads(); }
    float gs = rb(red[0]);
    float g = rb(1.f / (1.f + expf(-gs)));
    float y = 0.f;
    for (int j = 0; j <= s; ++j) y += bf2f(ssm_k[(size_t)c * S + (s - j)]) * bf2f(kv[(size_t)j * M + c]);
    y = rb(y);
    float kvv = bf2f(kv[(size_t)s * M + c]);
    y = rb(y + rb(kvv * bf2f(ssm_D[c])));
    float b = rb(bf2f(q[(size_t)s * M + c]) * y);
    float r = rb(rb(g * bf2f(a[(size_t)s * M + c])) + rb(rb(1.f - g) * b));
    out[(size_t)s * M + c] = f2bf(r);
}

extern "C" void kernel_launch(void* const* d_in, const int* in_sizes, int n_in,
                              void* d_out, int out_size, void* d_ws, size_t ws_size,
                              hipStream_t stream) {
    const u16* x = (const u16*)d_in[0];
    const u16* ckv = (const u16*)d_in[1];
    const u16* wq = (const u16*)d_in[2];
    const u16* wuk = (const u16*)d_in[3];
    const u16* wuv = (const u16*)d_in[4];
    const u16* wo = (const u16*)d_in[5];
    const u16* bq = (const u16*)d_in[6];
    const u16* bk = (const u16*)d_in[7];
    const u16* bv = (const u16*)d_in[8];
    const u16* shk = (const u16*)d_in[9];
    const u16* ssmk = (const u16*)d_in[10];
    const u16* ssmD = (const u16*)d_in[11];
    const u16* gw = (const u16*)d_in[12];
    u16* ws = (u16*)d_ws;
    u16* Q = ws; u16* K = Q + (size_t)S * HD; u16* V = K + (size_t)S * HD; u16* ctx = V + (size_t)S * HD;
    u16* a = ctx + (size_t)S * HD; u16* kv = a + (size_t)S * M;
    naive_gemm<<<dim3(HD / 16, S / 16), dim3(16, 16), 0, stream>>>(x, wq, Q, S, HD, M);
    naive_gemm<<<dim3(HD / 16, S / 16), dim3(16, 16), 0, stream>>>(ckv, wuk, K, S, HD, DC);
    naive_gemm<<<dim3(HD / 16, S / 16), dim3(16, 16), 0, stream>>>(ckv, wuv, V, S, HD, DC);
    naive_attn<<<dim3(S, H), 128, 0, stream>>>(Q, K, V, ctx);
    naive_gemm<<<dim3(M / 16, S / 16), dim3(16, 16), 0, stream>>>(ctx, wo, a, S, M, HD);
    naive_kv<<<dim3(M / 128, S), 128, 0, stream>>>(bk, bv, shk, kv);
    naive_final<<<S, 640, 0, stream>>>(x, a, x, kv, ssmk, ssmD, gw, (u16*)d_out);
}
```

```cpp
#include <hip/hip_runtime.h>
#include <hip/hip_bf16.h>
#include <stdint.h>

typedef unsigned short u16;
typedef short bf16x8 __attribute__((ext_vector_type(8)));
typedef short s16x4 __attribute__((ext_vector_type(4)));
typedef float f32x16 __attribute__((ext_vector_type(16)));
typedef float f32x4 __attribute__((ext_vector_type(4)));
typedef unsigned u32x4 __attribute__((ext_vector_type(4)));
typedef short v4i16_t __attribute__((ext_vector_type(4)));
#define LAS __attribute__((address_space(3)))
typedef LAS const char* lds_cptr;
typedef LAS char* lds_ptr;
#define DI __device__ __forceinline__
#define SBAR() __builtin_amdgcn_sched_barrier(0)
#define PIN(x) asm volatile("" : "+v"(x))
#define MFMA32(a, b, c) __builtin_amdgcn_mfma_f32_32x32x16_bf16(a, b, c, 0, 0, 0)

constexpr int S = 2048, H = 32, D = 128, M = 640, DC = 512, HD = H * D;
constexpr int LDS_BYTES = 163840;
#define STAMP(k, i)

DI float bf2f(u16 b) { return __uint_as_float(((unsigned)b) << 16); }
DI u16 f2bf(float f) { unsigned u = __float_as_uint(f); u += 0x7FFFu + ((u >> 16) & 1u); return (u16)(u >> 16); }
DI float rb(float f) { return bf2f(f2bf(f)); }
typedef float f32x2 __attribute__((ext_vector_type(2)));
typedef __bf16 bf16x2_t __attribute__((ext_vector_type(2)));
DI unsigned cvtpk(float lo, float hi) { f32x2 v = {lo, hi}; bf16x2_t b = __builtin_convertvector(v, bf16x2_t); return __builtin_bit_cast(unsigned, b); }
DI unsigned rfl(unsigned x) { return __builtin_amdgcn_readfirstlane(x); }

DI void dma1(unsigned ldsaddr, unsigned voff, const void* sbase) {
    asm volatile("s_mov_b32 m0, %0\n\ts_nop 0\n\tglobal_load_lds_dwordx4 %1, %2" :: "s"(ldsaddr), "v"(voff), "s"(sbase) : "memory");
}
DI void dma4(unsigned ldsaddr, unsigned voff, const void* sbase) {
    asm volatile("s_mov_b32 m0, %0\n\ts_nop 0\n\tglobal_load_lds_dwordx4 %1, %2\n\tglobal_load_lds_dwordx4 %1, %2 offset:1024\n\t"
                 "global_load_lds_dwordx4 %1, %2 offset:2048\n\tglobal_load_lds_dwordx4 %1, %2 offset:3072" :: "s"(ldsaddr), "v"(voff), "s"(sbase) : "memory");
}
template <int J> DI void dma_lin(unsigned ldsaddr, unsigned voff, const void* sbase) {
    asm volatile("s_mov_b32 m0, %0\n\ts_nop 0\n\tglobal_load_lds_dwordx4 %1, %2 offset:%c3" :: "s"(ldsaddr), "v"(voff), "s"(sbase), "i"(J * 1024) : "memory");
}
template <int N> DI void wait_bar_v() { asm volatile("s_waitcnt vmcnt(%0)\n\ts_barrier" :: "i"(N) : "memory"); }
template <int N> DI void wait_bar() { asm volatile("s_waitcnt vmcnt(%0) lgkmcnt(0)\n\ts_barrier" :: "i"(N) : "memory"); }
DI bf16x8 lds_frag(lds_cptr p) { return *(const LAS bf16x8*)p; }
DI s16x4 vtr(lds_cptr p) { return __builtin_bit_cast(s16x4, __builtin_amdgcn_ds_read_tr16_b64_v4i16((LAS v4i16_t*)p)); }
DI bf16x8 tr_frag(lds_cptr p) { const s16x4 lo = vtr(p), hi = vtr(p + 512); return __builtin_shufflevector(lo, hi, 0, 1, 2, 3, 4, 5, 6, 7); }
DI u32x4 pack8(const f32x16& x, int s) {
    u32x4 w; w[0] = cvtpk(x[8 * s + 0], x[8 * s + 1]); w[1] = cvtpk(x[8 * s + 2], x[8 * s + 3]); w[2] = cvtpk(x[8 * s + 4], x[8 * s + 5]); w[3] = cvtpk(x[8 * s + 6], x[8 * s + 7]); return w;
}

constexpr int TSLOT = 32 * 1040;
template <int MAT>
DI void kvproj_body(const u16* ckv, const u16* wuk, const u16* wuv, char* dst, int h, int r, char* lds, int wid, int lane) {
    const unsigned lds0 = (unsigned)(uintptr_t)lds;
    const char* wsrc = (const char*)((wid >> 2) ? wuv : wuk) + (size_t)h * 256 + ((wid >> 1) & 1) * 128 + (size_t)(wid & 1) * 4 * 8 * 8192;
    const unsigned wvoff = (unsigned)(lane >> 3) * 8192u + (unsigned)(((lane & 7) ^ (((lane >> 4) & 1) << 2)) * 16);
    const unsigned wlds = lds0 + ((wid >> 2) * 2 + ((wid >> 1) & 1)) * 8192 + (wid & 1) * 4 * 1024;
    auto dma_w = [&](int c, int slot) {
#pragma unroll
        for (int i = 0; i < 4; ++i) dma1(wlds + slot * TSLOT + i * 1024, wvoff, wsrc + (size_t)c * 64 * 8192 + (size_t)i * 8 * 8192);
    };
    const char* tsrc = (const char*)ckv + (size_t)(256 * r + 4 * wid) * 1024;
    const unsigned tvoff = lane * 16;
    const unsigned tlds = lds0 + 4 * wid * 1040;
    auto dma_t = [&](int i, int slot) {
        const char* g = tsrc + (size_t)i * 32 * 1024; const unsigned l = tlds + slot * TSLOT;
        asm volatile("s_mov_b32 m0, %0\n\ts_nop 0\n\tglobal_load_lds_dwordx4 %1, %2\n\t"
                     "s_add_u32 m0, m0, 16\n\ts_nop 0\n\tglobal_load_lds_dwordx4 %1, %2 offset:1024\n\t"
                     "s_add_u32 m0, m0, 16\n\ts_nop 0\n\tglobal_load_lds_dwordx4 %1, %2 offset:2048\n\t"
                     "s_add_u32 m0, m0, 16\n\ts_nop 0\n\tglobal_load_lds_dwordx4 %1, %2 offset:3072" :: "s"(l), "v"(tvoff), "s"(g) : "memory", "scc");
    };
    const int g4 = lane >> 4, q = (lane & 15) >> 2, p = lane & 3;
    const lds_cptr wrd = (lds_cptr)lds + (MAT * 2 + ((wid >> 1) & 1)) * 8192 + (8 * (g4 >> 1) + q) * 128 + (((wid & 1) ^ (q >> 1)) * 64) + (g4 & 1) * 32 + p * 8;
    const lds_cptr trd = (lds_cptr)lds + (lane & 31) * 1040 + (lane >> 5) * 16;
    char* dstw = dst + ((size_t)(h * 64 + 8 * r) * 8 + 2 * (wid & 3)) * 1024 + lane * 16;

    bf16x8 Wf[32];
    dma_t(0, 3); dma_w(0, 0); dma_w(1, 1); dma_w(2, 2);
    f32x16 acc = f32x16{};
#pragma unroll
    for (int c = 0; c < 8; ++c) {
        if (c == 0) wait_bar<8>(); else wait_bar<4>();
        if (c >= 1) { if (c + 2 <= 7) dma_w(c + 2, (c + 2) % 3); else dma_t(c + 2 - 7, (c + 2) % 3); }
#pragma unroll
        for (int s = 0; s < 4; ++s) {
            Wf[4 * c + s] = tr_frag(wrd + (c % 3) * TSLOT + s * 2048);
            const bf16x8 a = lds_frag(trd + 3 * TSLOT + (4 * c + s) * 32);
            acc = MAT == 0 ? MFMA32(Wf[4 * c + s], a, acc) : MFMA32(a, Wf[4 * c + s], acc);
        }
    }
    *(u32x4*)(dstw) = pack8(acc, 0); *(u32x4*)(dstw + 1024) = pack8(acc, 1);
    STAMP(0, 1);
#pragma unroll
    for (int i = 1; i < 8; ++i) {
        const int j = 7 + i;
        if (i == 1) wait_bar<6>(); else if (i < 7) wait_bar<8>(); else wait_bar<4>();
        acc = f32x16{};
        {
            const lds_cptr tb = trd + (j % 3) * TSLOT;
            const char* gnext = tsrc + (size_t)(i + 2) * 32 * 1024; const unsigned lnext = tlds + ((j + 2) % 3) * TSLOT;
            bf16x8 af[4];
#pragma unroll
            for (int s = 0; s < 4; ++s) af[s] = lds_frag(tb + s * 32);
            SBAR();
#pragma unroll
            for (int s = 0; s < 32; ++s) {
                acc = MAT == 0 ? MFMA32(Wf[s], af[s & 3], acc) : MFMA32(af[s & 3], Wf[s], acc); PIN(acc);
                if (s + 4 < 32) af[s & 3] = lds_frag(tb + (s + 4) * 32);
                if (i + 2 <= 7) {
                    if (s == 1) dma_lin<0>(lnext, tvoff, gnext);
                    if (s == 7) dma_lin<1>(lnext + 16, tvoff, gnext);
                    if (s == 13) dma_lin<2>(lnext + 32, tvoff, gnext);
                    if (s == 19) dma_lin<3>(lnext + 48, tvoff, gnext);
                }
                SBAR();
            }
        }
        *(u32x4*)(dstw + i * 8192) = pack8(acc, 0); *(u32x4*)(dstw + i * 8192 + 1024) = pack8(acc, 1);
    }
}
__global__ __launch_bounds__(512, 2) void k_kvproj(const u16* ckv, const u16* wuk, const u16* wuv, char* Kws, char* Vws) {
    extern __shared__ __attribute__((aligned(16))) char lds[];
    const int lane = threadIdx.x & 63, wid = rfl(threadIdx.x >> 6);
    const int xcd = blockIdx.x & 7, idx = blockIdx.x >> 3, h = xcd * 4 + (idx >> 3), r = idx & 7;
    STAMP(0, 0);
    if (wid < 4) kvproj_body<0>(ckv, wuk, wuv, Kws, h, r, lds, wid, lane);
    else kvproj_body<1>(ckv, wuk, wuv, Vws, h, r, lds, wid, lane);
    STAMP(0, 3);
}

constexpr float QSCALE = 0.08838834764831845f * 1.4426950408889634f;
constexpr float THR = 8.f;
constexpr int QCH = 49152;
constexpr int KRING = 0, VRING = 65536, SPARE = 131072, QA_OFF = VRING + 32768;
DI int crow(int r, int hi) { return (r & 3) + 8 * (r >> 2) + 4 * hi; }
#define MX3(a, b, c) __builtin_fmaxf(__builtin_fmaxf((a), (b)), (c))
DI float rowmax(const f32x16& p0, const f32x16& p1) {
    float a = MX3(p0[0], p0[1], p1[0]), b = MX3(p0[2], p0[3], p1[1]); a = MX3(a, p1[2], p1[3]);
#pragma unroll
    for (int r = 4; r < 16; r += 4) { a = MX3(a, p0[r], p0[r + 1]); b = MX3(b, p0[r + 2], p0[r + 3]); a = MX3(a, p1[r], p1[r + 1]); b = MX3(b, p1[r + 2], p1[r + 3]); }
    float m = __builtin_fmaxf(a, b); auto rr = __builtin_amdgcn_permlane32_swap(__float_as_uint(m), __float_as_uint(m), false, false);
    return __builtin_fmaxf(__uint_as_float(rr[0]), __uint_as_float(rr[1]));
}

__global__ __launch_bounds__(512, 2) void k_attn(const u16* x, const u16* wq, const char* Kws, const char* Vws, char* ctx) {
    extern __shared__ __attribute__((aligned(16))) char lds[];
    const int lane = threadIdx.x & 63, wid = rfl(threadIdx.x >> 6);
    const int xcd = blockIdx.x & 7, idx = blockIdx.x >> 3, h = xcd * 4 + (idx >> 3), pr = idx & 7;
    const unsigned lds0 = (unsigned)(uintptr_t)lds;
    const int r32 = lane & 31, hi = lane >> 5, rg = wid & 3, par = wid >> 2;
    const int blkA = 15 - pr, blkB = pr;
    bf16x8 qf[8];
    STAMP(1, 0);
    {
        const int myblk = par ? blkB : blkA;
        const char* xsrc = (const char*)x + (size_t)(128 * myblk + 32 * rg) * 1280;
        const unsigned xvoff = (unsigned)(lane >> 3) * 1280u + (unsigned)(((lane & 7) ^ ((lane >> 4) & 7)) * 16);
        const unsigned xvoff2 = (unsigned)(lane >> 3) * 1280u + (unsigned)(((lane & 7) ^ ((4 + (lane >> 4)) & 7)) * 16);
        const unsigned xlds = lds0 + wid * 4096;
        const char* wsrc = (const char*)wq + (size_t)h * 256 + (wid >> 2) * 128 + (size_t)(wid & 3) * 2 * 8 * 8192;
        const unsigned wvoff = (unsigned)(lane >> 3) * 8192u + (unsigned)(((lane & 7) ^ (((lane >> 4) & 1) << 2)) * 16);
        const unsigned wlds = lds0 + 32768 + (wid >> 2) * 8192 + (wid & 3) * 2048;
        auto dma_c = [&](int c, int slot) {
#pragma unroll
            for (int i = 0; i < 4; ++i) dma1(xlds + slot * QCH + i * 1024, (i & 1) ? xvoff2 : xvoff, xsrc + (size_t)c * 128 + (size_t)i * 8 * 1280);
#pragma unroll
            for (int i = 0; i < 2; ++i) dma1(wlds + slot * QCH + i * 1024, wvoff, wsrc + (size_t)c * 64 * 8192 + (size_t)i * 8 * 8192);
        };
        const int g4 = lane >> 4, q = (lane & 15) >> 2, p = lane & 3;
        const lds_cptr wrd = (lds_cptr)lds + 32768 + (8 * (g4 >> 1) + q) * 128 + (g4 & 1) * 32 + p * 8;
        const int hsw = (q >> 1) & 1;
        const int ysw = hi ^ ((r32 >> 1) & 7);
        const lds_cptr xrd = (lds_cptr)lds + wid * 4096 + r32 * 128;
        f32x16 acc[4] = {};
        dma_c(0, 0); dma_c(1, 1);
        auto dma_p = [&](int c, int slot, int i) {
            if (i < 4) dma1(xlds + slot * QCH + i * 1024, (i & 1) ? xvoff2 : xvoff, xsrc + (size_t)c * 128 + (size_t)i * 8 * 1280);
            else dma1(wlds + slot * QCH + (i - 4) * 1024, wvoff, wsrc + (size_t)c * 64 * 8192 + (size_t)(i - 4) * 8 * 8192);
        };
        int slot = 0;
#pragma unroll 1
        for (int c = 0; c < 10; ++c) {
            if (c < 8) wait_bar<6>(); else wait_bar<0>();
            const int sn = slot == 0 ? 2 : slot - 1;
            const int cn = c + 2 < 10 ? c + 2 : 9;
            const lds_cptr xb = xrd + slot * QCH; const lds_cptr wb = wrd + slot * QCH;
            const lds_cptr wb0 = wb + hsw * 64, wb1 = wb + (hsw ^ 1) * 64;
#define WFR(i) tr_frag((((i) & 1) ? wb1 : wb0) + (((i) >> 1) & 1) * 8192 + ((i) >> 2) * 2048)
            bf16x8 wf[4], xf[2];
            xf[0] = lds_frag(xb + ((0 ^ ysw) << 4));
#pragma unroll
            for (int i = 0; i < 4; ++i) wf[i] = WFR(i);
            SBAR();
#pragma unroll
            for (int i = 0; i < 16; ++i) {
                acc[i & 3] = MFMA32(wf[i & 3], xf[(i >> 2) & 1], acc[i & 3]); PIN(acc[i & 3]);
                if (i + 4 < 16) wf[i & 3] = WFR(i + 4);
                if ((i & 3) == 0 && i + 4 < 16) xf[((i >> 2) + 1) & 1] = lds_frag(xb + (((2 * ((i >> 2) + 1)) ^ ysw) << 4));
                if (c < 8) { if (i == 0) dma_p(cn, sn, 0); if (i == 3) dma_p(cn, sn, 1); if (i == 5) dma_p(cn, sn, 2); if (i == 8) dma_p(cn, sn, 3); if (i == 10) dma_p(cn, sn, 4); if (i == 13) dma_p(cn, sn, 5); }
                SBAR();
            }
            slot = slot == 2 ? 0 : slot + 1;
        }
        wait_bar<0>();
        lds_ptr qw = (lds_ptr)lds + (par ? SPARE : QA_OFF) + rg * 8192 + lane * 16;
#pragma unroll
        for (int db = 0; db < 4; ++db) {
#pragma unroll
            for (int e = 0; e < 16; ++e) acc[db][e] *= QSCALE;
            *(LAS u32x4*)(qw + (2 * db) * 1024) = pack8(acc[db], 0);
            *(LAS u32x4*)(qw + (2 * db + 1) * 1024) = pack8(acc[db], 1);
        }
    }
    STAMP(1, 1);
    const char* Kh = Kws + (size_t)h * 64 * 8192;
    const char* Vh = Vws + (size_t)h * 64 * 8192;
    const unsigned tvoff = lane * 16;
    const unsigned kl0 = lds0 + KRING + wid * 2048, vl0 = lds0 + VRING + wid * 2048;
    const char* Kw = Kh + wid * 2048; const char* Vw = Vh + wid * 2048;
    const lds_cptr krd = (lds_cptr)lds + KRING + par * 8192 + lane * 16;
    const lds_cptr vrd = (lds_cptr)lds + VRING + par * 8192 + lane * 16;
#define DMA_K(u, g) do { dma_lin<0>(kl0 + ((g) & 3) * 16384, tvoff, Kw + (size_t)(u) * 16384); dma_lin<1>(kl0 + ((g) & 3) * 16384, tvoff, Kw + (size_t)(u) * 16384); } while (0)
#define DMA_V(u, g) do { dma_lin<0>(vl0 + ((g) & 3) * 16384, tvoff, Vw + (size_t)(u) * 16384); dma_lin<1>(vl0 + ((g) & 3) * 16384, tvoff, Vw + (size_t)(u) * 16384); } while (0)
    DMA_K(0, 0); DMA_K(1, 1); DMA_K(2, 2); DMA_K(3, 3);
    int g0 = 0;
#define A_LDS 1
#define A_DMA 1
#define VF(i) ((((i) & 3) * 2 + ((i) >> 2)) * 1024)
#define EXP2(v) __builtin_amdgcn_exp2f(v)
#define LGKM(n) __builtin_amdgcn_s_waitcnt(0xC07F | ((n) << 8))
#define MFMA_C(D, A, B, C) asm volatile("v_mfma_f32_32x32x16_bf16 %0, %2, %3, %1" : "=&v"(D), "+v"(C) : "v"(A), "v"(B))
#define SETM0(base, imm) asm volatile("s_add_u32 m0, %0, %1" :: "s"(base), "i"(imm) : "scc")
#define GLDS(voff, sbase, imm) asm volatile("global_load_lds_dwordx4 %0, %1 offset:%c2" :: "v"(voff), "s"(sbase), "i"(imm) : "memory")
#define SLOT(X, Y, PN, PP, ODD, QK, SM, PV, FIRST, DM, PFK, MASK) do { \
        unsigned long long fire = 0; \
        _Pragma("unroll") for (int i = 0; i < 16; ++i) { \
            if (A_LDS && i < 8 && QK && !(i & 1)) { LGKM(2); SBAR(); } \
            if (A_LDS && i >= 8 && PV && !(i & 1)) { LGKM(2); SBAR(); } \
            if (i < 8) { if (QK) { if (i == 0) { if (FIRST) X = MFMA32(kf[0], qf[0], zero16); else MFMA_C(X, kf[0], qf[0], negm); } else X = MFMA32(kf[i & 3], qf[i], X); PIN(X); \
                                   if (A_LDS && i < 4) kf[i] = lds_frag(kp + ((ODD) ? 0 : 16384) + (i + 4) * 1024); } \
                         if (A_LDS && PV && i >= 4) vf[i - 4] = lds_frag(vp + ((ODD) ? 0 : 16384) + VF(i - 4)); } \
            else { if (PV) { o[i & 3] = MFMA32(vf[i & 3], __builtin_bit_cast(bf16x8, PP[(i - 8) >> 2]), o[i & 3]); PIN(o[i & 3]); if (A_LDS && i < 12) vf[i & 3] = lds_frag(vp + ((ODD) ? 0 : 16384) + VF(i - 4)); } \
                   if (A_LDS && PFK && i >= 12) kf[i - 12] = lds_frag(kp + ((ODD) ? 16384 : 0) + (i - 12) * 1024); } \
            if (!(ODD) && i == 5) { kp = (lds_cptr)((unsigned)(uintptr_t)kp ^ 0x8000u); PIN(kp); } \
            if (!(ODD) && i == 13) { vp = (lds_cptr)((unsigned)(uintptr_t)vp ^ 0x8000u); PIN(vp); } \
            if (DM && A_DMA) { \
                if (i == 0) SETM0(dk, (ODD) ? 16384 : 0); \
                if (i == 1) GLDS(uoff, Kw, 0); \
                if (i == 3) GLDS(uoff, Kw, 1024); \
                if (i == 4) SETM0(dv, (ODD) ? 16384 : 0); \
                if (i == 5) GLDS(uoffv, Vw, 0); \
                if (i == 7) GLDS(uoffv, Vw, 1024); \
                if (i == 8) { uoff += (j + 5 == n) ? 16384u * (unsigned)(1 - n) : 16384u; PIN(uoff); } \
                if (i == 11) { uoffv += 16384u; PIN(uoffv); } \
                if ((ODD) && i == 9) { dk ^= 0x8000u; dv ^= 0x8000u; asm volatile("" : "+s"(dk), "+s"(dv)); } } \
            if (SM) { Y[i] = EXP2(Y[i]); \
                if (i > 0) { if (i & 1) l += Y[i - 1]; else l2 += Y[i - 1]; PIN(l); PIN(l2); } \
                if (i >= 3 && (i & 1)) { PN[(i - 3) >> 3][((i - 3) >> 1) & 3] = cvtpk(Y[i - 3], Y[i - 2]); PIN(PN[(i - 3) >> 3]); } \
                if (i == 15) { PN[1][3] = cvtpk(Y[14], Y[15]); l2 += Y[15]; PIN(l2); PIN(PN[1]); } else PIN(Y); } \
            if (QK) { \
                if (MASK && i == 9 && (MASK == 1 || j + 1 >= n - 2)) { int qq = qrel - 64 * (j + 1 - (n - 2)); asm volatile("" : "+v"(qq)); \
                    _Pragma("unroll") for (int r = 0; r < 16; ++r) { const int c = (r & 3) + 8 * (r >> 2); if (c > qq) X[r] = -INFINITY; } } \
                if (i == 9) { mxa = MX3(X[0], X[1], X[2]); PIN(mxa); } \
                if (i == 10) { mxb = MX3(X[3], X[4], X[5]); mxa = MX3(mxa, X[6], X[7]); PIN(mxb); PIN(mxa); } \
                if (i == 12) { mxb = MX3(mxb, X[8], X[9]); mxa = MX3(mxa, X[10], X[11]); PIN(mxb); PIN(mxa); } \
                if (i == 14) { mxb = MX3(mxb, X[12], X[13]); mxa = MX3(mxa, X[14], X[15]); PIN(mxb); PIN(mxa); } \
                if (i == 15) { mxa = MX3(mxa, mxb, X[0]); if (!(FIRST)) { fire = __ballot(mxa > THR); asm volatile("" : "+s"(fire)); } } } \
            SBAR(); \
        } \
        if (__builtin_expect((fire | pend) != 0, 0) || FIRST) { \
            if (PV && pend) { _Pragma("unroll") for (int d = 0; d < 4; ++d) _Pragma("unroll") for (int r = 0; r < 16; ++r) o[d][r] *= alp; } \
            pend = 0; \
            if (QK && (FIRST || fire)) { \
                auto rr = __builtin_amdgcn_permlane32_swap(__float_as_uint(mxa), __float_as_uint(mxa), false, false); \
                const float rm = __builtin_fmaxf(__uint_as_float(rr[0]), __uint_as_float(rr[1])); \
                if (FIRST) { const float dl = __builtin_fmaxf(rm, -1e30f); m = dl; \
                    _Pragma("unroll") for (int r = 0; r < 16; ++r) { X[r] -= dl; negm[r] = -dl; } PIN(negm); } \
                else { const float dl = __builtin_fmaxf(rm, 0.f); m += dl; alp = EXP2(-dl); l *= alp; l2 *= alp; pend = 1; \
                    const float ndl = -dl, aone = hi ? 0.f : 1.f; \
                    asm volatile("s_nop 1\n\tv_mfma_f32_32x32x2_f32 %0, %2, %3, %0\n\tv_mfma_f32_32x32x2_f32 %1, %2, %3, %1\n\ts_nop 7\n\ts_nop 7\n\ts_nop 7" : "+v"(X), "+v"(negm) : "v"(aone), "v"(ndl)); } } } \
        SBAR(); \
        } while (0)
#define TBAR() wait_bar_v<4>()
    const f32x16 zero16 = {};
#pragma unroll 1
    for (int pass = 0; pass < 2; ++pass) {
        const int blk = pass ? blkB : blkA, n = 2 * (blk + 1);
        DMA_V(0, g0); DMA_V(1, g0 + 1);
        if (pass == 0) wait_bar<4>();
        {
            const lds_cptr qr = (lds_cptr)lds + (pass ? SPARE : QA_OFF) + rg * 8192 + lane * 16;
#pragma unroll
            for (int s = 0; s < 8; ++s) qf[s] = lds_frag(qr + s * 1024);
        }
        float m = -1e30f, l = 0.f, l2 = 0.f, alp = 1.f, mxa = 0.f, mxb = 0.f; unsigned long long pend = 0; f32x16 o[4] = {};
        u32x4 pk0[2] = {}, pk1[2] = {};
        f32x16 s0, s1, negm;
        bf16x8 kf[4], vf[4] = {};
        const int qrel = 32 * rg + r32 - 32 * par - 4 * hi;
        lds_cptr kp = krd + (g0 & 2) * 16384, vp = vrd + ((g0 & 2) ^ 2) * 16384;
        unsigned dk = kl0 + (g0 & 2) * 16384, dv = vl0 + ((g0 + 2) & 2) * 16384;
        unsigned uoffv = tvoff + 2 * 16384;
        unsigned uoff = tvoff + ((4 < n) ? 4 : 4 - n) * 16384;
#pragma unroll
        for (int i = 0; i < 4; ++i) kf[i] = lds_frag(kp + i * 1024);
        int j = -1;
        SLOT(s0, s1, pk1, pk0, true, true, false, false, true, false, true, 2);
        wait_bar_v<63>();
        j = 0;
        SLOT(s1, s0, pk0, pk1, false, true, true, false, false, true, true, 2);
        TBAR();
#pragma unroll 1
        for (j = 1; j < n - 3; j += 2) {
            SLOT(s0, s1, pk1, pk0, true, true, true, true, false, true, true, 0);
            TBAR();
            ++j;
            SLOT(s1, s0, pk0, pk1, false, true, true, true, false, true, true, 0);
            TBAR();
            --j;
        }
        if (n >= 4) {
            j = n - 3;
            SLOT(s0, s1, pk1, pk0, true, true, true, true, false, true, true, 1);
            TBAR();
            j = n - 2;
            SLOT(s1, s0, pk0, pk1, false, true, true, true, false, true, true, 1);
            TBAR();
        }
        j = n - 1;
        SLOT(s0, s1, pk1, pk0, true, false, true, true, false, true, false, 0);
        TBAR();
        j = n;
        SLOT(s1, s0, pk0, pk1, false, false, false, true, false, false, false, 0);
        g0 += n;
        STAMP(1, 2 + 2 * pass);
        {
            l += l2;
            auto rr = __builtin_amdgcn_permlane32_swap(__float_as_uint(l), __float_as_uint(l), false, false);
            l = __uint_as_float(rr[0]) + __uint_as_float(rr[1]);
            wait_bar<0>();
            LAS float* ml = (LAS float*)((lds_ptr)lds + VRING + 32768 + 28672);
            ml[(wid * 64 + lane) * 2] = m; ml[(wid * 64 + lane) * 2 + 1] = l;
            wait_bar<63>();
            const float mo = ml[((wid ^ 4) * 64 + lane) * 2], lo = ml[((wid ^ 4) * 64 + lane) * 2 + 1];
            const float mm = __builtin_fmaxf(m, mo), fo = EXP2(m - mm), fx = EXP2(mo - mm);
            const float inv = 1.f / (l * fo + lo * fx);
            const float sc = fo * inv;
            wait_bar<63>();
            lds_ptr sendp = (lds_ptr)lds + VRING + par * 32768 + rg * 8192 + lane * 16;
            lds_cptr recvp = (lds_cptr)lds + VRING + (par ^ 1) * 32768 + rg * 8192 + lane * 16;
#pragma unroll
            for (int d = 0; d < 2; ++d)
#pragma unroll
                for (int g = 0; g < 4; ++g) {
                    f32x4 v4;
#pragma unroll
                    for (int e = 0; e < 4; ++e) v4[e] = (par ? o[d][4 * g + e] : o[2 + d][4 * g + e]) * sc;
                    *(LAS f32x4*)(sendp + (d * 4 + g) * 1024) = v4;
                }
            wait_bar<63>();
            char* cdst = ctx + ((size_t)(blk * 4 + rg) * 32 + h) * 8192 + (par * 4) * 1024 + lane * 16;
#pragma unroll
            for (int d = 0; d < 2; ++d) {
                f32x16 fin;
#pragma unroll
                for (int g = 0; g < 4; ++g) {
                    const f32x4 v4 = *(const LAS f32x4*)(recvp + (d * 4 + g) * 1024);
#pragma unroll
                    for (int e = 0; e < 4; ++e) fin[4 * g + e] = (par ? o[2 + d][4 * g + e] : o[d][4 * g + e]) * sc + v4[e];
                }
                *(u32x4*)(cdst + (2 * d) * 1024) = pack8(fin, 0);
                *(u32x4*)(cdst + (2 * d + 1) * 1024) = pack8(fin, 1);
            }
            wait_bar<63>();
        }
        STAMP(1, 3 + 2 * pass);
    }
}

#define MFMA16(a, b, c) __builtin_amdgcn_mfma_f32_16x16x32_bf16(a, b, c, 0, 0, 0)
DI void pre_body(int b, const u16* x, const u16* bk, const u16* bv, const u16* shk, const u16* gw, u16* urT, float* gate, char* lds, int tid) {
    const int tb = b >> 3, cb = b & 7, t0 = tb * 64, c0 = cb * 80;
    LAS u16* tile = (LAS u16*)lds;
    for (int e = tid; e < 64 * 40; e += 512) {
        const int t = t0 + e / 40, c = c0 + (e % 40) * 2;
        float r[2];
        const unsigned vv = *(const unsigned*)(bv + (size_t)t * M + c);
        unsigned kk[4];
#pragma unroll
        for (int i = 0; i < 4; ++i) { const int si = t + i - 3; kk[i] = si >= 0 ? *(const unsigned*)(bk + (size_t)si * M + c) : 0u; }
#pragma unroll
        for (int q = 0; q < 2; ++q) {
            float ks = 0.f;
#pragma unroll
            for (int i = 0; i < 4; ++i) { const float kp = bf2f((u16)(q ? kk[i] >> 16 : kk[i] & 0xffff)); ks = rb(ks + rb(kp * bf2f(shk[(c + q) * 4 + 3 - i]))); }
            r[q] = ks * bf2f((u16)(q ? vv >> 16 : vv & 0xffff));
        }
        const int tl = e / 40, cl = (e % 40) * 2;
        tile[cl * 66 + (63 - tl)] = f2bf(r[0]); tile[(cl + 1) * 66 + (63 - tl)] = f2bf(r[1]);
    }
    {
        const int w = tid >> 6, lane = tid & 63, t = 8 * b + w;
        float sum = 0.f;
#pragma unroll
        for (int i = 0; i < 5; ++i) { const int c = (i * 64 + lane) * 2; const unsigned xv = *(const unsigned*)(x + (size_t)t * M + c), gv = *(const unsigned*)(gw + c);
            sum += rb(bf2f((u16)(xv & 0xffff)) * bf2f((u16)(gv & 0xffff))) + rb(bf2f((u16)(xv >> 16)) * bf2f((u16)(gv >> 16))); }
#pragma unroll
        for (int o = 32; o > 0; o >>= 1) sum += __shfl_xor(sum, o);
        if (lane == 0) { const float gs = rb(sum); gate[t] = rb(1.f / (1.f + __expf(-gs))); }
    }
    __syncthreads();
    for (int e = tid; e < 80 * 32; e += 512) { const int cl = e >> 5, d = e & 31;
        *(unsigned*)(urT + (size_t)(c0 + cl) * S + (2047 - t0 - 63) + 2 * d) = *(const LAS unsigned*)(tile + cl * 66 + 2 * d); }
}
__global__ __launch_bounds__(512) void k_pre(const u16* x, const u16* bk, const u16* bv, const u16* shk, const u16* gw, u16* urT, float* gate) {
    extern __shared__ __attribute__((aligned(16))) char lds[];
    pre_body(blockIdx.x, x, bk, bv, shk, gw, urT, gate, lds, threadIdx.x);
}

constexpr int CSTR = 4160;
constexpr int CCH = 8 * CSTR + 4608;
DI void conv_body(int b, const u16* urT, const u16* ssmk, const u16* ssmD, u16* zT, char* lds, int wid, int lane) {
    const int ch = wid >> 1, c = 4 * b + ch, half = wid & 1;
    lds_ptr base = (lds_ptr)lds + ch * CCH;
    const u16* ur = urT + (size_t)c * S;
#pragma unroll
    for (int it = 0; it < 2; ++it) {
        const int y8 = half * 128 + it * 64 + lane;
        const u32x4 w0 = *(const u32x4*)(ur + 8 * y8);
        u32x4 w1 = {0u, 0u, 0u, 0u}; if (y8 + 1 < 256) w1 = *(const u32x4*)(ur + 8 * y8 + 8);
        unsigned w[8] = {w0[0], w0[1], w0[2], w0[3], w1[0], w1[1], w1[2], w1[3]}, o[7];
#pragma unroll
        for (int e = 0; e < 7; ++e) o[e] = __builtin_amdgcn_alignbit(w[e + 1], w[e], 16);
#pragma unroll
        for (int sg = 0; sg < 8; ++sg) { const int e = sg >> 1; u32x4 v;
            if (sg & 1) v = (u32x4){o[e], o[e + 1], o[e + 2], o[e + 3]}; else v = (u32x4){w[e], w[e + 1], w[e + 2], w[e + 3]};
            *(LAS u32x4*)(base + sg * CSTR + y8 * 16) = v; }
    }
    if (half == 1 && lane < 16) { const int sg = lane >> 1, y8 = 256 + (lane & 1); *(LAS u32x4*)(base + sg * CSTR + y8 * 16) = (u32x4){0u, 0u, 0u, 0u}; }
    {
        lds_ptr hb = base + 8 * CSTR;
        const u16* hs = ssmk + (size_t)c * S;
#pragma unroll
        for (int it = 0; it < 2; ++it) { const int y8 = half * 128 + it * 64 + lane; *(LAS u32x4*)(hb + 512 + y8 * 16) = *(const u32x4*)(hs + 8 * y8); }
        if (half == 0 && lane < 32) *(LAS u32x4*)(hb + lane * 16) = (u32x4){0u, 0u, 0u, 0u};
    }
    asm volatile("s_waitcnt vmcnt(0) lgkmcnt(0)\n\ts_barrier" ::: "memory");
    const int j = lane & 15, g = lane >> 4;
    const lds_cptr ard = (lds_cptr)base + ((7 - j) & 7) * CSTR + (2024 - 8 * (j >> 3) + 8 * g) * 2;
    const lds_cptr brd = (lds_cptr)base + 8 * CSTR + (240 + 16 * j + 8 * g) * 2;
    f32x4 acc[8] = {};
#pragma unroll 1
    for (int rr = 0; rr < 4; ++rr) {
        const int rho = 2 * rr + half;
        bf16x8 A[8], B[8];
#pragma unroll
        for (int u = 0; u < 8; ++u) { A[u] = lds_frag(ard - (rho + 8 * u) * 64); B[u] = lds_frag(brd + (8 * u - rho) * 64); }
#pragma unroll
        for (int u = 0; u < 8; ++u)
#pragma unroll
            for (int v = 0; v + u < 8; ++v) acc[u + v] = MFMA16(A[u], B[v], acc[u + v]);
    }
    asm volatile("s_waitcnt lgkmcnt(0)\n\ts_barrier" ::: "memory");
    lds_ptr ex = base + CSTR;
    f32x4 keep[4];
    if (half) {
#pragma unroll
        for (int q = 0; q < 4; ++q) { *(LAS f32x4*)(ex + 4096 + q * 1024 + lane * 16) = acc[q]; keep[q] = acc[4 + q]; }
    } else {
#pragma unroll
        for (int q = 0; q < 4; ++q) { *(LAS f32x4*)(ex + q * 1024 + lane * 16) = acc[4 + q]; keep[q] = acc[q]; }
    }
    asm volatile("s_waitcnt lgkmcnt(0)\n\ts_barrier" ::: "memory");
    const float Dc = bf2f(ssmD[c]);
#pragma unroll
    for (int q = 0; q < 4; ++q) {
        const f32x4 o = *(const LAS f32x4*)(ex + (half ^ 1) * 4096 + q * 1024 + lane * 16);
        const int bb = half * 4 + q;
        const f32x4 y = keep[q] + o;
        const int t = 256 * bb + 16 * j + 4 * g;
        const s16x4 kvr = *(const LAS s16x4*)(base + (2047 - t - 3) * 2);
        u16 z[4];
#pragma unroll
        for (int r = 0; r < 4; ++r) { const float kv = bf2f((u16)kvr[3 - r]); z[r] = f2bf(rb(y[r]) + rb(kv * Dc)); }
        *(uint2*)(zT + (size_t)c * S + t) = make_uint2((unsigned)z[0] | ((unsigned)z[1] << 16), (unsigned)z[2] | ((unsigned)z[3] << 16));
    }
}
__global__ __launch_bounds__(512, 2) void k_conv(const u16* urT, const u16* ssmk, const u16* ssmD, u16* zT) {
    extern __shared__ __attribute__((aligned(16))) char lds[];
    conv_body(blockIdx.x, urT, ssmk, ssmD, zT, lds, rfl(threadIdx.x >> 6), threadIdx.x & 63);
}

constexpr int OCH = 36864;
DI void out_body(int b, const char* ctx, const u16* wo, const u16* x, const u16* zT, const float* gate, u16* out, char* lds, int wid, int lane, int tid) {
    const unsigned lds0 = (unsigned)(uintptr_t)lds;
    const int xcd = b & 7, idx = b >> 3, mt = xcd * 4 + (idx >> 3), nt = idx & 7;
    const int rh = wid & 1, kq = wid >> 1;
    const char* csrc = ctx + (size_t)(2 * mt + (wid >> 2)) * 32 * 8192 + (wid & 3) * 2048;
    const unsigned cl = lds0 + (wid >> 2) * 8192 + (wid & 3) * 2048;
    const unsigned tv = lane * 16;
    unsigned wv[3];
#pragma unroll
    for (int i = 0; i < 3; ++i) { const int cidx = (wid + 8 * i) * 64 + lane, row = cidx / 10, c16 = cidx % 10; wv[i] = (unsigned)(row * 1280 + c16 * 16); }
    const char* wsrc = (const char*)wo + nt * 160;
    const unsigned wl = lds0 + 16384 + wid * 1024;
    auto issue = [&](int h, int slot) {
        const char* cg = csrc + (size_t)h * 8192; const unsigned so = slot * OCH;
        dma_lin<0>(cl + so, tv, cg); dma_lin<1>(cl + so, tv, cg);
        const char* wg = wsrc + (size_t)h * 128 * 1280;
        dma1(wl + so, wv[0], wg); dma1(wl + so + 8192, wv[1], wg);
        if (wid < 4) dma1(wl + so + 16384, wv[2], wg);
    };
    const int j = lane & 15, g = lane >> 4, q = j >> 2, p = lane & 3;
    const lds_cptr ard = (lds_cptr)lds + rh * 8192 + kq * 2048 + (g >> 1) * 1024 + ((g & 1) * 32 + j) * 16;
    const lds_cptr brd = (lds_cptr)lds + 16384 + (32 * kq + 16 * (g >> 1) + 4 * (g & 1) + q) * 160 + p * 8;
    f32x4 acc[2][5] = {};
    issue(0, 0); issue(1, 1); issue(2, 2);
#pragma unroll 1
    for (int h = 0; h < 32; ++h) {
        const int slot = h & 3;
        if (h < 30) { if (wid < 4) wait_bar<10>(); else wait_bar<8>(); } else wait_bar<0>();
        if (h + 3 < 32) issue(h + 3, (h + 3) & 3);
        const lds_cptr ab = ard + slot * OCH, bb = brd + slot * OCH;
        const bf16x8 a0 = lds_frag(ab), a1 = lds_frag(ab + 256);
#pragma unroll
        for (int cb = 0; cb < 5; ++cb) {
            const s16x4 lo = vtr(bb + cb * 32), hi2 = vtr(bb + cb * 32 + 1280);
            const bf16x8 wf = __builtin_shufflevector(lo, hi2, 0, 1, 2, 3, 4, 5, 6, 7);
            acc[0][cb] = MFMA16(wf, a0, acc[0][cb]);
            acc[1][cb] = MFMA16(wf, a1, acc[1][cb]);
        }
    }
    wait_bar<0>();
    constexpr int PSTR = 84 * 4;
    {
        lds_ptr pb = (lds_ptr)lds + kq * 64 * PSTR;
#pragma unroll
        for (int rb2 = 0; rb2 < 2; ++rb2)
#pragma unroll
            for (int cb = 0; cb < 5; ++cb) *(LAS f32x4*)(pb + (32 * rh + 16 * rb2 + j) * PSTR + (16 * cb + 4 * g) * 4) = acc[rb2][cb];
        lds_ptr zb = (lds_ptr)lds + 4 * 64 * PSTR;
        for (int e = tid; e < 80 * 32; e += 512) { const int cl2 = e >> 5, d = e & 31;
            *(LAS unsigned*)(zb + cl2 * 132 + d * 4) = *(const unsigned*)(zT + (size_t)(nt * 80 + cl2) * S + mt * 64 + 2 * d); }
    }
    wait_bar<0>();
    {
        const int row = tid >> 3, c0 = (tid & 7) * 10, t = mt * 64 + row;
        const float gt = gate[t], g1 = rb(1.f - gt);
        const lds_cptr pb = (lds_cptr)lds + row * PSTR + c0 * 4;
        const lds_cptr zb = (lds_cptr)lds + 4 * 64 * PSTR + row * 2;
        const u16* xr = x + (size_t)t * M + nt * 80 + c0; u16* orow = out + (size_t)t * M + nt * 80 + c0;
#pragma unroll
        for (int e = 0; e < 10; e += 2) {
            unsigned res = 0; const unsigned xv = *(const unsigned*)(xr + e);
#pragma unroll
            for (int k = 0; k < 2; ++k) {
                const int cc = e + k;
                const float a = *(const LAS float*)(pb + cc * 4) + *(const LAS float*)(pb + 64 * PSTR + cc * 4) + *(const LAS float*)(pb + 128 * PSTR + cc * 4) + *(const LAS float*)(pb + 192 * PSTR + cc * 4);
                const float z = bf2f(*(const LAS u16*)(zb + (c0 + cc) * 132));
                const float bq = rb(bf2f((u16)(k ? xv >> 16 : xv & 0xffff)) * z);
                const float r = rb(gt * rb(a)) + rb(g1 * bq);
                res |= (unsigned)f2bf(r) << (16 * k);
            }
            *(unsigned*)(orow + e) = res;
        }
    }
}
__global__ __launch_bounds__(512, 2) void k_out(const char* ctx, const u16* wo, const u16* x, const u16* zT, const float* gate, u16* out) {
    extern __shared__ __attribute__((aligned(16))) char lds[];
    out_body(blockIdx.x, ctx, wo, x, zT, gate, out, lds, rfl(threadIdx.x >> 6), threadIdx.x & 63, threadIdx.x);
}

constexpr size_t WS_K = 0, WS_V = (size_t)S * HD * 2, WS_CTX = 2 * WS_V, WS_UR = 3 * WS_V, WS_Z = WS_UR + (size_t)S * M * 2, WS_G = WS_Z + (size_t)S * M * 2;

extern "C" void kernel_launch(void* const* d_in, const int* in_sizes, int n_in,
                              void* d_out, int out_size, void* d_ws, size_t ws_size,
                              hipStream_t stream) {
    const u16* x = (const u16*)d_in[0];
    const u16* ckv = (const u16*)d_in[1];
    const u16* wq = (const u16*)d_in[2];
    const u16* wuk = (const u16*)d_in[3];
    const u16* wuv = (const u16*)d_in[4];
    const u16* wo = (const u16*)d_in[5];
    const u16* bk = (const u16*)d_in[7];
    const u16* bv = (const u16*)d_in[8];
    const u16* shk = (const u16*)d_in[9];
    const u16* ssmk = (const u16*)d_in[10];
    const u16* ssmD = (const u16*)d_in[11];
    const u16* gw = (const u16*)d_in[12];
    char* ws = (char*)d_ws;
    static bool attr = false;
    if (!attr) {
        hipFuncSetAttribute((const void*)k_kvproj, hipFuncAttributeMaxDynamicSharedMemorySize, LDS_BYTES);
        hipFuncSetAttribute((const void*)k_attn, hipFuncAttributeMaxDynamicSharedMemorySize, LDS_BYTES);
        hipFuncSetAttribute((const void*)k_conv, hipFuncAttributeMaxDynamicSharedMemorySize, 4 * CCH);
        hipFuncSetAttribute((const void*)k_out, hipFuncAttributeMaxDynamicSharedMemorySize, LDS_BYTES);
        attr = true;
    }
    k_kvproj<<<256, 512, LDS_BYTES, stream>>>(ckv, wuk, wuv, ws + WS_K, ws + WS_V);
    k_attn<<<256, 512, LDS_BYTES, stream>>>(x, wq, ws + WS_K, ws + WS_V, ws + WS_CTX);
    k_pre<<<256, 512, 16384, stream>>>(x, bk, bv, shk, gw, (u16*)(ws + WS_UR), (float*)(ws + WS_G));
    k_conv<<<160, 512, 4 * CCH, stream>>>((const u16*)(ws + WS_UR), ssmk, ssmD, (u16*)(ws + WS_Z));
    k_out<<<256, 512, LDS_BYTES, stream>>>(ws + WS_CTX, wo, x, (const u16*)(ws + WS_Z), (const float*)(ws + WS_G), (u16*)d_out);
}
```

```cpp
#include <hip/hip_runtime.h>
#include <hip/hip_bf16.h>
#include <stdint.h>

typedef unsigned short u16;
typedef unsigned long long u64;
typedef short bf16x8 __attribute__((ext_vector_type(8)));
typedef short s16x4 __attribute__((ext_vector_type(4)));
typedef float f32x16 __attribute__((ext_vector_type(16)));
typedef float f32x4 __attribute__((ext_vector_type(4)));
typedef float f32x2 __attribute__((ext_vector_type(2)));
typedef __bf16 bf16x2_t __attribute__((ext_vector_type(2)));
typedef unsigned u32x4 __attribute__((ext_vector_type(4)));
typedef unsigned u32x2 __attribute__((ext_vector_type(2)));
typedef short v4i16_t __attribute__((ext_vector_type(4)));
#define LAS __attribute__((address_space(3)))
#define GAS __attribute__((address_space(1)))
typedef LAS const char* lds_cptr;
typedef LAS char* lds_ptr;
#define DI __device__ __forceinline__
#define SBAR() __builtin_amdgcn_sched_barrier(0)
#define PIN(x) asm volatile("" : "+v"(x))
#define PINS(x) asm volatile("" : "+s"(x))
#define MFMA32(a, b, c) __builtin_amdgcn_mfma_f32_32x32x16_bf16(a, b, c, 0, 0, 0)
#define MFMA16(a, b, c) __builtin_amdgcn_mfma_f32_16x16x32_bf16(a, b, c, 0, 0, 0)
#define LGKM(n) __builtin_amdgcn_s_waitcnt(0xC07F | ((n) << 8))
#define EXP2(v) __builtin_amdgcn_exp2f(v)

constexpr int S = 2048, H = 32, D = 128, M = 640, DC = 512, HD = H * D;
constexpr int LDS_BYTES = 163840;
constexpr size_t WS_K = 0, WS_V = (size_t)S * HD * 2, WS_CTX = 2 * WS_V, WS_UR = 3 * WS_V, WS_Z = WS_UR + (size_t)S * M * 2, WS_WO = WS_Z + (size_t)S * M * 2,
                 WS_G = WS_WO + (size_t)HD * M * 2, WS_F = WS_G + 8192, WS_END = WS_F + 32768;
constexpr int FL_F0 = 16, FL_F1 = FL_F0 + 256, FL_F3 = FL_F1 + 256, FL_F4 = FL_F3 + 512, FL_FW = FL_F4 + 160;

#define STAMP(i)

DI float bf2f(u16 b) { return __uint_as_float(((unsigned)b) << 16); }
DI unsigned cvtpk(float lo, float hi) { f32x2 v = {lo, hi}; bf16x2_t b = __builtin_convertvector(v, bf16x2_t); return __builtin_bit_cast(unsigned, b); }
DI u16 f2bf(float f) { return (u16)cvtpk(f, f); }
DI float rb(float f) { return __uint_as_float(cvtpk(f, f) << 16); }
DI unsigned rfl(unsigned x) { return __builtin_amdgcn_readfirstlane(x); }

#define SETM0(base, imm) asm volatile("s_add_u32 m0, %0, %1" :: "s"(base), "i"(imm) : "scc")
#define GLDS(voff, sbase, imm) asm volatile("global_load_lds_dwordx4 %0, %1 offset:%c2" :: "v"(voff), "s"(sbase), "i"(imm) : "memory")
#define GLDSC(voff, sbase, imm) asm volatile("global_load_lds_dwordx4 %0, %1 offset:%c2 sc1" :: "v"(voff), "s"(sbase), "i"(imm) : "memory")
DI void dma1(unsigned ldsaddr, unsigned voff, const void* sbase) {
    asm volatile("s_mov_b32 m0, %0\n\ts_nop 0\n\tglobal_load_lds_dwordx4 %1, %2" :: "s"(ldsaddr), "v"(voff), "s"(sbase) : "memory");
}
DI void dma2(unsigned ldsaddr, unsigned voff, const void* sbase) {
    asm volatile("s_mov_b32 m0, %0\n\ts_nop 0\n\tglobal_load_lds_dwordx4 %1, %2\n\tglobal_load_lds_dwordx4 %1, %2 offset:1024" :: "s"(ldsaddr), "v"(voff), "s"(sbase) : "memory");
}
DI void dma1c(unsigned ldsaddr, unsigned voff, const void* sbase) {
    asm volatile("s_mov_b32 m0, %0\n\ts_nop 0\n\tglobal_load_lds_dwordx4 %1, %2 sc1" :: "s"(ldsaddr), "v"(voff), "s"(sbase) : "memory");
}
DI void dma2c(unsigned ldsaddr, unsigned voff, const void* sbase) {
    asm volatile("s_mov_b32 m0, %0\n\ts_nop 0\n\tglobal_load_lds_dwordx4 %1, %2 sc1\n\tglobal_load_lds_dwordx4 %1, %2 offset:1024 sc1" :: "s"(ldsaddr), "v"(voff), "s"(sbase) : "memory");
}
template <int N> DI void wait_bar_v() { asm volatile("s_waitcnt vmcnt(%0)\n\ts_barrier" :: "i"(N) : "memory"); }
template <int N> DI void wait_bar() { asm volatile("s_waitcnt vmcnt(%0) lgkmcnt(0)\n\ts_barrier" :: "i"(N) : "memory"); }
DI bf16x8 lds_frag(lds_cptr p) { return *(const LAS bf16x8*)p; }
DI s16x4 vtr(lds_cptr p) { return __builtin_bit_cast(s16x4, __builtin_amdgcn_ds_read_tr16_b64_v4i16((LAS v4i16_t*)p)); }
DI bf16x8 tr_frag(lds_cptr p, int second) { const s16x4 lo = vtr(p), hi = vtr(p + second); return __builtin_shufflevector(lo, hi, 0, 1, 2, 3, 4, 5, 6, 7); }
DI u32x4 pack8(const f32x16& x, int s) {
    u32x4 w; w[0] = cvtpk(x[8 * s + 0], x[8 * s + 1]); w[1] = cvtpk(x[8 * s + 2], x[8 * s + 3]); w[2] = cvtpk(x[8 * s + 4], x[8 * s + 5]); w[3] = cvtpk(x[8 * s + 6], x[8 * s + 7]); return w;
}
typedef __amdgpu_buffer_rsrc_t rsrc_t;
DI void st16(rsrc_t r, unsigned off, u32x4 v) { __builtin_amdgcn_raw_buffer_store_b128(v, r, off, 0, 16); }
DI void st8(rsrc_t r, unsigned off, u32x2 v) { __builtin_amdgcn_raw_buffer_store_b64(v, r, off, 0, 16); }
DI void st4(rsrc_t r, unsigned off, unsigned v) { __builtin_amdgcn_raw_buffer_store_b32(v, r, off, 0, 16); }
DI void signal_flag(GAS u64* flag, u64 tag, int tid) {
    asm volatile("s_waitcnt vmcnt(0) lgkmcnt(0)\n\ts_barrier" ::: "memory");
    if (tid == 0) __hip_atomic_store(flag, tag, __ATOMIC_RELAXED, __HIP_MEMORY_SCOPE_AGENT);
}
DI void poll_flags(GAS u64* flags, int idx, u64 tag) {
    for (unsigned spins = 0; spins < (1u << 16); ++spins) {
        const u64 v = idx >= 0 ? __hip_atomic_load(flags + idx, __ATOMIC_RELAXED, __HIP_MEMORY_SCOPE_AGENT) : tag;
        if (__all(v == tag)) break;
        __builtin_amdgcn_s_sleep(2);
    }
}
DI unsigned ld4c(rsrc_t r, unsigned off) { return __builtin_amdgcn_raw_buffer_load_b32(r, off, 0, 16); }
DI void flag_store(GAS u64* flag, u64 tag) { __hip_atomic_store(flag, tag, __ATOMIC_RELAXED, __HIP_MEMORY_SCOPE_AGENT); }

struct Args { const u16 *x, *ckv, *wq, *wuk, *wuv, *wo, *bk, *bv, *shk, *ssmk, *ssmD, *gw; u16* out; char* ws; };

constexpr int P0_OFF = 133120;
constexpr int P0_K = P0_OFF, P0_V = P0_K + 11264, P0_T = P0_V + 10240, P0_KV = 2 * 33280;
DI float blo(unsigned w) { return __uint_as_float(w << 16); }
DI float bhi(unsigned w) { return __uint_as_float(w & 0xffff0000u); }
DI void p0a_load(const Args& a, int cb, int tb, unsigned lds0, int wid, int lane) {
#pragma unroll
    for (int i = 0; i < 3; ++i) { const int p = wid + 8 * i;
        if (p < 11) { const int ci = p * 64 + lane, row = (ci < 670 ? ci : 669) / 10, c16 = ci % 10; int t = tb * 64 - 3 + row; t = t < 0 ? 0 : t;
            dma1(lds0 + P0_K + p * 1024, (unsigned)(t * 1280 + c16 * 16), (const char*)a.bk + cb * 160); }
        else if (p < 21) { const int ci = (p - 11) * 64 + lane, row = ci / 10, c16 = ci % 10;
            dma1(lds0 + P0_V + (p - 11) * 1024, (unsigned)((tb * 64 + row) * 1280 + c16 * 16), (const char*)a.bv + cb * 160); }
        else if (p == 21) dma1(lds0 + P0_T, (unsigned)((lane < 40 ? lane : 39) * 16), (const char*)a.shk + cb * 640); }
}
DI void p0a_finish(rsrc_t wr, int cb, int tb, char* lds, int tid) {
    const int t0 = tb * 64, c0 = cb * 80, cc = tid % 10, tp = tid / 10;
    LAS u16* tile = (LAS u16*)((lds_ptr)lds + P0_KV);
    if (tid < 320) {
        u32x4 kk[5], vv[2], sh[4];
#pragma unroll
        for (int i = 0; i < 5; ++i) kk[i] = *(const LAS u32x4*)((lds_cptr)lds + P0_K + (2 * tp + i) * 160 + cc * 16);
#pragma unroll
        for (int i = 0; i < 2; ++i) vv[i] = *(const LAS u32x4*)((lds_cptr)lds + P0_V + (2 * tp + i) * 160 + cc * 16);
#pragma unroll
        for (int i = 0; i < 4; ++i) sh[i] = *(const LAS u32x4*)((lds_cptr)lds + P0_T + cc * 64 + i * 16);
#pragma unroll
        for (int q = 0; q < 8; ++q) {
            float tap[4], kx[5], r2[2];
#pragma unroll
            for (int i = 0; i < 4; ++i) { const unsigned w = sh[q >> 1][2 * (q & 1) + (i >> 1)]; tap[i] = (i & 1) ? bhi(w) : blo(w); }
#pragma unroll
            for (int i = 0; i < 5; ++i) { const unsigned w = (t0 + 2 * tp + i - 3 < 0) ? 0u : kk[i][q >> 1]; kx[i] = (q & 1) ? bhi(w) : blo(w); }
#pragma unroll
            for (int r = 0; r < 2; ++r) { const unsigned w = vv[r][q >> 1];
                r2[r] = (kx[r] * tap[3] + kx[r + 1] * tap[2] + kx[r + 2] * tap[1] + kx[r + 3] * tap[0]) * ((q & 1) ? bhi(w) : blo(w)); }
            *(LAS unsigned*)(tile + (8 * cc + q) * 72 + (62 - 2 * tp)) = cvtpk(r2[1], r2[0]);
        }
    }
    wait_bar<63>();
#pragma unroll
    for (int it = 0; it < 2; ++it) { const int e = tid + it * 512, cl = e >> 3, d = e & 7;
        if (e < 640) st16(wr, (unsigned)(WS_UR + ((size_t)(c0 + cl) * S + (2047 - t0 - 63) + 8 * d) * 2), *(const LAS u32x4*)(tile + cl * 72 + 8 * d)); }
    wait_bar<63>();
}
DI void p0b_all(const Args& a, rsrc_t wr, int jb0, char* lds, int wid, int lane) {
    const unsigned lds0 = (unsigned)(uintptr_t)lds;
    const int nj = jb0 + 192 < 256 ? 3 : 2;
    unsigned wv[3];
#pragma unroll
    for (int i = 0; i < 3; ++i) { const int cidx = (wid + 8 * i) * 64 + lane, row = cidx / 10, c16 = cidx % 10; wv[i] = (unsigned)(row * 1280 + c16 * 16); }
    u32x4 xg[3][2], gg[2];
#pragma unroll
    for (int k = 0; k < 3; ++k) { const int jb = k < nj ? jb0 + 96 * k : jb0, cb = jb & 7, tb = jb >> 3;
        const char* wg = (const char*)a.wo + (size_t)tb * 128 * 1280 + cb * 160;
#pragma unroll
        for (int i = 0; i < 3; ++i) if (wid + 8 * i < 20) dma1(lds0 + k * 20480 + (wid + 8 * i) * 1024, wv[i], wg);
        const int tg = 64 * tb + 8 * cb + wid;
        xg[k][0] = *(const u32x4*)(a.x + (size_t)tg * M + lane * 8); xg[k][1] = *(const u32x4*)(a.x + (size_t)tg * M + 512 + (lane & 15) * 8); }
    gg[0] = *(const u32x4*)(a.gw + lane * 8); gg[1] = *(const u32x4*)(a.gw + 512 + (lane & 15) * 8); if (lane >= 16) gg[1] = (u32x4){0u, 0u, 0u, 0u};
    float sum[3];
#pragma unroll
    for (int k = 0; k < 3; ++k) { sum[k] = 0.f;
#pragma unroll
        for (int i = 0; i < 2; ++i)
#pragma unroll
            for (int e = 0; e < 4; ++e) sum[k] += blo(xg[k][i][e]) * blo(gg[i][e]) + bhi(xg[k][i][e]) * bhi(gg[i][e]); }
#pragma unroll
    for (int o = 32; o > 0; o >>= 1)
#pragma unroll
        for (int k = 0; k < 3; ++k) sum[k] += __shfl_xor(sum[k], o);
    if (lane < nj) { const int jb = jb0 + 96 * lane; const float sv = lane == 0 ? sum[0] : (lane == 1 ? sum[1] : sum[2]);
        st4(wr, (unsigned)(WS_G + (64 * (jb >> 3) + 8 * (jb & 7) + wid) * 4), __float_as_uint(1.f / (1.f + __expf(-sv)))); }
    wait_bar<0>();
    const int j = lane & 15, g = lane >> 4, q = j >> 2, p = lane & 3;
    const lds_cptr brd = (lds_cptr)lds + (16 * (g >> 1) + 4 * (g & 1) + q) * 160 + p * 8;
#pragma unroll
    for (int k = 0; k < 3; ++k) if (k < nj) { const int jb = jb0 + 96 * k, cb = jb & 7, tb = jb >> 3;
#pragma unroll
        for (int i = 0; i < 3; ++i) if (wid + 8 * i < 20) { const int f = wid + 8 * i, kq = f / 5, cbk = f % 5;
            const bf16x8 w = tr_frag(brd + k * 20480 + kq * 32 * 160 + cbk * 32, 1280);
            st16(wr, (unsigned)(WS_WO + ((size_t)(cb * 32 + tb) * 20 + f) * 1024 + lane * 16), __builtin_bit_cast(u32x4, w)); } }
}

constexpr int TSLOT = 32 * 1040;
struct P1Ctx { const char* wsrc; const char* tsrc; unsigned wvoff, wlds, tvoff, tlds; };
DI P1Ctx p1_setup(const Args& a, int h, int r, unsigned lds0, int wid, int lane) {
    P1Ctx c;
    c.wsrc = (const char*)((wid >> 2) ? a.wuv : a.wuk) + (size_t)h * 256 + ((wid >> 1) & 1) * 128 + (size_t)(wid & 1) * 4 * 8 * 8192;
    c.wvoff = (unsigned)(lane >> 3) * 8192u + (unsigned)(((lane & 7) ^ (((lane >> 4) & 1) << 2)) * 16);
    c.wlds = lds0 + ((wid >> 2) * 2 + ((wid >> 1) & 1)) * 8192 + (wid & 1) * 4 * 1024;
    c.tsrc = (const char*)a.ckv + (size_t)(256 * r + 4 * wid) * 1024;
    c.tvoff = lane * 16; c.tlds = lds0 + 4 * wid * 1040;
    return c;
}
DI void p1_dma_w1(const P1Ctx& c, int ch, int slot, int i) { dma1(c.wlds + slot * TSLOT + i * 1024, c.wvoff, c.wsrc + (size_t)ch * 64 * 8192 + (size_t)i * 8 * 8192); }
template <int J> DI void p1_dma_t1(const P1Ctx& c, int t, int slot) {
    asm volatile("s_mov_b32 m0, %0\n\ts_nop 0\n\tglobal_load_lds_dwordx4 %1, %2 offset:%c3" :: "s"(c.tlds + slot * TSLOT + 16 * J), "v"(c.tvoff), "s"(c.tsrc + (size_t)t * 32 * 1024), "i"(J * 1024) : "memory");
}
DI void p1_prime01(const P1Ctx& c) {
    p1_dma_t1<0>(c, 0, 3); p1_dma_t1<1>(c, 0, 3); p1_dma_t1<2>(c, 0, 3); p1_dma_t1<3>(c, 0, 3);
#pragma unroll
    for (int ch = 0; ch < 2; ++ch)
#pragma unroll
        for (int i = 0; i < 4; ++i) p1_dma_w1(c, ch, ch, i);
}
DI void p1_prime2(const P1Ctx& c) {
#pragma unroll
    for (int i = 0; i < 4; ++i) p1_dma_w1(c, 2, 2, i);
}
constexpr int STG_UR = 3 * TSLOT, STG_H = STG_UR + 16384;
template <int MAT>
DI void p1_body(const Args& a, const P1Ctx& c, rsrc_t wr, int b, int h, int r, char* lds, int wid, int lane, int tid, GAS u64* flags, u64 tag) {
    const int g4 = lane >> 4, q = (lane & 15) >> 2, p = lane & 3;
    const lds_cptr wrd = (lds_cptr)lds + (MAT * 2 + ((wid >> 1) & 1)) * 8192 + (8 * (g4 >> 1) + q) * 128 + (((wid & 1) ^ (q >> 1)) * 64) + (g4 & 1) * 32 + p * 8;
    const lds_cptr trd = (lds_cptr)lds + (lane & 31) * 1040 + (lane >> 5) * 16;
    const unsigned dst = (unsigned)((MAT ? WS_V : WS_K) + ((size_t)(h * 64 + 8 * r) * 8 + 2 * (wid & 3)) * 1024) + lane * 16;
    bf16x8 Wf[32];
    f32x16 acc = f32x16{};
    const lds_cptr t0p = trd + 3 * TSLOT;
#define P1M(A_, W_) (MAT == 0 ? MFMA32(W_, A_, acc) : MFMA32(A_, W_, acc))
#pragma unroll
    for (int ch = 0; ch < 8; ++ch) {
        if (ch == 0) wait_bar<8>(); else wait_bar<4>();
        if (ch == 3 && tid == 0) flag_store(flags + FL_F0 + (b & 7) * 32 + (b >> 3), tag);
        const lds_cptr wb = wrd + (ch % 3) * TSLOT;
        bf16x8 af[4];
#pragma unroll
        for (int s = 0; s < 4; ++s) { Wf[4 * ch + s] = tr_frag(wb + s * 2048, 512); af[s] = lds_frag(t0p + (4 * ch + s) * 32); }
        SBAR();
#pragma unroll
        for (int s = 0; s < 4; ++s) {
            if (ch >= 1) { if (ch + 2 <= 7) p1_dma_w1(c, ch + 2, (ch + 2) % 3, s);
                           else { if (s == 0) p1_dma_t1<0>(c, ch - 5, (ch + 2) % 3); if (s == 1) p1_dma_t1<1>(c, ch - 5, (ch + 2) % 3); if (s == 2) p1_dma_t1<2>(c, ch - 5, (ch + 2) % 3); if (s == 3) p1_dma_t1<3>(c, ch - 5, (ch + 2) % 3); } }
            acc = P1M(af[s], Wf[4 * ch + s]); PIN(acc);
            SBAR();
        }
    }
    st16(wr, dst, pack8(acc, 0)); st16(wr, dst + 1024, pack8(acc, 1));
#pragma unroll
    for (int i = 1; i < 8; ++i) {
        const int j = 7 + i;
        if (i == 1) wait_bar<6>(); else if (i < 7) wait_bar<8>(); else wait_bar<4>();
        if (b < 160) {
            const unsigned lds0 = (unsigned)(uintptr_t)lds;
            if (i == 1) dma2(lds0 + STG_H + wid * 2048, lane * 16, (const char*)a.ssmk + (size_t)(4 * b) * 4096 + wid * 2048);
            if (i == 4 && wid == 0) poll_flags(flags, lane < 32 ? FL_F0 + (b / 20) * 32 + lane : -1, tag);
            if (i == 5) dma2c(lds0 + STG_UR + wid * 2048, lane * 16, a.ws + WS_UR + (size_t)(4 * b) * 4096 + wid * 2048);
        }
        const lds_cptr tb = trd + (j % 3) * TSLOT;
        bf16x8 af[4];
#pragma unroll
        for (int s = 0; s < 4; ++s) af[s] = lds_frag(tb + s * 32);
        SBAR();
        acc = f32x16{};
#pragma unroll
        for (int s = 0; s < 32; ++s) {
            if (!(s & 1) && s + 4 < 32) { LGKM(2); SBAR(); }
            acc = P1M(af[s & 3], Wf[s]); PIN(acc);
            if (s + 4 < 32) af[s & 3] = lds_frag(tb + (s + 4) * 32);
            if (i + 2 <= 7) {
                if (s == 1) p1_dma_t1<0>(c, i + 2, (j + 2) % 3);
                if (s == 7) p1_dma_t1<1>(c, i + 2, (j + 2) % 3);
                if (s == 13) p1_dma_t1<2>(c, i + 2, (j + 2) % 3);
                if (s == 19) p1_dma_t1<3>(c, i + 2, (j + 2) % 3);
            }
            SBAR();
        }
        st16(wr, dst + i * 8192, pack8(acc, 0)); st16(wr, dst + i * 8192 + 1024, pack8(acc, 1));
    }
#undef P1M
}

constexpr float QSCALE = 0.08838834764831845f * 1.4426950408889634f;
constexpr float THR = 8.f;
constexpr int QCH = 49152;
constexpr int KRING = 0, VRING = 65536, SPARE = 131072, QA_OFF = VRING + 32768;
#define MX3(a, b, c) __builtin_fmaxf(__builtin_fmaxf((a), (b)), (c))

DI void p2_body(const Args& a, int b, int h, int r, int blkA, int blkB, char* lds, int wid, int lane, int tid, GAS u64* flags, u64 tag) {
    const unsigned lds0 = (unsigned)(uintptr_t)lds;
    const int r32 = lane & 31, hi = lane >> 5, rg = wid & 3, par = wid >> 2;
    const int myblk = par ? blkB : blkA;
    const char* xg = (const char*)a.x + (size_t)(128 * myblk + 32 * rg) * 1280;
    const unsigned xvoff = (unsigned)(lane >> 3) * 1280u + (unsigned)(((lane & 7) ^ ((lane >> 4) & 7)) * 16);
    const unsigned xvoff2 = (unsigned)(lane >> 3) * 1280u + 8 * 1280u + (unsigned)(((lane & 7) ^ ((4 + (lane >> 4)) & 7)) * 16);
    const unsigned xlds = lds0 + wid * 4096;
    const char* wg = (const char*)a.wq + (size_t)h * 256 + (wid >> 2) * 128 + (size_t)(wid & 3) * 2 * 8 * 8192;
    const unsigned wvoff = (unsigned)(lane >> 3) * 8192u + (unsigned)(((lane & 7) ^ (((lane >> 4) & 1) << 2)) * 16);
    const unsigned wvoff2 = wvoff + 8 * 8192u;
    const unsigned wlds = lds0 + 32768 + (wid >> 2) * 8192 + (wid & 3) * 2048;
    const int g4 = lane >> 4, q = (lane & 15) >> 2, p = lane & 3;
    const int hsw = (q >> 1) & 1, ysw = hi ^ ((r32 >> 1) & 7);
    const lds_cptr wrd0 = (lds_cptr)lds + 32768 + (8 * (g4 >> 1) + q) * 128 + (g4 & 1) * 32 + p * 8 + hsw * 64;
    const lds_cptr wrd1 = (lds_cptr)lds + 32768 + (8 * (g4 >> 1) + q) * 128 + (g4 & 1) * 32 + p * 8 + (hsw ^ 1) * 64;
    lds_cptr xrd[4];
#pragma unroll
    for (int s = 0; s < 4; ++s) xrd[s] = (lds_cptr)lds + wid * 4096 + r32 * 128 + (((2 * s) ^ ysw) << 4);
    f32x16 acc[4] = {};
#pragma unroll
    for (int c = 0; c < 2; ++c) { const char* xp = xg + c * 128; const char* wp = wg + (size_t)c * 64 * 8192;
        dma1(xlds + c * QCH, xvoff, xp); dma1(xlds + c * QCH + 1024, xvoff2, xp); dma1(xlds + c * QCH + 2048, xvoff, xp + 16 * 1280); dma1(xlds + c * QCH + 3072, xvoff2, xp + 16 * 1280);
        dma1(wlds + c * QCH, wvoff, wp); dma1(wlds + c * QCH + 1024, wvoff2, wp); }
    unsigned so = 0;
    const char* xp = xg + 2 * 128; const char* wp = wg + (size_t)2 * 64 * 8192;
#pragma unroll 1
    for (int c = 0; c < 10; ++c) {
        wait_bar<6>();
        if (c == 1) {
            if (b < 160) { if (tid == 0) flag_store(flags + FL_F4 + b, tag); }
            else if (tid < 3) { const int jb = b - 160 + 96 * tid; if (jb < 256) flag_store(flags + FL_FW + (jb & 7) * 32 + (jb >> 3), tag); } }
        if (c == 8 && wid == 0) poll_flags(flags, lane < 8 ? FL_F1 + h * 8 + lane : -1, tag);
        const unsigned sn = so == 0 ? 2 * QCH : so - QCH;
        const unsigned xl_ = xlds + sn, wl_ = wlds + sn; const char* xp2 = xp + 16 * 1280;
        const lds_cptr wb0 = wrd0 + so, wb1 = wrd1 + so;
#define WFR(i) tr_frag((((i) & 1) ? wb1 : wb0) + (((i) >> 1) & 1) * 8192 + ((i) >> 2) * 2048, 512)
        bf16x8 wf[4], xf[2];
        xf[0] = lds_frag(xrd[0] + so);
#pragma unroll
        for (int i = 0; i < 4; ++i) wf[i] = WFR(i);
        SBAR();
#pragma unroll
        for (int i = 0; i < 16; ++i) {
            acc[i & 3] = MFMA32(wf[i & 3], xf[(i >> 2) & 1], acc[i & 3]); PIN(acc[i & 3]);
            if (i + 4 < 16) wf[i & 3] = WFR(i + 4);
            if ((i & 3) == 0 && i + 4 < 16) xf[((i >> 2) + 1) & 1] = lds_frag(xrd[(i >> 2) + 1] + so);
            if (i == 0) { SETM0(xl_, 0); } if (i == 1) { GLDS(xvoff, xp, 0); SETM0(xl_, 1024); }
            if (i == 3) { GLDS(xvoff2, xp, 0); SETM0(xl_, 2048); } if (i == 5) { GLDS(xvoff, xp2, 0); SETM0(xl_, 3072); }
            if (i == 8) { GLDS(xvoff2, xp2, 0); SETM0(wl_, 0); } if (i == 10) { GLDS(wvoff, wp, 0); SETM0(wl_, 1024); }
            if (i == 13) { GLDS(wvoff2, wp, 0); }
            SBAR();
        }
        so = so == 2 * QCH ? 0 : so + QCH;
        const int adv = c < 7 ? 1 : 0; xp += adv * 128; wp += (size_t)adv * 64 * 8192;
    }
    wait_bar<0>();
    lds_ptr qw = (lds_ptr)lds + (par ? SPARE : QA_OFF) + rg * 8192 + lane * 16;
#pragma unroll
    for (int db = 0; db < 4; ++db) {
#pragma unroll
        for (int e = 0; e < 16; ++e) acc[db][e] *= QSCALE;
        *(LAS u32x4*)(qw + (2 * db) * 1024) = pack8(acc[db], 0);
        *(LAS u32x4*)(qw + (2 * db + 1) * 1024) = pack8(acc[db], 1);
    }
#undef WFR
}

DI void p3_body(rsrc_t wr, const char* ws, int h, int blkA, int blkB, char* lds, int wid, int lane, int tid, GAS u64* flags, u64 tag) {
    const unsigned lds0 = (unsigned)(uintptr_t)lds;
    const int r32 = lane & 31, hi = lane >> 5, rg = wid & 3, par = wid >> 2;
    const unsigned tvoff = lane * 16;
    const unsigned kl0 = lds0 + KRING + wid * 2048, vl0 = lds0 + VRING + wid * 2048;
    const char* Kw = ws + WS_K + (size_t)h * 64 * 8192 + wid * 2048; const char* Vw = ws + WS_V + (size_t)h * 64 * 8192 + wid * 2048;
    const lds_cptr krd = (lds_cptr)lds + KRING + par * 8192 + lane * 16;
    const lds_cptr vrd = (lds_cptr)lds + VRING + par * 8192 + lane * 16;
#pragma unroll
    for (int u = 0; u < 4; ++u) dma2c(kl0 + u * 16384, tvoff, Kw + (size_t)u * 16384);
    int g0 = 0;
    bf16x8 qf[8];
#define VF(i) ((((i) & 3) * 2 + ((i) >> 2)) * 1024)
#define MFMA_C(D, A, B, C) asm volatile("v_mfma_f32_32x32x16_bf16 %0, %2, %3, %1" : "=&v"(D), "+v"(C) : "v"(A), "v"(B))
#define SLOT(X, Y, PN, PP, ODD, QK, SM, PV, FIRST, DM, PFK, MASK) do { \
        unsigned long long fire = 0; \
        _Pragma("unroll") for (int i = 0; i < 16; ++i) { \
            if (i < 8 && QK && !(i & 1)) { LGKM(2); SBAR(); } \
            if (i >= 8 && PV && !(i & 1)) { LGKM(2); SBAR(); } \
            if (i < 8) { if (QK) { if (i == 0) { if (FIRST) X = MFMA32(kf[0], qf[0], zero16); else MFMA_C(X, kf[0], qf[0], negm); } else X = MFMA32(kf[i & 3], qf[i], X); PIN(X); \
                                   if (i < 4) kf[i] = lds_frag(kp + ((ODD) ? 0 : 16384) + (i + 4) * 1024); } \
                         if (PV && i >= 4) vf[i - 4] = lds_frag(vp + ((ODD) ? 0 : 16384) + VF(i - 4)); } \
            else { if (PV) { o[i & 3] = MFMA32(vf[i & 3], __builtin_bit_cast(bf16x8, PP[(i - 8) >> 2]), o[i & 3]); PIN(o[i & 3]); if (i < 12) vf[i & 3] = lds_frag(vp + ((ODD) ? 0 : 16384) + VF(i - 4)); } \
                   if (PFK && i >= 12) kf[i - 12] = lds_frag(kp + ((ODD) ? 16384 : 0) + (i - 12) * 1024); } \
            if (!(ODD) && i == 5) { kp = (lds_cptr)(uintptr_t)((unsigned)(uintptr_t)kp ^ 0x8000u); PIN(kp); } \
            if (!(ODD) && i == 13) { vp = (lds_cptr)(uintptr_t)((unsigned)(uintptr_t)vp ^ 0x8000u); PIN(vp); } \
            if (DM) { \
                if (i == 0) SETM0(dk, (ODD) ? 16384 : 0); \
                if (i == 1) GLDSC(uoff, Kw, 0); \
                if (i == 3) GLDSC(uoff, Kw, 1024); \
                if (i == 4) SETM0(dv, (ODD) ? 16384 : 0); \
                if (i == 5) GLDSC(uoffv, Vw, 0); \
                if (i == 7) GLDSC(uoffv, Vw, 1024); \
                if (i == 8) { uoff += (j + 5 == n) ? 16384u * (unsigned)(1 - n) : 16384u; PIN(uoff); } \
                if (i == 11) { uoffv += (j + 3 < n) ? 16384u : 0u; PIN(uoffv); } \
                if ((ODD) && i == 9) { dk ^= 0x8000u; dv ^= 0x8000u; PINS(dk); PINS(dv); } } \
            if (SM) { Y[i] = EXP2(Y[i]); \
                if (i > 0) { if (i & 1) l += Y[i - 1]; else l2 += Y[i - 1]; PIN(l); PIN(l2); } \
                if (i >= 3 && (i & 1)) { PN[(i - 3) >> 3][((i - 3) >> 1) & 3] = cvtpk(Y[i - 3], Y[i - 2]); PIN(PN[(i - 3) >> 3]); } \
                if (i == 15) { PN[1][3] = cvtpk(Y[14], Y[15]); l2 += Y[15]; PIN(l2); PIN(PN[1]); } else PIN(Y); } \
            if (QK) { \
                if (MASK && i == 9 && (MASK == 1 || j + 1 >= n - 2)) { int qq = qrel - 64 * (j + 1 - (n - 2)); PIN(qq); \
                    _Pragma("unroll") for (int r = 0; r < 16; ++r) { const int c = (r & 3) + 8 * (r >> 2); if (c > qq) X[r] = -INFINITY; } } \
                if (i == 9) { mxa = MX3(X[0], X[1], X[2]); PIN(mxa); } \
                if (i == 10) { mxb = MX3(X[3], X[4], X[5]); mxa = MX3(mxa, X[6], X[7]); PIN(mxb); PIN(mxa); } \
                if (i == 12) { mxb = MX3(mxb, X[8], X[9]); mxa = MX3(mxa, X[10], X[11]); PIN(mxb); PIN(mxa); } \
                if (i == 14) { mxb = MX3(mxb, X[12], X[13]); mxa = MX3(mxa, X[14], X[15]); PIN(mxb); PIN(mxa); } \
                if (i == 15) { mxa = MX3(mxa, mxb, X[0]); if (!(FIRST)) { fire = __ballot(mxa > THR); PINS(fire); } } } \
            SBAR(); \
        } \
        if (__builtin_expect((fire | pend) != 0, 0) || FIRST) { \
            if (PV && pend) { _Pragma("unroll") for (int d = 0; d < 4; ++d) _Pragma("unroll") for (int r = 0; r < 16; ++r) o[d][r] *= alp; } \
            pend = 0; \
            if (QK && (FIRST || fire)) { \
                auto rr = __builtin_amdgcn_permlane32_swap(__float_as_uint(mxa), __float_as_uint(mxa), false, false); \
                const float rm = __builtin_fmaxf(__uint_as_float(rr[0]), __uint_as_float(rr[1])); \
                if (FIRST) { const float dl = __builtin_fmaxf(rm, -1e30f); m = dl; \
                    _Pragma("unroll") for (int r = 0; r < 16; ++r) { X[r] -= dl; negm[r] = -dl; } PIN(negm); } \
                else { const float dl = __builtin_fmaxf(rm, 0.f); m += dl; alp = EXP2(-dl); l *= alp; l2 *= alp; pend = 1; \
                    const float ndl = -dl, aone = hi ? 0.f : 1.f; \
                    asm volatile("s_nop 1\n\tv_mfma_f32_32x32x2_f32 %0, %2, %3, %0\n\tv_mfma_f32_32x32x2_f32 %1, %2, %3, %1\n\ts_nop 7\n\ts_nop 7\n\ts_nop 7" : "+v"(X), "+v"(negm) : "v"(aone), "v"(ndl)); } } } \
        SBAR(); \
        } while (0)
#define TBAR() wait_bar_v<4>()
    const f32x16 zero16 = {};
#pragma unroll 1
    for (int pass = 0; pass < 2; ++pass) {
        const int blk = pass ? blkB : blkA, n = 2 * (blk + 1);
        dma2c(vl0 + (g0 & 3) * 16384, tvoff, Vw); dma2c(vl0 + ((g0 + 1) & 3) * 16384, tvoff, Vw + 16384);
        if (pass == 0) wait_bar<4>();
        if (pass == 0) {
            const lds_cptr qr = (lds_cptr)lds + QA_OFF + rg * 8192 + lane * 16;
#pragma unroll
            for (int s = 0; s < 8; ++s) qf[s] = lds_frag(qr + s * 1024);
        }
        float m = -1e30f, l = 0.f, l2 = 0.f, alp = 1.f, mxa = 0.f, mxb = 0.f; unsigned long long pend = 0; f32x16 o[4] = {};
        u32x4 pk0[2] = {}, pk1[2] = {};
        f32x16 s0, s1, negm;
        bf16x8 kf[4], vf[4] = {};
        const int qrel = 32 * rg + r32 - 32 * par - 4 * hi;
        lds_cptr kp = krd + (g0 & 2) * 16384, vp = vrd + ((g0 & 2) ^ 2) * 16384;
        unsigned dk = kl0 + (g0 & 2) * 16384, dv = vl0 + ((g0 + 2) & 2) * 16384;
        unsigned uoffv = tvoff + ((2 < n) ? 2 : 1) * 16384;
        unsigned uoff = tvoff + ((4 < n) ? 4 : 4 - n) * 16384;
#pragma unroll
        for (int i = 0; i < 4; ++i) kf[i] = lds_frag(kp + i * 1024);
        int j = -1;
        SLOT(s0, s1, pk1, pk0, true, true, false, false, true, false, true, 2);
        wait_bar_v<63>();
        j = 0;
        SLOT(s1, s0, pk0, pk1, false, true, true, false, false, true, true, 2);
        TBAR();
        if (pass == 1 && tid == 0) flag_store(flags + FL_F3 + blkA * 32 + h, tag);
#pragma unroll 1
        for (j = 1; j < n - 3; j += 2) {
            SLOT(s0, s1, pk1, pk0, true, true, true, true, false, true, true, 0);
            TBAR();
            ++j;
            SLOT(s1, s0, pk0, pk1, false, true, true, true, false, true, true, 0);
            TBAR();
            --j;
        }
        if (n >= 4) {
            j = n - 3;
            SLOT(s0, s1, pk1, pk0, true, true, true, true, false, true, true, 1);
            TBAR();
            j = n - 2;
            SLOT(s1, s0, pk0, pk1, false, true, true, true, false, true, true, 1);
            TBAR();
        }
        j = n - 1;
        SLOT(s0, s1, pk1, pk0, true, false, true, true, false, true, false, 0);
        TBAR();
        if (pass == 0) {
            const lds_cptr qr = (lds_cptr)lds + SPARE + rg * 8192 + lane * 16;
#pragma unroll
            for (int s = 0; s < 8; ++s) qf[s] = lds_frag(qr + s * 1024);
        }
        j = n;
        SLOT(s1, s0, pk0, pk1, false, false, false, true, false, false, false, 0);
        g0 += n;
        STAMP(4 + 2 * pass);
        {
            l += l2;
            auto rr = __builtin_amdgcn_permlane32_swap(__float_as_uint(l), __float_as_uint(l), false, false);
            l = __uint_as_float(rr[0]) + __uint_as_float(rr[1]);
            wait_bar<0>();
            LAS float* ml = (LAS float*)((lds_ptr)lds + SPARE);
            *(LAS f32x2*)(ml + (wid * 64 + lane) * 2) = (f32x2){m, l};
            lds_ptr sendp = (lds_ptr)lds + VRING + par * 32768 + rg * 8192 + lane * 16;
            lds_cptr recvp = (lds_cptr)lds + VRING + (par ^ 1) * 32768 + rg * 8192 + lane * 16;
            f32x16 keep[2];
            if (par) {
#pragma unroll
                for (int d = 0; d < 2; ++d) { keep[d] = o[2 + d];
#pragma unroll
                    for (int g = 0; g < 4; ++g) *(LAS f32x4*)(sendp + (d * 4 + g) * 1024) = (f32x4){o[d][4 * g], o[d][4 * g + 1], o[d][4 * g + 2], o[d][4 * g + 3]}; }
            } else {
#pragma unroll
                for (int d = 0; d < 2; ++d) { keep[d] = o[d];
#pragma unroll
                    for (int g = 0; g < 4; ++g) *(LAS f32x4*)(sendp + (d * 4 + g) * 1024) = (f32x4){o[2 + d][4 * g], o[2 + d][4 * g + 1], o[2 + d][4 * g + 2], o[2 + d][4 * g + 3]}; }
            }
            wait_bar<63>();
            const f32x2 mlo = *(const LAS f32x2*)(ml + ((wid ^ 4) * 64 + lane) * 2);
            const float mm = __builtin_fmaxf(m, mlo[0]), fo = EXP2(m - mm), fx = EXP2(mlo[0] - mm);
            const float inv = __builtin_amdgcn_rcpf(l * fo + mlo[1] * fx), sc = fo * inv, sx = fx * inv;
            const unsigned cdst = (unsigned)(WS_CTX + ((size_t)(blk * 4 + rg) * 32 + h) * 8192 + (par * 4) * 1024) + lane * 16;
#pragma unroll
            for (int d = 0; d < 2; ++d) {
#pragma unroll
                for (int g = 0; g < 4; ++g) {
                    const f32x4 v4 = *(const LAS f32x4*)(recvp + (d * 4 + g) * 1024);
#pragma unroll
                    for (int e = 0; e < 4; ++e) keep[d][4 * g + e] = keep[d][4 * g + e] * sc + v4[e] * sx;
                }
                st16(wr, cdst + (2 * d) * 1024, pack8(keep[d], 0));
                st16(wr, cdst + (2 * d + 1) * 1024, pack8(keep[d], 1));
            }
            if (pass == 0) wait_bar<63>();
            else signal_flag(flags + FL_F3 + blk * 32 + h, tag, tid);
        }
        STAMP(5 + 2 * pass);
    }
#undef SLOT
#undef TBAR
#undef VF
}

constexpr int CSTR = 4160;
constexpr int CCH = 8 * CSTR + 4608;
DI void p4_body(const Args& a, rsrc_t wr, int b, int h, int r, char* lds, int wid, int lane, int tid, GAS u64* flags, u64 tag) {
    const int ch = wid >> 1, c = 4 * b + ch, half = wid & 1;
    lds_ptr base = (lds_ptr)lds + ch * CCH;
    const lds_cptr ur = (lds_cptr)lds + STG_UR + ch * 4096, hs = (lds_cptr)lds + STG_H + ch * 4096;
    u32x4 w0[2], w1[2], hv[2];
#pragma unroll
    for (int it = 0; it < 2; ++it) { const int y8 = half * 128 + it * 64 + lane;
        w0[it] = *(const LAS u32x4*)(ur + 16 * y8); w1[it] = (u32x4){0u, 0u, 0u, 0u}; if (y8 + 1 < 256) w1[it] = *(const LAS u32x4*)(ur + 16 * y8 + 16);
        hv[it] = *(const LAS u32x4*)(hs + 16 * y8); }
    wait_bar<63>();
    const float Dc = bf2f(a.ssmD[c]);
#pragma unroll
    for (int it = 0; it < 2; ++it) {
        const int y8 = half * 128 + it * 64 + lane;
        unsigned w[8] = {w0[it][0], w0[it][1], w0[it][2], w0[it][3], w1[it][0], w1[it][1], w1[it][2], w1[it][3]}, o[7];
#pragma unroll
        for (int e = 0; e < 7; ++e) o[e] = __builtin_amdgcn_alignbit(w[e + 1], w[e], 16);
#pragma unroll
        for (int sg = 0; sg < 8; ++sg) { const int e = sg >> 1; u32x4 v;
            if (sg & 1) v = (u32x4){o[e], o[e + 1], o[e + 2], o[e + 3]}; else v = (u32x4){w[e], w[e + 1], w[e + 2], w[e + 3]};
            *(LAS u32x4*)(base + sg * CSTR + y8 * 16) = v; }
        *(LAS u32x4*)(base + 8 * CSTR + 512 + y8 * 16) = hv[it];
    }
    if (half == 1 && lane < 16) { const int sg = lane >> 1, y8 = 256 + (lane & 1); *(LAS u32x4*)(base + sg * CSTR + y8 * 16) = (u32x4){0u, 0u, 0u, 0u}; }
    if (half == 0 && lane < 32) *(LAS u32x4*)(base + 8 * CSTR + lane * 16) = (u32x4){0u, 0u, 0u, 0u};
    wait_bar<0>();
    if (tid == 0) flag_store(flags + FL_F1 + h * 8 + r, tag);
    const int j = lane & 15, g = lane >> 4;
    const lds_cptr ard = (lds_cptr)base + ((7 - j) & 7) * CSTR + (2024 - 8 * (j >> 3) + 8 * g) * 2;
    const lds_cptr brd = (lds_cptr)base + 8 * CSTR + (240 + 16 * j + 8 * g) * 2;
    f32x4 acc[8] = {};
#pragma unroll 1
    for (int rr = 0; rr < 4; ++rr) {
        const int rho = 2 * rr + half;
        bf16x8 A[8], B[8];
#pragma unroll
        for (int u = 0; u < 8; ++u) { A[u] = lds_frag(ard - (rho + 8 * u) * 64); B[u] = lds_frag(brd + (8 * u - rho) * 64); }
#pragma unroll
        for (int u = 0; u < 8; ++u)
#pragma unroll
            for (int v = 0; v + u < 8; ++v) acc[u + v] = MFMA16(A[u], B[v], acc[u + v]);
    }
    wait_bar<63>();
    lds_ptr ex = base + CSTR;
    f32x4 keep[4];
    if (half) {
#pragma unroll
        for (int q = 0; q < 4; ++q) { *(LAS f32x4*)(ex + 4096 + q * 1024 + lane * 16) = acc[q]; keep[q] = acc[4 + q]; }
    } else {
#pragma unroll
        for (int q = 0; q < 4; ++q) { *(LAS f32x4*)(ex + q * 1024 + lane * 16) = acc[4 + q]; keep[q] = acc[q]; }
    }
    wait_bar<63>();
    u32x2 zp[4];
#pragma unroll
    for (int q = 0; q < 4; ++q) {
        const f32x4 o = *(const LAS f32x4*)(ex + (half ^ 1) * 4096 + q * 1024 + lane * 16);
        const f32x4 y = keep[q] + o;
        const int t = 256 * (half * 4 + q) + 16 * j + 4 * g;
        const s16x4 kvr = *(const LAS s16x4*)(base + (2047 - t - 3) * 2);
        float z[4];
#pragma unroll
        for (int r = 0; r < 4; ++r) { const float kv = bf2f((u16)kvr[3 - r]); z[r] = y[r] + kv * Dc; }
        zp[q] = (u32x2){cvtpk(z[0], z[1]), cvtpk(z[2], z[3])};
    }
#pragma unroll
    for (int q = 0; q < 4; q += 2) {
        auto s0 = __builtin_amdgcn_permlane16_swap(zp[q][0], zp[q + 1][0], false, false);
        auto s1 = __builtin_amdgcn_permlane16_swap(zp[q][1], zp[q + 1][1], false, false);
        const int t = 256 * (half * 4 + q + (g & 1)) + 16 * j + 8 * (g >> 1);
        st16(wr, (unsigned)(WS_Z + ((size_t)c * S + t) * 2), (u32x4){s0[0], s1[0], s0[1], s1[1]});
    }
}

constexpr int OCH = 36864;
DI void p5_body(const Args& a, rsrc_t wr, int mt, int nt, char* lds, int wid, int lane, int tid) {
    const unsigned lds0 = (unsigned)(uintptr_t)lds;
    const int rh = wid & 1, kq = wid >> 1;
    const char* cg = a.ws + WS_CTX + (size_t)(2 * mt + (wid >> 2)) * 32 * 8192 + (wid & 3) * 2048;
    const unsigned cl = lds0 + (wid >> 2) * 8192 + (wid & 3) * 2048;
    const unsigned tv = lane * 16;
    const char* wg = a.ws + WS_WO + (size_t)nt * 32 * 20480 + wid * 2048;
    const char* wg2 = a.ws + WS_WO + (size_t)nt * 32 * 20480 + 16384 + (wid & 3) * 1024;
    const unsigned wl = lds0 + 16384 + wid * 2048, wl2 = lds0 + 32768 + (wid & 3) * 1024;
    const int j = lane & 15, g = lane >> 4;
    const lds_cptr ard = (lds_cptr)lds + rh * 8192 + kq * 2048 + (g >> 1) * 1024 + ((g & 1) * 32 + j) * 16;
    const lds_cptr brd = (lds_cptr)lds + 16384 + kq * 5120 + lane * 16;
    f32x4 acc[2][5] = {};
#define P5LOOP(FIVE) do { \
        _Pragma("unroll") for (int h = 0; h < 3; ++h) { dma2c(cl + h * OCH, tv, cg + (size_t)h * 8192); dma2c(wl + h * OCH, tv, wg + (size_t)h * 20480); if (FIVE) dma1c(wl2 + h * OCH, tv, wg2 + (size_t)h * 20480); } \
        const char* cgn = cg + 3 * 8192; const char* wgn = wg + 3 * 20480; const char* wg2n = wg2 + 3 * 20480; \
        unsigned so = 0; \
        _Pragma("unroll 1") for (int h = 0; h < 32; ++h) { \
            wait_bar<(FIVE) ? 10 : 8>(); \
            const lds_cptr ab = ard + so, bb = brd + so; \
            const unsigned sn = so == 0 ? 3 * OCH : so - OCH; \
            const bf16x8 a0 = lds_frag(ab), a1 = lds_frag(ab + 256); \
            bf16x8 wf[5]; \
            _Pragma("unroll") for (int cb = 0; cb < 5; ++cb) wf[cb] = lds_frag(bb + cb * 1024); \
            SBAR(); \
            dma2c(cl + sn, tv, cgn); dma2c(wl + sn, tv, wgn); if (FIVE) dma1c(wl2 + sn, tv, wg2n); \
            SBAR(); \
            _Pragma("unroll") for (int cb = 0; cb < 5; ++cb) { \
                acc[0][cb] = MFMA16(wf[cb], a0, acc[0][cb]); PIN(acc[0][cb]); \
                SBAR(); \
                acc[1][cb] = MFMA16(wf[cb], a1, acc[1][cb]); PIN(acc[1][cb]); \
                SBAR(); \
            } \
            so = so == 3 * OCH ? 0 : so + OCH; \
            const int adv = h < 28 ? 1 : 0; cgn += adv * 8192; wgn += adv * 20480; wg2n += adv * 20480; \
        } } while (0)
    if (wid < 4) P5LOOP(true); else P5LOOP(false);
#undef P5LOOP
    wait_bar<0>();
    constexpr int PSTR = 84 * 4;
    {
        lds_ptr pb = (lds_ptr)lds + kq * 64 * PSTR;
#pragma unroll
        for (int rb2 = 0; rb2 < 2; ++rb2)
#pragma unroll
            for (int cb = 0; cb < 5; ++cb) *(LAS f32x4*)(pb + (32 * rh + 16 * rb2 + j) * PSTR + (16 * cb + 4 * g) * 4) = acc[rb2][cb];
        lds_ptr zb = (lds_ptr)lds + 4 * 64 * PSTR;
#pragma unroll
        for (int it = 0; it < 5; ++it) { const int e = tid + it * 512, cl2 = e >> 5, d = e & 31;
            *(LAS unsigned*)(zb + cl2 * 132 + d * 4) = ld4c(wr, (unsigned)(WS_Z + ((size_t)(nt * 80 + cl2) * S + mt * 64 + 2 * d) * 2)); }
    }
    wait_bar<0>();
    {
        const int row = tid >> 3, c0 = (tid & 7) * 10, t = mt * 64 + row;
        const float gt = __uint_as_float(ld4c(wr, (unsigned)(WS_G + t * 4))), g1 = 1.f - gt;
        const lds_cptr pb = (lds_cptr)lds + row * PSTR + c0 * 4;
        const lds_cptr zb = (lds_cptr)lds + 4 * 64 * PSTR + row * 2;
        const u16* xr = a.x + (size_t)t * M + nt * 80 + c0; u16* orow = a.out + (size_t)t * M + nt * 80 + c0;
#pragma unroll
        for (int e = 0; e < 10; e += 2) {
            float r2[2]; const unsigned xv = *(const unsigned*)(xr + e);
#pragma unroll
            for (int k = 0; k < 2; ++k) {
                const int cc = e + k;
                const float av = *(const LAS float*)(pb + cc * 4) + *(const LAS float*)(pb + 64 * PSTR + cc * 4) + *(const LAS float*)(pb + 128 * PSTR + cc * 4) + *(const LAS float*)(pb + 192 * PSTR + cc * 4);
                const float z = bf2f(*(const LAS u16*)(zb + (c0 + cc) * 132));
                r2[k] = gt * av + g1 * (bf2f((u16)(k ? xv >> 16 : xv & 0xffff)) * z);
            }
            *(unsigned*)(orow + e) = cvtpk(r2[0], r2[1]);
        }
    }
}

__global__ __launch_bounds__(512, 2) void k_fused(const u16* x, const u16* ckv, const u16* wq, const u16* wuk, const u16* wuv, const u16* wo, const u16* bk, const u16* bv,
                                                  const u16* shk, const u16* ssmk, const u16* ssmD, const u16* gw, u16* out, char* ws) {
    extern __shared__ __attribute__((aligned(16))) char lds[];
    const Args a = {x, ckv, wq, wuk, wuv, wo, bk, bv, shk, ssmk, ssmD, gw, out, ws};
    const int tid = threadIdx.x, lane = tid & 63, wid = rfl(tid >> 6);
    const int b = blockIdx.x, xcd = b & 7, idx = b >> 3, h = xcd * 4 + (idx >> 3), r = idx & 7;
    const unsigned lds0 = (unsigned)(uintptr_t)lds;
    const rsrc_t wr = __builtin_amdgcn_make_buffer_rsrc(ws, 0, (int)WS_END, 0x00020000);
    GAS u64* flags = (GAS u64*)(ws + WS_F);
    STAMP(0);
    const P1Ctx c1 = p1_setup(a, h, r, lds0, wid, lane);
    const u64 tag = *(const __attribute__((address_space(4))) u64*)(ws + WS_F) + 1;
    p0a_load(a, b & 7, b >> 3, lds0, wid, lane);
    p1_prime01(c1);
    wait_bar<12>();
    p0a_finish(wr, b & 7, b >> 3, lds, tid);
    p1_prime2(c1);
    STAMP(1);
    if (wid < 4) p1_body<0>(a, c1, wr, b, h, r, lds, wid, lane, tid, flags, tag); else p1_body<1>(a, c1, wr, b, h, r, lds, wid, lane, tid, flags, tag);
    STAMP(2);
    if (b < 160) p4_body(a, wr, b, h, r, lds, wid, lane, tid, flags, tag);
    else { signal_flag(flags + FL_F1 + h * 8 + r, tag, tid); p0b_all(a, wr, b - 160, lds, wid, lane); }
    wait_bar<63>();
    STAMP(8);
    p2_body(a, b, h, r, 15 - r, r, lds, wid, lane, tid, flags, tag);
    STAMP(3);
    p3_body(wr, ws, h, 15 - r, r, lds, wid, lane, tid, flags, tag);
    {
        const int mt = xcd * 4 + (idx >> 3), nt = idx & 7;
        if (wid == 0) {
            poll_flags(flags, lane < 20 ? FL_F4 + nt * 20 + lane : (lane < 28 ? FL_FW + (lane - 20) * 32 + mt : -1), tag);
            poll_flags(flags, lane < 32 ? FL_F3 + (mt >> 1) * 32 + lane : FL_FW + nt * 32 + (lane - 32), tag);
        }
        wait_bar<0>();
        STAMP(9);
        p5_body(a, wr, mt, nt, lds, wid, lane, tid);
    }
    if (b == 0 && tid == 0) flag_store(flags, tag);
    STAMP(10);
}

extern "C" void kernel_launch(void* const* d_in, const int* in_sizes, int n_in,
                              void* d_out, int out_size, void* d_ws, size_t ws_size,
                              hipStream_t stream) {
    static bool attr = false;
    if (!attr) { (void)hipFuncSetAttribute((const void*)k_fused, hipFuncAttributeMaxDynamicSharedMemorySize, LDS_BYTES); attr = true; }
    k_fused<<<256, 512, LDS_BYTES, stream>>>((const u16*)d_in[0], (const u16*)d_in[1], (const u16*)d_in[2], (const u16*)d_in[3], (const u16*)d_in[4], (const u16*)d_in[5],
                                             (const u16*)d_in[7], (const u16*)d_in[8], (const u16*)d_in[9], (const u16*)d_in[10], (const u16*)d_in[11], (const u16*)d_in[12],
                                             (u16*)d_out, (char*)d_ws);
}
```

```cpp
#include <hip/hip_runtime.h>
#include <hip/hip_bf16.h>
#include <stdint.h>

typedef unsigned short u16;
typedef unsigned long long u64;
typedef short bf16x8 __attribute__((ext_vector_type(8)));
typedef short s16x4 __attribute__((ext_vector_type(4)));
typedef float f32x16 __attribute__((ext_vector_type(16)));
typedef float f32x4 __attribute__((ext_vector_type(4)));
typedef float f32x2 __attribute__((ext_vector_type(2)));
typedef __bf16 bf16x2_t __attribute__((ext_vector_type(2)));
typedef unsigned u32x4 __attribute__((ext_vector_type(4)));
typedef unsigned u32x2 __attribute__((ext_vector_type(2)));
typedef short v4i16_t __attribute__((ext_vector_type(4)));
#define LAS __attribute__((address_space(3)))
#define GAS __attribute__((address_space(1)))
typedef LAS const char* lds_cptr;
typedef LAS char* lds_ptr;
#define DI __device__ __forceinline__
#define SBAR() __builtin_amdgcn_sched_barrier(0)
#define PIN(x) asm volatile("" : "+v"(x))
#define PINS(x) asm volatile("" : "+s"(x))
#define MFMA32(a, b, c) __builtin_amdgcn_mfma_f32_32x32x16_bf16(a, b, c, 0, 0, 0)
#define MFMA16(a, b, c) __builtin_amdgcn_mfma_f32_16x16x32_bf16(a, b, c, 0, 0, 0)
#define LGKM(n) __builtin_amdgcn_s_waitcnt(0xC07F | ((n) << 8))
#define EXP2(v) __builtin_amdgcn_exp2f(v)

constexpr int S = 2048, H = 32, D = 128, M = 640, DC = 512, HD = H * D;
constexpr int LDS_BYTES = 163840;
constexpr size_t WS_K = 0, WS_V = (size_t)S * HD * 2, WS_CTX = 2 * WS_V, WS_UR = 3 * WS_V, WS_Z = WS_UR + (size_t)S * M * 2, WS_WO = WS_Z + (size_t)S * M * 2,
                 WS_G = WS_WO + (size_t)HD * M * 2, WS_F = WS_G + 8192, WS_END = WS_F + 32768;
constexpr int FL_F0 = 16, FL_F1 = FL_F0 + 256, FL_F3 = FL_F1 + 256, FL_F4 = FL_F3 + 512, FL_FW = FL_F4 + 160;

#define STAMP(i)

DI float bf2f(u16 b) { return __uint_as_float(((unsigned)b) << 16); }
DI unsigned cvtpk(float lo, float hi) { f32x2 v = {lo, hi}; bf16x2_t b = __builtin_convertvector(v, bf16x2_t); return __builtin_bit_cast(unsigned, b); }
DI u16 f2bf(float f) { return (u16)cvtpk(f, f); }
DI float rb(float f) { return __uint_as_float(cvtpk(f, f) << 16); }
DI unsigned rfl(unsigned x) { return __builtin_amdgcn_readfirstlane(x); }

#define SETM0(base, imm) asm volatile("s_add_u32 m0, %0, %1" :: "s"(base), "i"(imm) : "scc")
#define GLDS(voff, sbase, imm) asm volatile("global_load_lds_dwordx4 %0, %1 offset:%c2" :: "v"(voff), "s"(sbase), "i"(imm) : "memory")
#define GLDSC(voff, sbase, imm) asm volatile("global_load_lds_dwordx4 %0, %1 offset:%c2 sc1" :: "v"(voff), "s"(sbase), "i"(imm) : "memory")
DI void dma1(unsigned ldsaddr, unsigned voff, const void* sbase) {
    asm volatile("s_mov_b32 m0, %0\n\ts_nop 0\n\tglobal_load_lds_dwordx4 %1, %2" :: "s"(ldsaddr), "v"(voff), "s"(sbase) : "memory");
}
DI void dma2(unsigned ldsaddr, unsigned voff, const void* sbase) {
    asm volatile("s_mov_b32 m0, %0\n\ts_nop 0\n\tglobal_load_lds_dwordx4 %1, %2\n\tglobal_load_lds_dwordx4 %1, %2 offset:1024" :: "s"(ldsaddr), "v"(voff), "s"(sbase) : "memory");
}
DI void dma1c(unsigned ldsaddr, unsigned voff, const void* sbase) {
    asm volatile("s_mov_b32 m0, %0\n\ts_nop 0\n\tglobal_load_lds_dwordx4 %1, %2 sc1" :: "s"(ldsaddr), "v"(voff), "s"(sbase) : "memory");
}
DI void dma2c(unsigned ldsaddr, unsigned voff, const void* sbase) {
    asm volatile("s_mov_b32 m0, %0\n\ts_nop 0\n\tglobal_load_lds_dwordx4 %1, %2 sc1\n\tglobal_load_lds_dwordx4 %1, %2 offset:1024 sc1" :: "s"(ldsaddr), "v"(voff), "s"(sbase) : "memory");
}
template <int N> DI void wait_bar_v() { asm volatile("s_waitcnt vmcnt(%0)\n\ts_barrier" :: "i"(N) : "memory"); }
template <int N> DI void wait_bar() { asm volatile("s_waitcnt vmcnt(%0) lgkmcnt(0)\n\ts_barrier" :: "i"(N) : "memory"); }
DI bf16x8 lds_frag(lds_cptr p) { return *(const LAS bf16x8*)p; }
DI s16x4 vtr(lds_cptr p) { return __builtin_bit_cast(s16x4, __builtin_amdgcn_ds_read_tr16_b64_v4i16((LAS v4i16_t*)p)); }
DI bf16x8 tr_frag(lds_cptr p, int second) { const s16x4 lo = vtr(p), hi = vtr(p + second); return __builtin_shufflevector(lo, hi, 0, 1, 2, 3, 4, 5, 6, 7); }
DI u32x4 pack8(const f32x16& x, int s) {
    u32x4 w; w[0] = cvtpk(x[8 * s + 0], x[8 * s + 1]); w[1] = cvtpk(x[8 * s + 2], x[8 * s + 3]); w[2] = cvtpk(x[8 * s + 4], x[8 * s + 5]); w[3] = cvtpk(x[8 * s + 6], x[8 * s + 7]); return w;
}
typedef __amdgpu_buffer_rsrc_t rsrc_t;
DI void st16(rsrc_t r, unsigned off, u32x4 v) { __builtin_amdgcn_raw_buffer_store_b128(v, r, off, 0, 16); }
DI void st8(rsrc_t r, unsigned off, u32x2 v) { __builtin_amdgcn_raw_buffer_store_b64(v, r, off, 0, 16); }
DI void st4(rsrc_t r, unsigned off, unsigned v) { __builtin_amdgcn_raw_buffer_store_b32(v, r, off, 0, 16); }
DI void signal_flag(GAS u64* flag, u64 tag, int tid) {
    asm volatile("s_waitcnt vmcnt(0) lgkmcnt(0)\n\ts_barrier" ::: "memory");
    if (tid == 0) __hip_atomic_store(flag, tag, __ATOMIC_RELAXED, __HIP_MEMORY_SCOPE_AGENT);
}
DI void poll_flags(GAS u64* flags, int idx, u64 tag) {
    for (unsigned spins = 0; spins < (1u << 16); ++spins) {
        const u64 v = idx >= 0 ? __hip_atomic_load(flags + idx, __ATOMIC_RELAXED, __HIP_MEMORY_SCOPE_AGENT) : tag;
        if (__all(v == tag)) break;
        __builtin_amdgcn_s_sleep(2);
    }
}
DI unsigned ld4c(rsrc_t r, unsigned off) { return __builtin_amdgcn_raw_buffer_load_b32(r, off, 0, 16); }
DI void flag_store(GAS u64* flag, u64 tag) { __hip_atomic_store(flag, tag, __ATOMIC_RELAXED, __HIP_MEMORY_SCOPE_AGENT); }

struct Args { const u16 *x, *ckv, *wq, *wuk, *wuv, *wo, *bk, *bv, *shk, *ssmk, *ssmD, *gw; u16* out; char* ws; };

constexpr int P0_OFF = 133120;
DI float blo(unsigned w) { return __uint_as_float(w << 16); }
DI float bhi(unsigned w) { return __uint_as_float(w & 0xffff0000u); }
template <int CTRL> DI float dpp_add(float v) { return v + __builtin_bit_cast(float, __builtin_amdgcn_update_dpp(0, __builtin_bit_cast(int, v), CTRL, 0xf, 0xf, true)); }
DI float wave_sum(float v) {
    v = dpp_add<0x128>(v); v = dpp_add<0x124>(v); v = dpp_add<0x122>(v); v = dpp_add<0x121>(v);
    auto a = __builtin_amdgcn_permlane16_swap(__float_as_uint(v), __float_as_uint(v), false, false); v = __uint_as_float(a[0]) + __uint_as_float(a[1]);
    auto b = __builtin_amdgcn_permlane32_swap(__float_as_uint(v), __float_as_uint(v), false, false); return __uint_as_float(b[0]) + __uint_as_float(b[1]);
}
DI int div10(int x) { return (x * 6554) >> 16; }
DI void p0a_load(const Args& a, int jb0, unsigned lds0, int wid, int lane) {
#pragma unroll
    for (int i = 0; i < 9; ++i) {
        const int p = wid + 8 * i;
        if (p < 66) {
            const int k = p >= 44 ? 2 : (p >= 22 ? 1 : 0), q = p - 22 * k;
            int jb = jb0 + 96 * k; if (jb >= 256) jb = jb0;
            const int cb = jb & 7, tb = jb >> 3;
            if (q < 11) { const int ci = q * 64 + lane, cj = ci < 670 ? ci : 669, row = div10(cj); int t = tb * 64 - 3 + row; t = t < 0 ? 0 : t;
                dma1(lds0 + p * 1024, (unsigned)(t * 1280 + (cj - 10 * row) * 16), (const char*)a.bk + cb * 160); }
            else if (q < 21) { const int ci = (q - 11) * 64 + lane, row = div10(ci);
                dma1(lds0 + p * 1024, (unsigned)(row * 1280 + (ci - 10 * row) * 16), (const char*)a.bv + (size_t)tb * 64 * 1280 + cb * 160); }
            else dma1(lds0 + p * 1024, (unsigned)((lane < 40 ? lane : 39) * 16), (const char*)a.shk + cb * 640);
        }
    }
}
DI void p0a_compute(int jb0, char* lds, int tid) {
    const int nj = jb0 + 192 < 256 ? 3 : 2;
#pragma unroll
    for (int it = 0; it < 2; ++it) {
        const int item = tid + it * 512, k = item >= 640 ? 2 : (item >= 320 ? 1 : 0), e = item - k * 320, tp = div10(e), cc = e - 10 * tp;
        if (k < nj && item < 960) {
            const int t0 = ((jb0 + 96 * k) >> 3) * 64;
            const lds_cptr in = (lds_cptr)lds + k * 22528;
            LAS char* tile = (LAS char*)lds + P0_OFF + k * 10240;
            u32x4 kk[5], vv[2], sh[4];
#pragma unroll
            for (int i = 0; i < 5; ++i) kk[i] = *(const LAS u32x4*)(in + (2 * tp + i) * 160 + cc * 16);
#pragma unroll
            for (int i = 0; i < 2; ++i) vv[i] = *(const LAS u32x4*)(in + 11264 + (2 * tp + i) * 160 + cc * 16);
#pragma unroll
            for (int i = 0; i < 4; ++i) sh[i] = *(const LAS u32x4*)(in + 21504 + cc * 64 + i * 16);
            if (t0 + 2 * tp < 3) {
#pragma unroll
                for (int i = 0; i < 3; ++i) if (t0 + 2 * tp + i - 3 < 0) kk[i] = (u32x4){0u, 0u, 0u, 0u};
            }
            const int dw = 31 - tp, pos = (((dw >> 2) ^ (cc & 7)) << 4) + (dw & 3) * 4;
#pragma unroll
            for (int qp = 0; qp < 4; ++qp) {
                f32x2 kx[5], r2[2];
#pragma unroll
                for (int i = 0; i < 5; ++i) kx[i] = (f32x2){blo(kk[i][qp]), bhi(kk[i][qp])};
                const f32x2 t0_ = {blo(sh[qp][0]), blo(sh[qp][2])}, t1_ = {bhi(sh[qp][0]), bhi(sh[qp][2])}, t2_ = {blo(sh[qp][1]), blo(sh[qp][3])}, t3_ = {bhi(sh[qp][1]), bhi(sh[qp][3])};
#pragma unroll
                for (int r = 0; r < 2; ++r)
                    r2[r] = (kx[r] * t3_ + kx[r + 1] * t2_ + kx[r + 2] * t1_ + kx[r + 3] * t0_) * (f32x2){blo(vv[r][qp]), bhi(vv[r][qp])};
                *(LAS unsigned*)(tile + (8 * cc + 2 * qp) * 128 + pos) = cvtpk(r2[1][0], r2[0][0]);
                *(LAS unsigned*)(tile + (8 * cc + 2 * qp + 1) * 128 + pos) = cvtpk(r2[1][1], r2[0][1]);
            }
        }
    }
}
DI void p0a_store(rsrc_t wr, int jb0, char* lds, int tid) {
    const int nj = jb0 + 192 < 256 ? 3 : 2;
#pragma unroll
    for (int it = 0; it < 4; ++it) { const int e = tid + it * 512, k = e >= 1280 ? 2 : (e >= 640 ? 1 : 0), f = e - k * 640, cl = f >> 3, d = f & 7;
        if (k < nj && e < 1920) { const int jb = jb0 + 96 * k;
            st16(wr, (unsigned)(WS_UR + ((size_t)((jb & 7) * 80 + cl) * S + (2047 - (jb >> 3) * 64 - 63) + 8 * d) * 2),
                 *(const LAS u32x4*)((lds_cptr)lds + P0_OFF + k * 10240 + cl * 128 + ((d ^ ((cl >> 3) & 7)) << 4))); } }
}
DI void p0b_load(const Args& a, int b, unsigned lds0, int wid, int lane) {
    const int cb = b & 7, tb = b >> 3;
#pragma unroll
    for (int i = 0; i < 3; ++i) { const int p = wid + 8 * i;
        if (p < 20) { const int ci = p * 64 + lane, row = div10(ci);
            dma1(lds0 + P0_OFF + p * 1024, (unsigned)(row * 1280 + (ci - 10 * row) * 16), (const char*)a.wo + (size_t)tb * 128 * 1280 + cb * 160); }
    }
}
DI void p0b_finish(rsrc_t wr, int b, char* lds, int wid, int lane) {
    const int cb = b & 7, tb = b >> 3;
    const int j = lane & 15, g = lane >> 4, q = j >> 2, p = lane & 3;
    const lds_cptr brd = (lds_cptr)lds + P0_OFF + (16 * (g >> 1) + 4 * (g & 1) + q) * 160 + p * 8;
#pragma unroll
    for (int i = 0; i < 3; ++i) if (wid + 8 * i < 20) { const int f = wid + 8 * i, kq = f / 5, cbk = f % 5;
        const bf16x8 w = tr_frag(brd + kq * 32 * 160 + cbk * 32, 1280);
        st16(wr, (unsigned)(WS_WO + ((size_t)(cb * 32 + tb) * 20 + f) * 1024 + lane * 16), __builtin_bit_cast(u32x4, w)); }
}

#define PF_TILE 6
constexpr int TSLOT = 32 * 1040;
struct P1Ctx { const char* wsrc; const char* tsrc; unsigned wvoff, wlds, tvoff, tlds; };
DI P1Ctx p1_setup(const Args& a, int h, int r, unsigned lds0, int wid, int lane) {
    P1Ctx c;
    c.wsrc = (const char*)((wid >> 2) ? a.wuv : a.wuk) + (size_t)h * 256 + ((wid >> 1) & 1) * 128 + (size_t)(wid & 1) * 4 * 8 * 8192;
    c.wvoff = (unsigned)(lane >> 3) * 8192u + (unsigned)(((lane & 7) ^ (((lane >> 4) & 1) << 2)) * 16);
    c.wlds = lds0 + ((wid >> 2) * 2 + ((wid >> 1) & 1)) * 8192 + (wid & 1) * 4 * 1024;
    c.tsrc = (const char*)a.ckv + (size_t)(256 * r + 4 * wid) * 1024;
    c.tvoff = lane * 16; c.tlds = lds0 + 4 * wid * 1040;
    return c;
}
DI void p1_dma_w1(const P1Ctx& c, int ch, int slot, int i) { dma1(c.wlds + slot * TSLOT + i * 1024, c.wvoff, c.wsrc + (size_t)ch * 64 * 8192 + (size_t)i * 8 * 8192); }
template <int J> DI void p1_dma_t1(const P1Ctx& c, int t, int slot) {
    asm volatile("s_mov_b32 m0, %0\n\ts_nop 0\n\tglobal_load_lds_dwordx4 %1, %2 offset:%c3" :: "s"(c.tlds + slot * TSLOT + 16 * J), "v"(c.tvoff), "s"(c.tsrc + (size_t)t * 32 * 1024), "i"(J * 1024) : "memory");
}
DI void p1_prime_t(const P1Ctx& c) { p1_dma_t1<0>(c, 0, 3); p1_dma_t1<1>(c, 0, 3); p1_dma_t1<2>(c, 0, 3); p1_dma_t1<3>(c, 0, 3); }
DI void p1_prime_w(const P1Ctx& c) {
#pragma unroll
    for (int ch = 0; ch < 3; ++ch)
#pragma unroll
        for (int i = 0; i < 4; ++i) p1_dma_w1(c, ch, ch, i);
}
constexpr int STG_UR = 3 * TSLOT, STG_H = STG_UR + 16384;
template <int MAT>
DI void p1_body(const Args& a, const P1Ctx& c, rsrc_t wr, int b, int h, int r, char* lds, int wid, int lane, int tid, GAS u64* flags, u64 tag) {
    const int g4 = lane >> 4, q = (lane & 15) >> 2, p = lane & 3;
    const lds_cptr wrd = (lds_cptr)lds + (MAT * 2 + ((wid >> 1) & 1)) * 8192 + (8 * (g4 >> 1) + q) * 128 + (((wid & 1) ^ (q >> 1)) * 64) + (g4 & 1) * 32 + p * 8;
    const lds_cptr trd = (lds_cptr)lds + (lane & 31) * 1040 + (lane >> 5) * 16;
    const unsigned dst = (unsigned)((MAT ? WS_V : WS_K) + ((size_t)(h * 64 + 8 * r) * 8 + 2 * (wid & 3)) * 1024) + lane * 16;
    unsigned pfd = 0;
    bf16x8 Wf[32];
    f32x16 acc = f32x16{};
    const lds_cptr t0p = trd + 3 * TSLOT;
#define P1M(A_, W_) (MAT == 0 ? MFMA32(W_, A_, acc) : MFMA32(A_, W_, acc))
#pragma unroll
    for (int ch = 0; ch < 8; ++ch) {
        if (ch == 0) wait_bar<8>(); else wait_bar<4>();
        if (ch == 3 && b >= 160 && tid < 3) { const int jb = b - 160 + 96 * tid; if (jb < 256) flag_store(flags + FL_F0 + (jb & 7) * 32 + (jb >> 3), tag); }
        const lds_cptr wb = wrd + (ch % 3) * TSLOT;
        bf16x8 af[4];
#pragma unroll
        for (int s = 0; s < 4; ++s) { Wf[4 * ch + s] = tr_frag(wb + s * 2048, 512); af[s] = lds_frag(t0p + (4 * ch + s) * 32); }
        SBAR();
#pragma unroll
        for (int s = 0; s < 4; ++s) {
            if (ch >= 1) { if (ch + 2 <= 7) p1_dma_w1(c, ch + 2, (ch + 2) % 3, s);
                           else { if (s == 0) p1_dma_t1<0>(c, ch - 5, (ch + 2) % 3); if (s == 1) p1_dma_t1<1>(c, ch - 5, (ch + 2) % 3); if (s == 2) p1_dma_t1<2>(c, ch - 5, (ch + 2) % 3); if (s == 3) p1_dma_t1<3>(c, ch - 5, (ch + 2) % 3); } }
            acc = P1M(af[s], Wf[4 * ch + s]); PIN(acc);
            SBAR();
        }
    }
    f32x16 accp = acc;
    bf16x8 af[4];
    wait_bar<4>();
#pragma unroll
    for (int s = 0; s < 4; ++s) af[s] = lds_frag(trd + (8 % 3) * TSLOT + s * 32);
    SBAR();
#pragma unroll
    for (int i = 1; i < 8; ++i) {
        const int j = 7 + i;
        const lds_cptr tb = trd + (j % 3) * TSLOT, tn = trd + ((j + 1) % 3) * TSLOT;
#pragma unroll
        for (int s = 0; s < 32; ++s) {
            if (s == 28 && i < 7) {
                if (b < 160 && i == 4 && wid == 0) poll_flags(flags, lane < 32 ? FL_F0 + (b / 20) * 32 + lane : -1, tag);
                if (i + 2 <= 7) wait_bar<6>(); else wait_bar<2>();
                const unsigned lds0 = (unsigned)(uintptr_t)lds;
                if (i == 1) p0b_load(a, b, lds0, wid, lane);
                if (i == PF_TILE) {
                    const int id = (b * 8 + wid) * 60 + (lane < 59 ? lane : 59);
                    const char* p = id < 40960 ? (const char*)a.x + (size_t)id * 64 : (const char*)a.wq + (size_t)(id - 40960) * 64;
                    asm volatile("global_load_dword %0, %1, off" : "+v"(pfd) : "v"(p) : "memory"); }
                if (b < 160) {
                    if (i == 1) dma2(lds0 + STG_H + wid * 2048, lane * 16, (const char*)a.ssmk + (size_t)(4 * b) * 4096 + wid * 2048);
                    if (i == 4) dma2c(lds0 + STG_UR + wid * 2048, lane * 16, a.ws + WS_UR + (size_t)(4 * b) * 4096 + wid * 2048);
                }
                SBAR();
            }
            if (!(s & 1) && s + 4 < 32) { LGKM(2); SBAR(); }
            if (s == 0) acc = MAT == 0 ? MFMA32(Wf[0], af[0], (f32x16{})) : MFMA32(af[0], Wf[0], (f32x16{})); else acc = P1M(af[s & 3], Wf[s]);
            PIN(acc);
            if (s + 4 < 32) af[s & 3] = lds_frag(tb + (s + 4) * 32); else if (i < 7) af[s & 3] = lds_frag(tn + (s - 28) * 32);
            if (s == 2) st16(wr, dst + (i - 1) * 8192, pack8(accp, 0));
            if (s == 4) st16(wr, dst + (i - 1) * 8192 + 1024, pack8(accp, 1));
            if (i + 2 <= 7) {
                if (s == 6) p1_dma_t1<0>(c, i + 2, (j + 2) % 3);
                if (s == 11) p1_dma_t1<1>(c, i + 2, (j + 2) % 3);
                if (s == 16) p1_dma_t1<2>(c, i + 2, (j + 2) % 3);
                if (s == 21) p1_dma_t1<3>(c, i + 2, (j + 2) % 3);
            }
            SBAR();
        }
        accp = acc;
    }
    asm volatile("" : "+v"(pfd));
    st16(wr, dst + 7 * 8192, pack8(accp, 0)); st16(wr, dst + 7 * 8192 + 1024, pack8(accp, 1));
#undef P1M
}

constexpr float QSCALE = 0.08838834764831845f * 1.4426950408889634f;
constexpr float THR = 8.f;
constexpr int QCH = 49152;
constexpr int KRING = 0, VRING = 65536, SPARE = 131072, QA_OFF = VRING + 32768;
#define MX3(a, b, c) __builtin_fmaxf(__builtin_fmaxf((a), (b)), (c))

DI void p2_body(const Args& a, int b, int h, int r, int blkA, int blkB, char* lds, int wid, int lane, int tid, GAS u64* flags, u64 tag) {
    const unsigned lds0 = (unsigned)(uintptr_t)lds;
    const int r32 = lane & 31, hi = lane >> 5, rg = wid & 3, par = wid >> 2;
    const int myblk = par ? blkB : blkA;
    const char* xg = (const char*)a.x + (size_t)(128 * myblk + 32 * rg) * 1280;
    const unsigned xvoff = (unsigned)(lane >> 3) * 1280u + (unsigned)(((lane & 7) ^ ((lane >> 4) & 7)) * 16);
    const unsigned xvoff2 = (unsigned)(lane >> 3) * 1280u + 8 * 1280u + (unsigned)(((lane & 7) ^ ((4 + (lane >> 4)) & 7)) * 16);
    const unsigned xlds = lds0 + 16384 + wid * 4096;
    const char* wg = (const char*)a.wq + (size_t)h * 256 + (wid >> 2) * 128 + (size_t)(wid & 3) * 2 * 8 * 8192;
    const unsigned wvoff = (unsigned)(lane >> 3) * 8192u + (unsigned)(((lane & 7) ^ (((lane >> 4) & 1) << 2)) * 16);
    const unsigned wvoff2 = wvoff + 8 * 8192u;
    const unsigned wlds = lds0 + 16384 + 32768 + (wid >> 2) * 8192 + (wid & 3) * 2048;
    const int g4 = lane >> 4, q = (lane & 15) >> 2, p = lane & 3;
    const int hsw = (q >> 1) & 1, ysw = hi ^ ((r32 >> 1) & 7);
    const lds_cptr wrd0 = (lds_cptr)lds + 16384 + 32768 + (8 * (g4 >> 1) + q) * 128 + (g4 & 1) * 32 + p * 8 + hsw * 64;
    const lds_cptr wrd1 = (lds_cptr)lds + 16384 + 32768 + (8 * (g4 >> 1) + q) * 128 + (g4 & 1) * 32 + p * 8 + (hsw ^ 1) * 64;
    lds_cptr xrd[4];
#pragma unroll
    for (int s = 0; s < 4; ++s) xrd[s] = (lds_cptr)lds + 16384 + wid * 4096 + r32 * 128 + (((2 * s) ^ ysw) << 4);
    f32x16 acc[4] = {};
#pragma unroll
    for (int c = 0; c < 2; ++c) { const char* xp = xg + c * 128; const char* wp = wg + (size_t)c * 64 * 8192; const unsigned so_ = ((c + 2) % 3) * QCH;
        dma1(xlds + so_, xvoff, xp); dma1(xlds + so_ + 1024, xvoff2, xp); dma1(xlds + so_ + 2048, xvoff, xp + 16 * 1280); dma1(xlds + so_ + 3072, xvoff2, xp + 16 * 1280);
        dma1(wlds + so_, wvoff, wp); dma1(wlds + so_ + 1024, wvoff2, wp); }
    unsigned so = 2 * QCH;
    const char* xp = xg + 2 * 128; const char* wp = wg + (size_t)2 * 64 * 8192;
    const unsigned kl0 = lds0 + KRING + wid * 2048, tvoff = lane * 16;
    const char* Kw = a.ws + WS_K + (size_t)h * 64 * 8192 + wid * 2048;
#define WFR(B0, B1, i) tr_frag((((i) & 1) ? (B1) : (B0)) + (((i) >> 1) & 1) * 8192 + ((i) >> 2) * 2048, 512)
    bf16x8 wf[4], xf[2];
#define P2CHUNK(MODE, EXTRA, LAST) do { \
        const unsigned sn = so == 0 ? 2 * QCH : so - QCH, sx = so == 2 * QCH ? 0 : so + QCH;        \
        const unsigned xl_ = xlds + sn, wl_ = wlds + sn; const char* xp2 = xp + 16 * 1280; \
        const lds_cptr wb0 = wrd0 + so, wb1 = wrd1 + so, wn0 = wrd0 + sx, wn1 = wrd1 + sx; \
        _Pragma("unroll") for (int i = 0; i < 16; ++i) { \
            if (i == 12 && !(LAST)) { \
                if ((EXTRA) == 2 && wid == 0) poll_flags(flags, lane < 8 ? FL_F1 + h * 8 + lane : -1, tag); \
                if ((MODE) == 0) wait_bar<6>(); else wait_bar<4>(); \
                if ((EXTRA) == 1 && tid == 0) { flag_store(flags + FL_FW + (b & 7) * 32 + (b >> 3), tag); if (b < 160) flag_store(flags + FL_F4 + b, tag); else flag_store(flags + FL_F1 + h * 8 + r, tag); } \
                SBAR(); } \
            acc[i & 3] = MFMA32(wf[i & 3], xf[(i >> 2) & 1], acc[i & 3]); PIN(acc[i & 3]); \
            if (i + 4 < 16) wf[i & 3] = WFR(wb0, wb1, i + 4); else if (!(LAST)) wf[i & 3] = WFR(wn0, wn1, i - 12); \
            if ((i & 3) == 0 && i + 4 < 16) xf[((i >> 2) + 1) & 1] = lds_frag(xrd[(i >> 2) + 1] + so); \
            if (i == 12 && !(LAST)) xf[0] = lds_frag(xrd[0] + sx); \
            if ((MODE) == 0) { \
                if (i == 0) { SETM0(xl_, 0); } if (i == 1) { GLDS(xvoff, xp, 0); SETM0(xl_, 1024); } \
                if (i == 3) { GLDS(xvoff2, xp, 0); SETM0(xl_, 2048); } if (i == 5) { GLDS(xvoff, xp2, 0); SETM0(xl_, 3072); } \
                if (i == 7) { GLDS(xvoff2, xp2, 0); SETM0(wl_, 0); } if (i == 9) { GLDS(wvoff, wp, 0); SETM0(wl_, 1024); } \
                if (i == 11) { GLDS(wvoff2, wp, 0); } \
            } else { \
                const char* ku_ = Kw + (size_t)(2 * ((MODE) - 1)) * 16384; \
                if (i == 0) { SETM0(kl0, (2 * ((MODE) - 1)) * 16384); } if (i == 1) GLDSC(tvoff, ku_, 0); if (i == 4) GLDSC(tvoff, ku_, 1024); \
                if (i == 6) { SETM0(kl0, (2 * ((MODE) - 1) + 1) * 16384); } if (i == 8) GLDSC(tvoff, ku_ + 16384, 0); if (i == 11) GLDSC(tvoff, ku_ + 16384, 1024); \
            } \
            SBAR(); \
        } \
        so = sx; xp += 128; wp += (size_t)64 * 8192; } while (0)
    wait_bar<6>();
    xf[0] = lds_frag(xrd[0] + so);
#pragma unroll
    for (int i = 0; i < 4; ++i) wf[i] = WFR(wrd0 + so, wrd1 + so, i);
    SBAR();
    P2CHUNK(0, 0, false);
    P2CHUNK(0, 1, false);
#pragma unroll 1
    for (int c = 2; c < 7; ++c) P2CHUNK(0, 0, false);
    P2CHUNK(0, 2, false);
    P2CHUNK(1, 0, false);
    P2CHUNK(2, 0, true);
#undef P2CHUNK
    wait_bar<63>();
    lds_ptr qw = (lds_ptr)lds + (par ? SPARE : QA_OFF) + rg * 8192 + lane * 16;
#pragma unroll
    for (int db = 0; db < 4; ++db) {
#pragma unroll
        for (int e = 0; e < 16; ++e) acc[db][e] *= QSCALE;
        *(LAS u32x4*)(qw + (2 * db) * 1024) = pack8(acc[db], 0);
        *(LAS u32x4*)(qw + (2 * db + 1) * 1024) = pack8(acc[db], 1);
    }
#undef WFR
}

DI void p3_body(rsrc_t wr, const char* ws, int h, int blkA, int blkB, char* lds, int wid, int lane, int tid, GAS u64* flags, u64 tag) {
    const unsigned lds0 = (unsigned)(uintptr_t)lds;
    const int r32 = lane & 31, hi = lane >> 5, rg = wid & 3, par = wid >> 2;
    const unsigned tvoff = lane * 16;
    const unsigned kl0 = lds0 + KRING + wid * 2048, vl0 = lds0 + VRING + wid * 2048;
    const char* Kw = ws + WS_K + (size_t)h * 64 * 8192 + wid * 2048; const char* Vw = ws + WS_V + (size_t)h * 64 * 8192 + wid * 2048;
    const lds_cptr krd = (lds_cptr)lds + KRING + par * 8192 + lane * 16;
    const lds_cptr vrd = (lds_cptr)lds + VRING + par * 8192 + lane * 16;
    int g0 = 0;
    bf16x8 qf[8];
#define VF(i) ((((i) & 3) * 2 + ((i) >> 2)) * 1024)
#define MFMA_C(D, A, B, C) asm volatile("v_mfma_f32_32x32x16_bf16 %0, %2, %3, %1" : "=&v"(D), "+v"(C) : "v"(A), "v"(B))
#define SLOT(X, Y, PN, PP, ODD, QK, SM, PV, FIRST, DM, PFK, MASK) do { \
        unsigned long long fire = 0; \
        _Pragma("unroll") for (int i = 0; i < 16; ++i) { \
            if (i < 8 && QK && !(i & 1)) { LGKM(2); SBAR(); } \
            if (i >= 8 && PV && !(i & 1)) { LGKM(2); SBAR(); } \
            if (i < 8) { if (QK) { if (i == 0) { if (FIRST) X = MFMA32(kf[0], qf[0], zero16); else MFMA_C(X, kf[0], qf[0], negm); } else X = MFMA32(kf[i & 3], qf[i], X); PIN(X); \
                                   if (i < 4) kf[i] = lds_frag(kp + ((ODD) ? 0 : 16384) + (i + 4) * 1024); } \
                         if (PV && i >= 4) vf[i - 4] = lds_frag(vp + ((ODD) ? 0 : 16384) + VF(i - 4)); } \
            else { if (PV) { o[i & 3] = MFMA32(vf[i & 3], __builtin_bit_cast(bf16x8, PP[(i - 8) >> 2]), o[i & 3]); PIN(o[i & 3]); if (i < 12) vf[i & 3] = lds_frag(vp + ((ODD) ? 0 : 16384) + VF(i - 4)); } \
                   if (PFK && i >= 12) kf[i - 12] = lds_frag(kp + ((ODD) ? 16384 : 0) + (i - 12) * 1024); } \
            if (!(ODD) && i == 5) { kp = (lds_cptr)(uintptr_t)((unsigned)(uintptr_t)kp ^ 0x8000u); PIN(kp); } \
            if (!(ODD) && i == 13) { vp = (lds_cptr)(uintptr_t)((unsigned)(uintptr_t)vp ^ 0x8000u); PIN(vp); } \
            if (DM) { \
                if (i == 0) SETM0(dk, (ODD) ? 16384 : 0); \
                if (i == 1) GLDSC(uoff, Kw, 0); \
                if (i == 3) GLDSC(uoff, Kw, 1024); \
                if ((DM) & 2) { \
                    if (i == 4) SETM0(dv, (ODD) ? 16384 : 0); \
                    if (i == 5) GLDSC(uoffv, Vw, 0); \
                    if (i == 7) GLDSC(uoffv, Vw, 1024); \
                    if (i == 11) { uoffv += 16384u; PIN(uoffv); } } \
                if (i == 8) { uoff += (j + 5 == n) ? 16384u * (unsigned)(1 - n) : 16384u; PIN(uoff); } \
                if ((ODD) && i == 9) { dk ^= 0x8000u; dv ^= 0x8000u; PINS(dk); PINS(dv); } } \
            if (SM) { Y[i] = EXP2(Y[i]); \
                if (i > 0) { if (i & 1) l += Y[i - 1]; else l2 += Y[i - 1]; PIN(l); PIN(l2); } \
                if (i >= 3 && (i & 1)) { PN[(i - 3) >> 3][((i - 3) >> 1) & 3] = cvtpk(Y[i - 3], Y[i - 2]); PIN(PN[(i - 3) >> 3]); } \
                if (i == 15) { PN[1][3] = cvtpk(Y[14], Y[15]); l2 += Y[15]; PIN(l2); PIN(PN[1]); } else PIN(Y); } \
            if (QK) { \
                if (MASK && i == 9 && (MASK == 1 || j + 1 >= n - 2)) { int qq = qrel - 64 * (j + 1 - (n - 2)); PIN(qq); \
                    _Pragma("unroll") for (int r = 0; r < 16; ++r) { const int c = (r & 3) + 8 * (r >> 2); if (c > qq) X[r] = -INFINITY; } } \
                if (i == 9) { mxa = MX3(X[0], X[1], X[2]); PIN(mxa); } \
                if (i == 10) { mxb = MX3(X[3], X[4], X[5]); mxa = MX3(mxa, X[6], X[7]); PIN(mxb); PIN(mxa); } \
                if (i == 12) { mxb = MX3(mxb, X[8], X[9]); mxa = MX3(mxa, X[10], X[11]); PIN(mxb); PIN(mxa); } \
                if (i == 14) { mxb = MX3(mxb, X[12], X[13]); mxa = MX3(mxa, X[14], X[15]); PIN(mxb); PIN(mxa); } \
                if (i == 15) { mxa = MX3(mxa, mxb, X[0]); if (!(FIRST)) { fire = __ballot(mxa > THR); PINS(fire); } } } \
            SBAR(); \
        } \
        if (__builtin_expect((fire | pend) != 0, 0) || FIRST) { \
            if (PV && pend) { _Pragma("unroll") for (int d = 0; d < 4; ++d) _Pragma("unroll") for (int r = 0; r < 16; ++r) o[d][r] *= alp; } \
            pend = 0; \
            if (QK && (FIRST || fire)) { \
                auto rr = __builtin_amdgcn_permlane32_swap(__float_as_uint(mxa), __float_as_uint(mxa), false, false); \
                const float rm = __builtin_fmaxf(__uint_as_float(rr[0]), __uint_as_float(rr[1])); \
                if (FIRST) { const float dl = __builtin_fmaxf(rm, -1e30f); m = dl; \
                    _Pragma("unroll") for (int r = 0; r < 16; ++r) { X[r] -= dl; negm[r] = -dl; } PIN(negm); } \
                else { const float dl = __builtin_fmaxf(rm, 0.f); m += dl; alp = EXP2(-dl); l *= alp; l2 *= alp; pend = 1; \
                    const float ndl = -dl, aone = hi ? 0.f : 1.f; \
                    asm volatile("s_nop 1\n\tv_mfma_f32_32x32x2_f32 %0, %2, %3, %0\n\tv_mfma_f32_32x32x2_f32 %1, %2, %3, %1\n\ts_nop 7\n\ts_nop 7\n\ts_nop 7" : "+v"(X), "+v"(negm) : "v"(aone), "v"(ndl)); } } } \
        SBAR(); \
        } while (0)
#define TBAR() wait_bar_v<4>()
    const f32x16 zero16 = {};
#pragma unroll 1
    for (int pass = 0; pass < 2; ++pass) {
        const int blk = pass ? blkB : blkA, n = 2 * (blk + 1);
        dma2c(vl0 + (g0 & 3) * 16384, tvoff, Vw); dma2c(vl0 + ((g0 + 1) & 3) * 16384, tvoff, Vw + 16384);
        if (pass == 0) wait_bar<4>();
        if (pass == 0) {
            const lds_cptr qr = (lds_cptr)lds + QA_OFF + rg * 8192 + lane * 16;
#pragma unroll
            for (int s = 0; s < 8; ++s) qf[s] = lds_frag(qr + s * 1024);
        }
        float m = -1e30f, l = 0.f, l2 = 0.f, alp = 1.f, mxa = 0.f, mxb = 0.f; unsigned long long pend = 0; f32x16 o[4] = {};
        u32x4 pk0[2] = {}, pk1[2] = {};
        f32x16 s0, s1, negm;
        bf16x8 kf[4], vf[4] = {};
        const int qrel = 32 * rg + r32 - 32 * par - 4 * hi;
        lds_cptr kp = krd + (g0 & 2) * 16384, vp = vrd + ((g0 & 2) ^ 2) * 16384;
        unsigned dk = kl0 + (g0 & 2) * 16384, dv = vl0 + ((g0 + 2) & 2) * 16384;
        unsigned uoffv = tvoff + 2 * 16384;
        unsigned uoff = tvoff + ((4 < n) ? 4 : 4 - n) * 16384;
#pragma unroll
        for (int i = 0; i < 4; ++i) kf[i] = lds_frag(kp + i * 1024);
        int j = -1;
        SLOT(s0, s1, pk1, pk0, true, true, false, false, true, false, true, 2);
        wait_bar_v<63>();
        j = 0;
        SLOT(s1, s0, pk0, pk1, false, true, true, false, false, 3, true, 2);
        TBAR();
        if (pass == 1 && tid == 0) flag_store(flags + FL_F3 + blkA * 32 + h, tag);
#pragma unroll 1
        for (j = 1; j < n - 3; j += 2) {
            SLOT(s0, s1, pk1, pk0, true, true, true, true, false, 3, true, 0);
            TBAR();
            ++j;
            SLOT(s1, s0, pk0, pk1, false, true, true, true, false, 3, true, 0);
            TBAR();
            --j;
        }
        if (n >= 4) {
            j = n - 3;
            SLOT(s0, s1, pk1, pk0, true, true, true, true, false, 3, true, 1);
            TBAR();
            j = n - 2;
            SLOT(s1, s0, pk0, pk1, false, true, true, true, false, 1, true, 1);
            wait_bar_v<2>();
        }
        j = n - 1;
        SLOT(s0, s1, pk1, pk0, true, false, true, true, false, 1, false, 0);
        wait_bar_v<2>();
        if (pass == 0) {
            const lds_cptr qr = (lds_cptr)lds + SPARE + rg * 8192 + lane * 16;
#pragma unroll
            for (int s = 0; s < 8; ++s) qf[s] = lds_frag(qr + s * 1024);
        }
        j = n;
        SLOT(s1, s0, pk0, pk1, false, false, false, true, false, false, false, 0);
        g0 += n;
        STAMP(4 + 2 * pass);
        {
            l += l2;
            auto rr = __builtin_amdgcn_permlane32_swap(__float_as_uint(l), __float_as_uint(l), false, false);
            l = __uint_as_float(rr[0]) + __uint_as_float(rr[1]);
            wait_bar<63>();
            LAS float* ml = (LAS float*)((lds_ptr)lds + SPARE);
            *(LAS f32x2*)(ml + (wid * 64 + lane) * 2) = (f32x2){m, l};
            lds_ptr sendp = (lds_ptr)lds + VRING + par * 32768 + rg * 8192 + lane * 16;
            lds_cptr recvp = (lds_cptr)lds + VRING + (par ^ 1) * 32768 + rg * 8192 + lane * 16;
            f32x16 keep[2];
            if (par) {
#pragma unroll
                for (int d = 0; d < 2; ++d) { keep[d] = o[2 + d];
#pragma unroll
                    for (int g = 0; g < 4; ++g) *(LAS f32x4*)(sendp + (d * 4 + g) * 1024) = (f32x4){o[d][4 * g], o[d][4 * g + 1], o[d][4 * g + 2], o[d][4 * g + 3]}; }
            } else {
#pragma unroll
                for (int d = 0; d < 2; ++d) { keep[d] = o[d];
#pragma unroll
                    for (int g = 0; g < 4; ++g) *(LAS f32x4*)(sendp + (d * 4 + g) * 1024) = (f32x4){o[2 + d][4 * g], o[2 + d][4 * g + 1], o[2 + d][4 * g + 2], o[2 + d][4 * g + 3]}; }
            }
            wait_bar<63>();
            const f32x2 mlo = *(const LAS f32x2*)(ml + ((wid ^ 4) * 64 + lane) * 2);
            const float mm = __builtin_fmaxf(m, mlo[0]), fo = EXP2(m - mm), fx = EXP2(mlo[0] - mm);
            const float inv = __builtin_amdgcn_rcpf(l * fo + mlo[1] * fx), sc = fo * inv, sx = fx * inv;
            const unsigned cdst = (unsigned)(WS_CTX + ((size_t)(blk * 4 + rg) * 32 + h) * 8192 + (par * 4) * 1024) + lane * 16;
#pragma unroll
            for (int d = 0; d < 2; ++d) {
#pragma unroll
                for (int g = 0; g < 4; ++g) {
                    const f32x4 v4 = *(const LAS f32x4*)(recvp + (d * 4 + g) * 1024);
#pragma unroll
                    for (int e = 0; e < 4; ++e) keep[d][4 * g + e] = keep[d][4 * g + e] * sc + v4[e] * sx;
                }
                st16(wr, cdst + (2 * d) * 1024, pack8(keep[d], 0));
                st16(wr, cdst + (2 * d + 1) * 1024, pack8(keep[d], 1));
            }
            if (pass == 0) wait_bar<63>();
            else signal_flag(flags + FL_F3 + blk * 32 + h, tag, tid);
        }
        STAMP(5 + 2 * pass);
    }
#undef SLOT
#undef TBAR
#undef VF
}

constexpr int CSTR = 4160;
constexpr int CCH = 4 * CSTR + 4608;
DI void p4_body(const Args& a, rsrc_t wr, int b, int h, int r, char* lds, int wid, int lane, int tid, GAS u64* flags, u64 tag) {
    const int ch = wid >> 1, c = 4 * b + ch, half = wid & 1;
    lds_ptr base = (lds_ptr)lds + ch * CCH;
    const lds_cptr ur = (lds_cptr)lds + STG_UR + ch * 4096, hs = (lds_cptr)lds + STG_H + ch * 4096;
    u32x4 w0[2], w1[2], hv[2];
#pragma unroll
    for (int it = 0; it < 2; ++it) { const int y8 = half * 128 + it * 64 + lane;
        w0[it] = *(const LAS u32x4*)(ur + 16 * y8); w1[it] = *(const LAS u32x4*)(ur + 16 * (y8 < 255 ? y8 + 1 : y8)); if (y8 == 255) w1[it] = (u32x4){0u, 0u, 0u, 0u};
        hv[it] = *(const LAS u32x4*)(hs + 16 * y8); }
    wait_bar<63>();
    const float Dc = bf2f(a.ssmD[c]);
#pragma unroll
    for (int it = 0; it < 2; ++it) {
        const int y8 = half * 128 + it * 64 + lane;
        unsigned w[6] = {w0[it][0], w0[it][1], w0[it][2], w0[it][3], w1[it][0], w1[it][1]}, o[5];
#pragma unroll
        for (int e = 0; e < 5; ++e) o[e] = __builtin_amdgcn_alignbit(w[e + 1], w[e], 16);
#pragma unroll
        for (int sg = 0; sg < 4; ++sg) { const int e = sg >> 1; u32x4 v;
            if (sg & 1) v = (u32x4){o[e], o[e + 1], o[e + 2], o[e + 3]}; else v = (u32x4){w[e], w[e + 1], w[e + 2], w[e + 3]};
            *(LAS u32x4*)(base + sg * CSTR + y8 * 16) = v; }
        *(LAS u32x4*)(base + 4 * CSTR + 512 + y8 * 16) = hv[it];
    }
    if (half == 1 && lane < 8) { const int sg = lane >> 1, y8 = 256 + (lane & 1); *(LAS u32x4*)(base + sg * CSTR + y8 * 16) = (u32x4){0u, 0u, 0u, 0u}; }
    if (half == 0 && lane < 32) *(LAS u32x4*)(base + 4 * CSTR + lane * 16) = (u32x4){0u, 0u, 0u, 0u};
    wait_bar<63>();
    const int j = lane & 15, g = lane >> 4;
    const lds_cptr ard = (lds_cptr)base + ((3 - j) & 3) * CSTR + (2031 - j - ((3 - j) & 3) + 8 * g) * 2;
    const lds_cptr brd = (lds_cptr)base + 4 * CSTR + (240 + 16 * j + 8 * g) * 2;
    f32x4 acc[8] = {};
#define P4LOAD(A, B, rho) do { _Pragma("unroll") for (int u = 0; u < 8; ++u) { const lds_cptr pa = ard - ((rho) + 8 * u) * 64; const s16x4 lo = *(const LAS s16x4*)pa, hi2 = *(const LAS s16x4*)(pa + 8); \
            A[u] = __builtin_shufflevector(lo, hi2, 0, 1, 2, 3, 4, 5, 6, 7); B[u] = lds_frag(brd + (8 * u - (rho)) * 64); } } while (0)
#define P4MM(A, B) do { _Pragma("unroll") for (int u = 0; u < 8; ++u) _Pragma("unroll") for (int v = 0; v + u < 8; ++v) acc[u + v] = MFMA16(A[u], B[v], acc[u + v]); } while (0)
    {
        bf16x8 A0[8], B0[8], A1[8], B1[8];
        P4LOAD(A0, B0, half); P4LOAD(A1, B1, 2 + half); SBAR();
        P4MM(A0, B0); SBAR();
        P4LOAD(A0, B0, 4 + half); SBAR();
        P4MM(A1, B1); SBAR();
        P4LOAD(A1, B1, 6 + half); SBAR();
        P4MM(A0, B0); SBAR();
        P4MM(A1, B1);
    }
#undef P4LOAD
#undef P4MM
    wait_bar<0>();
    if (tid == 0) flag_store(flags + FL_F1 + h * 8 + r, tag);
    lds_ptr ex = base + CSTR;
    f32x4 keep[4];
    if (half) {
#pragma unroll
        for (int q = 0; q < 4; ++q) { *(LAS f32x4*)(ex + 4096 + q * 1024 + lane * 16) = acc[q]; keep[q] = acc[4 + q]; }
    } else {
#pragma unroll
        for (int q = 0; q < 4; ++q) { *(LAS f32x4*)(ex + q * 1024 + lane * 16) = acc[4 + q]; keep[q] = acc[q]; }
    }
    wait_bar<63>();
    u32x2 zp[4];
#pragma unroll
    for (int q = 0; q < 4; ++q) {
        const f32x4 o = *(const LAS f32x4*)(ex + (half ^ 1) * 4096 + q * 1024 + lane * 16);
        const f32x4 y = keep[q] + o;
        const int t = 256 * (half * 4 + q) + 16 * j + 4 * g;
        const s16x4 kvr = *(const LAS s16x4*)(base + (2047 - t - 3) * 2);
        float z[4];
#pragma unroll
        for (int r = 0; r < 4; ++r) { const float kv = bf2f((u16)kvr[3 - r]); z[r] = y[r] + kv * Dc; }
        zp[q] = (u32x2){cvtpk(z[0], z[1]), cvtpk(z[2], z[3])};
    }
#pragma unroll
    for (int q = 0; q < 4; q += 2) {
        auto s0 = __builtin_amdgcn_permlane16_swap(zp[q][0], zp[q + 1][0], false, false);
        auto s1 = __builtin_amdgcn_permlane16_swap(zp[q][1], zp[q + 1][1], false, false);
        const int t = 256 * (half * 4 + q + (g & 1)) + 16 * j + 8 * (g >> 1);
        st16(wr, (unsigned)(WS_Z + ((size_t)c * S + t) * 2), (u32x4){s0[0], s1[0], s0[1], s1[1]});
    }
}

constexpr int OCH = 36864;
DI void p5_body(const Args& a, rsrc_t wr, int mt, int nt, char* lds, int wid, int lane, int tid) {
    const unsigned lds0 = (unsigned)(uintptr_t)lds;
    const int rh = wid & 1, kq = wid >> 1;
    const char* cg = a.ws + WS_CTX + (size_t)(2 * mt + (wid >> 2)) * 32 * 8192 + (wid & 3) * 2048;
    const unsigned cl = lds0 + (wid >> 2) * 8192 + (wid & 3) * 2048;
    const unsigned tv = lane * 16;
    const char* wg = a.ws + WS_WO + (size_t)nt * 32 * 20480 + wid * 2048;
    const char* wg2 = a.ws + WS_WO + (size_t)nt * 32 * 20480 + 16384 + (wid & 3) * 1024;
    const unsigned wl = lds0 + 16384 + wid * 2048, wl2 = lds0 + 32768 + (wid & 3) * 1024;
    const int j = lane & 15, g = lane >> 4;
    const lds_cptr ard = (lds_cptr)lds + rh * 8192 + kq * 2048 + (g >> 1) * 1024 + ((g & 1) * 32 + j) * 16;
    const lds_cptr brd = (lds_cptr)lds + 16384 + kq * 5120 + lane * 16;
    f32x4 acc[2][5] = {};
    unsigned zr[5], xr5[5];
    const int erow = tid >> 3, ec0 = (tid & 7) * 10, et = mt * 64 + erow;
#pragma unroll
    for (int it = 0; it < 5; ++it) { const int e = tid + it * 512, cl2 = e >> 5, d = e & 31;
        zr[it] = ld4c(wr, (unsigned)(WS_Z + ((size_t)(nt * 80 + cl2) * S + mt * 64 + 2 * d) * 2));
        xr5[it] = *(const unsigned*)(a.x + (size_t)et * M + nt * 80 + ec0 + 2 * it); }
    const float gt = __uint_as_float(ld4c(wr, (unsigned)(WS_G + et * 4)));
    const int rot = 2 * (mt & 3);
#define P5MM() do { _Pragma("unroll") for (int cb = 0; cb < 5; ++cb) { \
                acc[0][cb] = MFMA16(wf[cb], a0, acc[0][cb]); PIN(acc[0][cb]); \
                SBAR(); \
                acc[1][cb] = MFMA16(wf[cb], a1, acc[1][cb]); PIN(acc[1][cb]); \
                SBAR(); } } while (0)
#define P5LOOP(FIVE) do { \
        _Pragma("unroll") for (int h = 0; h < 3; ++h) { const int hh = (h + rot) & 31; dma2c(cl + h * OCH, tv, cg + (size_t)hh * 8192); dma2c(wl + h * OCH, tv, wg + (size_t)hh * 20480); if (FIVE) dma1c(wl2 + h * OCH, tv, wg2 + (size_t)hh * 20480); } \
        unsigned so = 0; \
        _Pragma("unroll 1") for (int h = 0; h < 32; ++h) { \
            wait_bar<(FIVE) ? 10 : 8>(); \
            const lds_cptr ab = ard + so, bb = brd + so; \
            const unsigned sn = so == 0 ? 3 * OCH : so - OCH; \
            const int hn = ((h < 28 ? h + 3 : 31) + rot) & 31; \
            const char* cgn = cg + (size_t)hn * 8192; const char* wgn = wg + (size_t)hn * 20480; const char* wg2n = wg2 + (size_t)hn * 20480; \
            const bf16x8 a0 = lds_frag(ab), a1 = lds_frag(ab + 256); \
            bf16x8 wf[5]; \
            _Pragma("unroll") for (int cb = 0; cb < 5; ++cb) wf[cb] = lds_frag(bb + cb * 1024); \
            SBAR(); \
            if (FIVE) { dma2c(cl + sn, tv, cgn); dma2c(wl + sn, tv, wgn); dma1c(wl2 + sn, tv, wg2n); SBAR(); P5MM(); } \
            else { P5MM(); dma2c(cl + sn, tv, cgn); dma2c(wl + sn, tv, wgn); SBAR(); } \
            so = so == 3 * OCH ? 0 : so + OCH; \
        } } while (0)
    if (wid < 4) P5LOOP(true); else P5LOOP(false);
#undef P5MM
#undef P5LOOP
    wait_bar<0>();
    constexpr int PSTR = 84 * 4;
    {
        lds_ptr pb = (lds_ptr)lds + kq * 64 * PSTR;
#pragma unroll
        for (int rb2 = 0; rb2 < 2; ++rb2)
#pragma unroll
            for (int cb = 0; cb < 5; ++cb) *(LAS f32x4*)(pb + (32 * rh + 16 * rb2 + j) * PSTR + (16 * cb + 4 * g) * 4) = acc[rb2][cb];
        lds_ptr zb = (lds_ptr)lds + 4 * 64 * PSTR;
#pragma unroll
        for (int it = 0; it < 5; ++it) { const int e = tid + it * 512, cl2 = e >> 5, d = e & 31; *(LAS unsigned*)(zb + cl2 * 132 + d * 4) = zr[it]; }
    }
    wait_bar<63>();
    {
        const float g1 = 1.f - gt;
        const lds_cptr pb = (lds_cptr)lds + erow * PSTR + ec0 * 4;
        const lds_cptr zb = (lds_cptr)lds + 4 * 64 * PSTR + erow * 2;
        u16* orow = a.out + (size_t)et * M + nt * 80 + ec0;
#pragma unroll
        for (int e = 0; e < 10; e += 2) {
            float r2[2]; const unsigned xv = xr5[e >> 1];
#pragma unroll
            for (int k = 0; k < 2; ++k) {
                const int cc = e + k;
                const float av = *(const LAS float*)(pb + cc * 4) + *(const LAS float*)(pb + 64 * PSTR + cc * 4) + *(const LAS float*)(pb + 128 * PSTR + cc * 4) + *(const LAS float*)(pb + 192 * PSTR + cc * 4);
                const float z = bf2f(*(const LAS u16*)(zb + (ec0 + cc) * 132));
                r2[k] = gt * av + g1 * (bf2f((u16)(k ? xv >> 16 : xv & 0xffff)) * z);
            }
            *(unsigned*)(orow + e) = cvtpk(r2[0], r2[1]);
        }
    }
}

__global__ __launch_bounds__(512, 2) void k_fused(const u16* x, const u16* ckv, const u16* wq, const u16* wuk, const u16* wuv, const u16* wo, const u16* bk, const u16* bv,
                                                  const u16* shk, const u16* ssmk, const u16* ssmD, const u16* gw, u16* out, char* ws) {
    extern __shared__ __attribute__((aligned(16))) char lds[];
    const Args a = {x, ckv, wq, wuk, wuv, wo, bk, bv, shk, ssmk, ssmD, gw, out, ws};
    const int tid = threadIdx.x, lane = tid & 63, wid = rfl(tid >> 6);
    const int b = blockIdx.x, xcd = b & 7, idx = b >> 3, h = xcd * 4 + (idx >> 3), r = idx & 7;
    const unsigned lds0 = (unsigned)(uintptr_t)lds;
    const rsrc_t wr = __builtin_amdgcn_make_buffer_rsrc(ws, 0, (int)WS_END, 0x00020000);
    GAS u64* flags = (GAS u64*)(ws + WS_F);
    STAMP(0);
    const P1Ctx c1 = p1_setup(a, h, r, lds0, wid, lane);
    const u64 tag = *(const __attribute__((address_space(4))) u64*)(ws + WS_F) + 1;
    if (b >= 160) {
        u32x4 xg[3][2];
        const u32x4 g0 = *(const u32x4*)(gw + lane * 8), g1 = *(const u32x4*)(gw + 512 + (lane & 15) * 8);
#pragma unroll
        for (int k = 0; k < 3; ++k) { int jb = b - 160 + 96 * k; if (jb >= 256) jb = b - 160; const u16* xr = x + (size_t)(64 * (jb >> 3) + 8 * (jb & 7) + wid) * M;
            xg[k][0] = *(const u32x4*)(xr + lane * 8); xg[k][1] = *(const u32x4*)(xr + 512 + (lane & 15) * 8); }
        p0a_load(a, b - 160, lds0, wid, lane);
        p1_prime_t(c1);
        STAMP(11);
        wait_bar<4>();
#pragma unroll
        for (int k = 0; k < 3; ++k) { PIN(xg[k][0]); PIN(xg[k][1]); }
        STAMP(12);
        p0a_compute(b - 160, lds, tid);
        wait_bar<63>();
        STAMP(13);
        p1_prime_w(c1);
        p0a_store(wr, b - 160, lds, tid);
#pragma unroll
        for (int k = 0; k < 3; ++k) { const int jb = b - 160 + 96 * k; float sum = 0.f, s1 = 0.f;
#pragma unroll
            for (int e = 0; e < 4; ++e) { sum += blo(xg[k][0][e]) * blo(g0[e]) + bhi(xg[k][0][e]) * bhi(g0[e]); s1 += blo(xg[k][1][e]) * blo(g1[e]) + bhi(xg[k][1][e]) * bhi(g1[e]); }
            if (lane < 16) sum += s1;
            sum = wave_sum(sum);
            if (lane == 0 && jb < 256) st4(wr, (unsigned)(WS_G + (64 * (jb >> 3) + 8 * (jb & 7) + wid) * 4), __float_as_uint(1.f / (1.f + __expf(-sum)))); }
    } else { p1_prime_t(c1); p1_prime_w(c1); }
    STAMP(1);
    if (wid < 4) p1_body<0>(a, c1, wr, b, h, r, lds, wid, lane, tid, flags, tag); else p1_body<1>(a, c1, wr, b, h, r, lds, wid, lane, tid, flags, tag);
    STAMP(2);
    wait_bar<63>();
    p0b_finish(wr, b, lds, wid, lane);
    STAMP(14);
    if (b < 160) p4_body(a, wr, b, h, r, lds, wid, lane, tid, flags, tag);
    wait_bar<63>();
    STAMP(8);
    p2_body(a, b, h, r, 15 - r, r, lds, wid, lane, tid, flags, tag);
    STAMP(3);
    p3_body(wr, ws, h, 15 - r, r, lds, wid, lane, tid, flags, tag);
    {
        const int mt = (xcd >> 1) * 8 + (idx >> 2), nt = (xcd & 1) * 4 + (idx & 3);
        if (wid == 0) {
            poll_flags(flags, lane < 20 ? FL_F4 + nt * 20 + lane : (lane < 28 ? FL_F0 + (lane - 20) * 32 + mt : -1), tag);
            poll_flags(flags, lane < 32 ? FL_F3 + (mt >> 1) * 32 + lane : FL_FW + nt * 32 + (lane - 32), tag);
        }
        wait_bar<0>();
        STAMP(9);
        p5_body(a, wr, mt, nt, lds, wid, lane, tid);
    }
    if (b == 0 && tid == 0) flag_store(flags, tag);
    STAMP(10);
}

extern "C" void kernel_launch(void* const* d_in, const int* in_sizes, int n_in,
                              void* d_out, int out_size, void* d_ws, size_t ws_size,
                              hipStream_t stream) {
    static bool attr = false;
    if (!attr) { (void)hipFuncSetAttribute((const void*)k_fused, hipFuncAttributeMaxDynamicSharedMemorySize, LDS_BYTES); attr = true; }
    k_fused<<<256, 512, LDS_BYTES, stream>>>((const u16*)d_in[0], (const u16*)d_in[1], (const u16*)d_in[2], (const u16*)d_in[3], (const u16*)d_in[4], (const u16*)d_in[5],
                                             (const u16*)d_in[7], (const u16*)d_in[8], (const u16*)d_in[9], (const u16*)d_in[10], (const u16*)d_in[11], (const u16*)d_in[12],
                                             (u16*)d_out, (char*)d_ws);
}
```

```cpp
#include <hip/hip_runtime.h>
#include <hip/hip_bf16.h>
#include <stdint.h>

typedef unsigned short u16;
typedef unsigned long long u64;
typedef short bf16x8 __attribute__((ext_vector_type(8)));
typedef short s16x4 __attribute__((ext_vector_type(4)));
typedef float f32x16 __attribute__((ext_vector_type(16)));
typedef float f32x4 __attribute__((ext_vector_type(4)));
typedef float f32x2 __attribute__((ext_vector_type(2)));
typedef __bf16 bf16x2_t __attribute__((ext_vector_type(2)));
typedef unsigned u32x4 __attribute__((ext_vector_type(4)));
typedef unsigned u32x2 __attribute__((ext_vector_type(2)));
typedef short v4i16_t __attribute__((ext_vector_type(4)));
#define LAS __attribute__((address_space(3)))
#define GAS __attribute__((address_space(1)))
typedef LAS const char* lds_cptr;
typedef LAS char* lds_ptr;
#define DI __device__ __forceinline__
#define SBAR() __builtin_amdgcn_sched_barrier(0)
#define PIN(x) asm volatile("" : "+v"(x))
#define PINS(x) asm volatile("" : "+s"(x))
#define MFMA32(a, b, c) __builtin_amdgcn_mfma_f32_32x32x16_bf16(a, b, c, 0, 0, 0)
#define MFMA16(a, b, c) __builtin_amdgcn_mfma_f32_16x16x32_bf16(a, b, c, 0, 0, 0)
#define LGKM(n) __builtin_amdgcn_s_waitcnt(0xC07F | ((n) << 8))
#define EXP2(v) __builtin_amdgcn_exp2f(v)

constexpr int S = 2048, H = 32, D = 128, M = 640, DC = 512, HD = H * D;
constexpr int LDS_BYTES = 163840;
constexpr size_t WS_K = 0, WS_V = (size_t)S * HD * 2, WS_CTX = 2 * WS_V, WS_UR = 3 * WS_V, WS_Z = WS_UR + (size_t)S * M * 2, WS_WO = WS_Z + (size_t)S * M * 2,
                 WS_G = WS_WO + (size_t)HD * M * 2, WS_F = WS_G + 8192, WS_SL = WS_F + 32768, WS_END = WS_SL + (size_t)64 * 16 * 20480;
constexpr int FL_F0 = 16, FL_F1 = FL_F0 + 256, FL_F3 = FL_F1 + 256, FL_F4 = FL_F3 + 512, FL_FW = FL_F4 + 160, FL_F5 = FL_FW + 256;

#define STAMP(i)

DI float bf2f(u16 b) { return __uint_as_float(((unsigned)b) << 16); }
DI unsigned cvtpk(float lo, float hi) { f32x2 v = {lo, hi}; bf16x2_t b = __builtin_convertvector(v, bf16x2_t); return __builtin_bit_cast(unsigned, b); }
DI u16 f2bf(float f) { return (u16)cvtpk(f, f); }
DI float rb(float f) { return __uint_as_float(cvtpk(f, f) << 16); }
DI unsigned rfl(unsigned x) { return __builtin_amdgcn_readfirstlane(x); }

#define SETM0(base, imm) asm volatile("s_add_u32 m0, %0, %1" :: "s"(base), "i"(imm) : "scc")
#define GLDS(voff, sbase, imm) asm volatile("global_load_lds_dwordx4 %0, %1 offset:%c2" :: "v"(voff), "s"(sbase), "i"(imm) : "memory")
#define GLDSC(voff, sbase, imm) asm volatile("global_load_lds_dwordx4 %0, %1 offset:%c2 sc1" :: "v"(voff), "s"(sbase), "i"(imm) : "memory")
DI void dma1(unsigned ldsaddr, unsigned voff, const void* sbase) {
    asm volatile("s_mov_b32 m0, %0\n\ts_nop 0\n\tglobal_load_lds_dwordx4 %1, %2" :: "s"(ldsaddr), "v"(voff), "s"(sbase) : "memory");
}
DI void dma2(unsigned ldsaddr, unsigned voff, const void* sbase) {
    asm volatile("s_mov_b32 m0, %0\n\ts_nop 0\n\tglobal_load_lds_dwordx4 %1, %2\n\tglobal_load_lds_dwordx4 %1, %2 offset:1024" :: "s"(ldsaddr), "v"(voff), "s"(sbase) : "memory");
}
DI void dma1c(unsigned ldsaddr, unsigned voff, const void* sbase) {
    asm volatile("s_mov_b32 m0, %0\n\ts_nop 0\n\tglobal_load_lds_dwordx4 %1, %2 sc1" :: "s"(ldsaddr), "v"(voff), "s"(sbase) : "memory");
}
DI void dma2c(unsigned ldsaddr, unsigned voff, const void* sbase) {
    asm volatile("s_mov_b32 m0, %0\n\ts_nop 0\n\tglobal_load_lds_dwordx4 %1, %2 sc1\n\tglobal_load_lds_dwordx4 %1, %2 offset:1024 sc1" :: "s"(ldsaddr), "v"(voff), "s"(sbase) : "memory");
}
template <int N> DI void wait_bar_v() { asm volatile("s_waitcnt vmcnt(%0)\n\ts_barrier" :: "i"(N) : "memory"); }
template <int N> DI void wait_bar() { asm volatile("s_waitcnt vmcnt(%0) lgkmcnt(0)\n\ts_barrier" :: "i"(N) : "memory"); }
DI bf16x8 lds_frag(lds_cptr p) { return *(const LAS bf16x8*)p; }
DI s16x4 vtr(lds_cptr p) { return __builtin_bit_cast(s16x4, __builtin_amdgcn_ds_read_tr16_b64_v4i16((LAS v4i16_t*)p)); }
DI bf16x8 tr_frag(lds_cptr p, int second) { const s16x4 lo = vtr(p), hi = vtr(p + second); return __builtin_shufflevector(lo, hi, 0, 1, 2, 3, 4, 5, 6, 7); }
DI u32x4 pack8(const f32x16& x, int s) {
    u32x4 w; w[0] = cvtpk(x[8 * s + 0], x[8 * s + 1]); w[1] = cvtpk(x[8 * s + 2], x[8 * s + 3]); w[2] = cvtpk(x[8 * s + 4], x[8 * s + 5]); w[3] = cvtpk(x[8 * s + 6], x[8 * s + 7]); return w;
}
typedef __amdgpu_buffer_rsrc_t rsrc_t;
DI void st16(rsrc_t r, unsigned off, u32x4 v) { __builtin_amdgcn_raw_buffer_store_b128(v, r, off, 0, 16); }
DI void st8(rsrc_t r, unsigned off, u32x2 v) { __builtin_amdgcn_raw_buffer_store_b64(v, r, off, 0, 16); }
DI void st4(rsrc_t r, unsigned off, unsigned v) { __builtin_amdgcn_raw_buffer_store_b32(v, r, off, 0, 16); }
DI void signal_flag(GAS u64* flag, u64 tag, int tid) {
    asm volatile("s_waitcnt vmcnt(0) lgkmcnt(0)\n\ts_barrier" ::: "memory");
    if (tid == 0) __hip_atomic_store(flag, tag, __ATOMIC_RELAXED, __HIP_MEMORY_SCOPE_AGENT);
}
DI void poll_flags(GAS u64* flags, int idx, u64 tag) {
    for (unsigned spins = 0; spins < (1u << 16); ++spins) {
        const u64 v = idx >= 0 ? __hip_atomic_load(flags + idx, __ATOMIC_RELAXED, __HIP_MEMORY_SCOPE_AGENT) : tag;
        if (__all(v == tag)) break;
        __builtin_amdgcn_s_sleep(2);
    }
}
DI unsigned ld4c(rsrc_t r, unsigned off) { return __builtin_amdgcn_raw_buffer_load_b32(r, off, 0, 16); }
DI void flag_store(GAS u64* flag, u64 tag) { __hip_atomic_store(flag, tag, __ATOMIC_RELAXED, __HIP_MEMORY_SCOPE_AGENT); }

struct Args { const u16 *x, *ckv, *wq, *wuk, *wuv, *wo, *bk, *bv, *shk, *ssmk, *ssmD, *gw; u16* out; char* ws; };

constexpr int P0_OFF = 133120;
DI float blo(unsigned w) { return __uint_as_float(w << 16); }
DI float bhi(unsigned w) { return __uint_as_float(w & 0xffff0000u); }
template <int CTRL> DI float dpp_add(float v) { return v + __builtin_bit_cast(float, __builtin_amdgcn_update_dpp(0, __builtin_bit_cast(int, v), CTRL, 0xf, 0xf, true)); }
DI float wave_sum(float v) {
    v = dpp_add<0x128>(v); v = dpp_add<0x124>(v); v = dpp_add<0x122>(v); v = dpp_add<0x121>(v);
    auto a = __builtin_amdgcn_permlane16_swap(__float_as_uint(v), __float_as_uint(v), false, false); v = __uint_as_float(a[0]) + __uint_as_float(a[1]);
    auto b = __builtin_amdgcn_permlane32_swap(__float_as_uint(v), __float_as_uint(v), false, false); return __uint_as_float(b[0]) + __uint_as_float(b[1]);
}
DI int div10(int x) { return (x * 6554) >> 16; }
DI void p0a_load(const Args& a, int jb0, unsigned lds0, int wid, int lane) {
#pragma unroll
    for (int i = 0; i < 9; ++i) {
        const int p = wid + 8 * i;
        if (p < 66) {
            const int k = p >= 44 ? 2 : (p >= 22 ? 1 : 0), q = p - 22 * k;
            int jb = jb0 + 96 * k; if (jb >= 256) jb = jb0;
            const int cb = jb & 7, tb = jb >> 3;
            if (q < 11) { const int ci = q * 64 + lane, cj = ci < 670 ? ci : 669, row = div10(cj); int t = tb * 64 - 3 + row; t = t < 0 ? 0 : t;
                dma1(lds0 + p * 1024, (unsigned)(t * 1280 + (cj - 10 * row) * 16), (const char*)a.bk + cb * 160); }
            else if (q < 21) { const int ci = (q - 11) * 64 + lane, row = div10(ci);
                dma1(lds0 + p * 1024, (unsigned)(row * 1280 + (ci - 10 * row) * 16), (const char*)a.bv + (size_t)tb * 64 * 1280 + cb * 160); }
            else dma1(lds0 + p * 1024, (unsigned)((lane < 40 ? lane : 39) * 16), (const char*)a.shk + cb * 640);
        }
    }
}
DI void p0a_compute(int jb0, char* lds, int tid) {
    const int nj = jb0 + 192 < 256 ? 3 : 2;
#pragma unroll
    for (int it = 0; it < 2; ++it) {
        const int item = tid + it * 512, k = item >= 640 ? 2 : (item >= 320 ? 1 : 0), e = item - k * 320, tp = div10(e), cc = e - 10 * tp;
        if (k < nj && item < 960) {
            const int t0 = ((jb0 + 96 * k) >> 3) * 64;
            const lds_cptr in = (lds_cptr)lds + k * 22528;
            LAS char* tile = (LAS char*)lds + P0_OFF + k * 10240;
            u32x4 kk[5], vv[2], sh[4];
#pragma unroll
            for (int i = 0; i < 5; ++i) kk[i] = *(const LAS u32x4*)(in + (2 * tp + i) * 160 + cc * 16);
#pragma unroll
            for (int i = 0; i < 2; ++i) vv[i] = *(const LAS u32x4*)(in + 11264 + (2 * tp + i) * 160 + cc * 16);
#pragma unroll
            for (int i = 0; i < 4; ++i) sh[i] = *(const LAS u32x4*)(in + 21504 + cc * 64 + i * 16);
            if (t0 + 2 * tp < 3) {
#pragma unroll
                for (int i = 0; i < 3; ++i) if (t0 + 2 * tp + i - 3 < 0) kk[i] = (u32x4){0u, 0u, 0u, 0u};
            }
            const int dw = 31 - tp, pos = (((dw >> 2) ^ (cc & 7)) << 4) + (dw & 3) * 4;
#pragma unroll
            for (int qp = 0; qp < 4; ++qp) {
                f32x2 kx[5], r2[2];
#pragma unroll
                for (int i = 0; i < 5; ++i) kx[i] = (f32x2){blo(kk[i][qp]), bhi(kk[i][qp])};
                const f32x2 t0_ = {blo(sh[qp][0]), blo(sh[qp][2])}, t1_ = {bhi(sh[qp][0]), bhi(sh[qp][2])}, t2_ = {blo(sh[qp][1]), blo(sh[qp][3])}, t3_ = {bhi(sh[qp][1]), bhi(sh[qp][3])};
#pragma unroll
                for (int r = 0; r < 2; ++r)
                    r2[r] = (kx[r] * t3_ + kx[r + 1] * t2_ + kx[r + 2] * t1_ + kx[r + 3] * t0_) * (f32x2){blo(vv[r][qp]), bhi(vv[r][qp])};
                *(LAS unsigned*)(tile + (8 * cc + 2 * qp) * 128 + pos) = cvtpk(r2[1][0], r2[0][0]);
                *(LAS unsigned*)(tile + (8 * cc + 2 * qp + 1) * 128 + pos) = cvtpk(r2[1][1], r2[0][1]);
            }
        }
    }
}
DI void p0a_store(rsrc_t wr, int jb0, char* lds, int tid) {
    const int nj = jb0 + 192 < 256 ? 3 : 2;
#pragma unroll
    for (int it = 0; it < 4; ++it) { const int e = tid + it * 512, k = e >= 1280 ? 2 : (e >= 640 ? 1 : 0), f = e - k * 640, cl = f >> 3, d = f & 7;
        if (k < nj && e < 1920) { const int jb = jb0 + 96 * k;
            st16(wr, (unsigned)(WS_UR + ((size_t)((jb & 7) * 80 + cl) * S + (2047 - (jb >> 3) * 64 - 63) + 8 * d) * 2),
                 *(const LAS u32x4*)((lds_cptr)lds + P0_OFF + k * 10240 + cl * 128 + ((d ^ ((cl >> 3) & 7)) << 4))); } }
}
DI void p0b_load(const Args& a, int b, unsigned lds0, int wid, int lane) {
    const int cb = b & 7, tb = b >> 3;
#pragma unroll
    for (int i = 0; i < 3; ++i) { const int p = wid + 8 * i;
        if (p < 20) { const int ci = p * 64 + lane, row = div10(ci);
            dma1(lds0 + P0_OFF + p * 1024, (unsigned)(row * 1280 + (ci - 10 * row) * 16), (const char*)a.wo + (size_t)tb * 128 * 1280 + cb * 160); }
    }
}
DI void p0b_finish(rsrc_t wr, int b, char* lds, int wid, int lane) {
    const int cb = b & 7, tb = b >> 3;
    const int j = lane & 15, g = lane >> 4, q = j >> 2, p = lane & 3;
    const lds_cptr brd = (lds_cptr)lds + P0_OFF + (16 * (g >> 1) + 4 * (g & 1) + q) * 160 + p * 8;
#pragma unroll
    for (int i = 0; i < 3; ++i) if (wid + 8 * i < 20) { const int f = wid + 8 * i, kq = f / 5, cbk = f % 5;
        const bf16x8 w = tr_frag(brd + kq * 32 * 160 + cbk * 32, 1280);
        st16(wr, (unsigned)(WS_WO + ((size_t)(cb * 32 + tb) * 20 + f) * 1024 + lane * 16), __builtin_bit_cast(u32x4, w)); }
}

#define PF_TILE 6
constexpr int TSLOT = 32 * 1040;
struct P1Ctx { const char* wsrc; const char* tsrc; unsigned wvoff, wlds, tvoff, tlds; };
DI P1Ctx p1_setup(const Args& a, int h, int r, unsigned lds0, int wid, int lane) {
    P1Ctx c;
    c.wsrc = (const char*)((wid >> 2) ? a.wuv : a.wuk) + (size_t)h * 256 + ((wid >> 1) & 1) * 128 + (size_t)(wid & 1) * 4 * 8 * 8192;
    c.wvoff = (unsigned)(lane >> 3) * 8192u + (unsigned)(((lane & 7) ^ (((lane >> 4) & 1) << 2)) * 16);
    c.wlds = lds0 + ((wid >> 2) * 2 + ((wid >> 1) & 1)) * 8192 + (wid & 1) * 4 * 1024;
    c.tsrc = (const char*)a.ckv + (size_t)(256 * r + 4 * wid) * 1024;
    c.tvoff = lane * 16; c.tlds = lds0 + 4 * wid * 1040;
    return c;
}
DI void p1_dma_w1(const P1Ctx& c, int ch, int slot, int i) { dma1(c.wlds + slot * TSLOT + i * 1024, c.wvoff, c.wsrc + (size_t)ch * 64 * 8192 + (size_t)i * 8 * 8192); }
template <int J> DI void p1_dma_t1(const P1Ctx& c, int t, int slot) {
    asm volatile("s_mov_b32 m0, %0\n\ts_nop 0\n\tglobal_load_lds_dwordx4 %1, %2 offset:%c3" :: "s"(c.tlds + slot * TSLOT + 16 * J), "v"(c.tvoff), "s"(c.tsrc + (size_t)t * 32 * 1024), "i"(J * 1024) : "memory");
}
DI void p1_prime_t(const P1Ctx& c) { p1_dma_t1<0>(c, 0, 3); p1_dma_t1<1>(c, 0, 3); p1_dma_t1<2>(c, 0, 3); p1_dma_t1<3>(c, 0, 3); }
DI void p1_prime_w(const P1Ctx& c) {
#pragma unroll
    for (int ch = 0; ch < 3; ++ch)
#pragma unroll
        for (int i = 0; i < 4; ++i) p1_dma_w1(c, ch, ch, i);
}
constexpr int STG_UR = 3 * TSLOT, STG_H = STG_UR + 16384;
template <int MAT>
DI void p1_body(const Args& a, const P1Ctx& c, rsrc_t wr, int b, int h, int r, char* lds, int wid, int lane, int tid, GAS u64* flags, u64 tag) {
    const int g4 = lane >> 4, q = (lane & 15) >> 2, p = lane & 3;
    const lds_cptr wrd = (lds_cptr)lds + (MAT * 2 + ((wid >> 1) & 1)) * 8192 + (8 * (g4 >> 1) + q) * 128 + (((wid & 1) ^ (q >> 1)) * 64) + (g4 & 1) * 32 + p * 8;
    const lds_cptr trd = (lds_cptr)lds + (lane & 31) * 1040 + (lane >> 5) * 16;
    const unsigned dst = (unsigned)((MAT ? WS_V : WS_K) + ((size_t)(h * 64 + 8 * r) * 8 + 2 * (wid & 3)) * 1024) + lane * 16;
    unsigned pfd = 0;
    bf16x8 Wf[32];
    f32x16 acc = f32x16{};
    const lds_cptr t0p = trd + 3 * TSLOT;
#define P1M(A_, W_) (MAT == 0 ? MFMA32(W_, A_, acc) : MFMA32(A_, W_, acc))
#pragma unroll
    for (int ch = 0; ch < 8; ++ch) {
        if (ch == 0) wait_bar<8>(); else wait_bar<4>();
        if (ch == 3 && b >= 160 && tid < 3) { const int jb = b - 160 + 96 * tid; if (jb < 256) flag_store(flags + FL_F0 + (jb & 7) * 32 + (jb >> 3), tag); }
        const lds_cptr wb = wrd + (ch % 3) * TSLOT;
        bf16x8 af[4];
#pragma unroll
        for (int s = 0; s < 4; ++s) { Wf[4 * ch + s] = tr_frag(wb + s * 2048, 512); af[s] = lds_frag(t0p + (4 * ch + s) * 32); }
        SBAR();
#pragma unroll
        for (int s = 0; s < 4; ++s) {
            if (ch >= 1) { if (ch + 2 <= 7) p1_dma_w1(c, ch + 2, (ch + 2) % 3, s);
                           else { if (s == 0) p1_dma_t1<0>(c, ch - 5, (ch + 2) % 3); if (s == 1) p1_dma_t1<1>(c, ch - 5, (ch + 2) % 3); if (s == 2) p1_dma_t1<2>(c, ch - 5, (ch + 2) % 3); if (s == 3) p1_dma_t1<3>(c, ch - 5, (ch + 2) % 3); } }
            acc = P1M(af[s], Wf[4 * ch + s]); PIN(acc);
            SBAR();
        }
    }
    f32x16 accp = acc;
    bf16x8 af[4];
    wait_bar<4>();
#pragma unroll
    for (int s = 0; s < 4; ++s) af[s] = lds_frag(trd + (8 % 3) * TSLOT + s * 32);
    SBAR();
#pragma unroll
    for (int i = 1; i < 8; ++i) {
        const int j = 7 + i;
        const lds_cptr tb = trd + (j % 3) * TSLOT, tn = trd + ((j + 1) % 3) * TSLOT;
#pragma unroll
        for (int s = 0; s < 32; ++s) {
            if (s == 28 && i < 7) {
                if (b < 160 && i == 4 && wid == 0) poll_flags(flags, lane < 32 ? FL_F0 + (b / 20) * 32 + lane : -1, tag);
                if (i + 2 <= 7) wait_bar<6>(); else wait_bar<2>();
                const unsigned lds0 = (unsigned)(uintptr_t)lds;
                if (i == 1) p0b_load(a, b, lds0, wid, lane);
                if (i == PF_TILE) {
                    const int id = (b * 8 + wid) * 60 + (lane < 59 ? lane : 59);
                    const char* p = id < 40960 ? (const char*)a.x + (size_t)id * 64 : (const char*)a.wq + (size_t)(id - 40960) * 64;
                    asm volatile("global_load_dword %0, %1, off" : "+v"(pfd) : "v"(p) : "memory"); }
                if (b < 160) {
                    if (i == 1) dma2(lds0 + STG_H + wid * 2048, lane * 16, (const char*)a.ssmk + (size_t)(4 * b) * 4096 + wid * 2048);
                    if (i == 4) dma2c(lds0 + STG_UR + wid * 2048, lane * 16, a.ws + WS_UR + (size_t)(4 * b) * 4096 + wid * 2048);
                }
                SBAR();
            }
            if (!(s & 1) && s + 4 < 32) { LGKM(2); SBAR(); }
            if (s == 0) acc = MAT == 0 ? MFMA32(Wf[0], af[0], (f32x16{})) : MFMA32(af[0], Wf[0], (f32x16{})); else acc = P1M(af[s & 3], Wf[s]);
            PIN(acc);
            if (s + 4 < 32) af[s & 3] = lds_frag(tb + (s + 4) * 32); else if (i < 7) af[s & 3] = lds_frag(tn + (s - 28) * 32);
            if (s == 2) st16(wr, dst + (i - 1) * 8192, pack8(accp, 0));
            if (s == 4) st16(wr, dst + (i - 1) * 8192 + 1024, pack8(accp, 1));
            if (i + 2 <= 7) {
                if (s == 6) p1_dma_t1<0>(c, i + 2, (j + 2) % 3);
                if (s == 11) p1_dma_t1<1>(c, i + 2, (j + 2) % 3);
                if (s == 16) p1_dma_t1<2>(c, i + 2, (j + 2) % 3);
                if (s == 21) p1_dma_t1<3>(c, i + 2, (j + 2) % 3);
            }
            SBAR();
        }
        accp = acc;
    }
    asm volatile("" : "+v"(pfd));
    st16(wr, dst + 7 * 8192, pack8(accp, 0)); st16(wr, dst + 7 * 8192 + 1024, pack8(accp, 1));
#undef P1M
}

constexpr float QSCALE = 0.08838834764831845f * 1.4426950408889634f;
constexpr float THR = 8.f;
constexpr int QCH = 49152;
constexpr int KRING = 0, VRING = 65536, SPARE = 131072, QA_OFF = VRING + 32768;
#define MX3(a, b, c) __builtin_fmaxf(__builtin_fmaxf((a), (b)), (c))

DI void p2_body(const Args& a, int b, int h, int r, int blkA, int blkB, char* lds, int wid, int lane, int tid, GAS u64* flags, u64 tag) {
    const unsigned lds0 = (unsigned)(uintptr_t)lds;
    const int r32 = lane & 31, hi = lane >> 5, rg = wid & 3, par = wid >> 2;
    const int myblk = par ? blkB : blkA;
    const char* xg = (const char*)a.x + (size_t)(128 * myblk + 32 * rg) * 1280;
    const unsigned xvoff = (unsigned)(lane >> 3) * 1280u + (unsigned)(((lane & 7) ^ ((lane >> 4) & 7)) * 16);
    const unsigned xvoff2 = (unsigned)(lane >> 3) * 1280u + 8 * 1280u + (unsigned)(((lane & 7) ^ ((4 + (lane >> 4)) & 7)) * 16);
    const unsigned xlds = lds0 + 16384 + wid * 4096;
    const char* wg = (const char*)a.wq + (size_t)h * 256 + (wid >> 2) * 128 + (size_t)(wid & 3) * 2 * 8 * 8192;
    const unsigned wvoff = (unsigned)(lane >> 3) * 8192u + (unsigned)(((lane & 7) ^ (((lane >> 4) & 1) << 2)) * 16);
    const unsigned wvoff2 = wvoff + 8 * 8192u;
    const unsigned wlds = lds0 + 16384 + 32768 + (wid >> 2) * 8192 + (wid & 3) * 2048;
    const int g4 = lane >> 4, q = (lane & 15) >> 2, p = lane & 3;
    const int hsw = (q >> 1) & 1, ysw = hi ^ ((r32 >> 1) & 7);
    const lds_cptr wrd0 = (lds_cptr)lds + 16384 + 32768 + (8 * (g4 >> 1) + q) * 128 + (g4 & 1) * 32 + p * 8 + hsw * 64;
    const lds_cptr wrd1 = (lds_cptr)lds + 16384 + 32768 + (8 * (g4 >> 1) + q) * 128 + (g4 & 1) * 32 + p * 8 + (hsw ^ 1) * 64;
    lds_cptr xrd[4];
#pragma unroll
    for (int s = 0; s < 4; ++s) xrd[s] = (lds_cptr)lds + 16384 + wid * 4096 + r32 * 128 + (((2 * s) ^ ysw) << 4);
    f32x16 acc[4] = {};
#pragma unroll
    for (int c = 0; c < 2; ++c) { const char* xp = xg + c * 128; const char* wp = wg + (size_t)c * 64 * 8192; const unsigned so_ = ((c + 2) % 3) * QCH;
        dma1(xlds + so_, xvoff, xp); dma1(xlds + so_ + 1024, xvoff2, xp); dma1(xlds + so_ + 2048, xvoff, xp + 16 * 1280); dma1(xlds + so_ + 3072, xvoff2, xp + 16 * 1280);
        dma1(wlds + so_, wvoff, wp); dma1(wlds + so_ + 1024, wvoff2, wp); }
    unsigned so = 2 * QCH;
    const char* xp = xg + 2 * 128; const char* wp = wg + (size_t)2 * 64 * 8192;
    const unsigned kl0 = lds0 + KRING + wid * 2048, tvoff = lane * 16;
    const char* Kw = a.ws + WS_K + (size_t)h * 64 * 8192 + wid * 2048;
#define WFR(B0, B1, i) tr_frag((((i) & 1) ? (B1) : (B0)) + (((i) >> 1) & 1) * 8192 + ((i) >> 2) * 2048, 512)
    bf16x8 wf[4], xf[2];
#define P2CHUNK(MODE, EXTRA, LAST) do { \
        const unsigned sn = so == 0 ? 2 * QCH : so - QCH, sx = so == 2 * QCH ? 0 : so + QCH;        \
        const unsigned xl_ = xlds + sn, wl_ = wlds + sn; const char* xp2 = xp + 16 * 1280; \
        const lds_cptr wb0 = wrd0 + so, wb1 = wrd1 + so, wn0 = wrd0 + sx, wn1 = wrd1 + sx; \
        _Pragma("unroll") for (int i = 0; i < 16; ++i) { \
            if (i == 12 && !(LAST)) { \
                if ((EXTRA) == 2 && wid == 0) poll_flags(flags, lane < 8 ? FL_F1 + h * 8 + lane : -1, tag); \
                if ((MODE) == 0) wait_bar<6>(); else wait_bar<4>(); \
                if ((EXTRA) == 1 && tid == 0) { flag_store(flags + FL_FW + (b & 7) * 32 + (b >> 3), tag); if (b < 160) flag_store(flags + FL_F4 + b, tag); else flag_store(flags + FL_F1 + h * 8 + r, tag); } \
                SBAR(); } \
            acc[i & 3] = MFMA32(wf[i & 3], xf[(i >> 2) & 1], acc[i & 3]); PIN(acc[i & 3]); \
            if (i + 4 < 16) wf[i & 3] = WFR(wb0, wb1, i + 4); else if (!(LAST)) wf[i & 3] = WFR(wn0, wn1, i - 12); \
            if ((i & 3) == 0 && i + 4 < 16) xf[((i >> 2) + 1) & 1] = lds_frag(xrd[(i >> 2) + 1] + so); \
            if (i == 12 && !(LAST)) xf[0] = lds_frag(xrd[0] + sx); \
            if ((MODE) == 0) { \
                if (i == 0) { SETM0(xl_, 0); } if (i == 1) { GLDS(xvoff, xp, 0); SETM0(xl_, 1024); } \
                if (i == 3) { GLDS(xvoff2, xp, 0); SETM0(xl_, 2048); } if (i == 5) { GLDS(xvoff, xp2, 0); SETM0(xl_, 3072); } \
                if (i == 7) { GLDS(xvoff2, xp2, 0); SETM0(wl_, 0); } if (i == 9) { GLDS(wvoff, wp, 0); SETM0(wl_, 1024); } \
                if (i == 11) { GLDS(wvoff2, wp, 0); } \
            } else { \
                const char* ku_ = Kw + (size_t)(2 * ((MODE) - 1)) * 16384; \
                if (i == 0) { SETM0(kl0, (2 * ((MODE) - 1)) * 16384); } if (i == 1) GLDSC(tvoff, ku_, 0); if (i == 4) GLDSC(tvoff, ku_, 1024); \
                if (i == 6) { SETM0(kl0, (2 * ((MODE) - 1) + 1) * 16384); } if (i == 8) GLDSC(tvoff, ku_ + 16384, 0); if (i == 11) GLDSC(tvoff, ku_ + 16384, 1024); \
            } \
            SBAR(); \
        } \
        so = sx; xp += 128; wp += (size_t)64 * 8192; } while (0)
    wait_bar<6>();
    xf[0] = lds_frag(xrd[0] + so);
#pragma unroll
    for (int i = 0; i < 4; ++i) wf[i] = WFR(wrd0 + so, wrd1 + so, i);
    SBAR();
    P2CHUNK(0, 0, false);
    P2CHUNK(0, 1, false);
#pragma unroll 1
    for (int c = 2; c < 7; ++c) P2CHUNK(0, 0, false);
    P2CHUNK(0, 2, false);
    P2CHUNK(1, 0, false);
    P2CHUNK(2, 0, true);
#undef P2CHUNK
    wait_bar<63>();
    lds_ptr qw = (lds_ptr)lds + (par ? SPARE : QA_OFF) + rg * 8192 + lane * 16;
#pragma unroll
    for (int db = 0; db < 4; ++db) {
#pragma unroll
        for (int e = 0; e < 16; ++e) acc[db][e] *= QSCALE;
        *(LAS u32x4*)(qw + (2 * db) * 1024) = pack8(acc[db], 0);
        *(LAS u32x4*)(qw + (2 * db + 1) * 1024) = pack8(acc[db], 1);
    }
#undef WFR
}

DI void p3_body(rsrc_t wr, const char* ws, int h, int blkA, int blkB, char* lds, int wid, int lane, int tid, GAS u64* flags, u64 tag) {
    const unsigned lds0 = (unsigned)(uintptr_t)lds;
    const int r32 = lane & 31, hi = lane >> 5, rg = wid & 3, par = wid >> 2;
    const unsigned tvoff = lane * 16;
    const unsigned kl0 = lds0 + KRING + wid * 2048, vl0 = lds0 + VRING + wid * 2048;
    const char* Kw = ws + WS_K + (size_t)h * 64 * 8192 + wid * 2048; const char* Vw = ws + WS_V + (size_t)h * 64 * 8192 + wid * 2048;
    const lds_cptr krd = (lds_cptr)lds + KRING + par * 8192 + lane * 16;
    const lds_cptr vrd = (lds_cptr)lds + VRING + par * 8192 + lane * 16;
    int g0 = 0;
    bf16x8 qf[8];
#define VF(i) ((((i) & 3) * 2 + ((i) >> 2)) * 1024)
#define MFMA_C(D, A, B, C) asm volatile("v_mfma_f32_32x32x16_bf16 %0, %2, %3, %1" : "=&v"(D), "+v"(C) : "v"(A), "v"(B))
#define SLOT(X, Y, PN, PP, ODD, QK, SM, PV, FIRST, DM, PFK, MASK) do { \
        unsigned long long fire = 0; \
        _Pragma("unroll") for (int i = 0; i < 16; ++i) { \
            if (i < 8 && QK && !(i & 1)) { LGKM(2); SBAR(); } \
            if (i >= 8 && PV && !(i & 1)) { LGKM(2); SBAR(); } \
            if (i < 8) { if (QK) { if (i == 0) { if (FIRST) X = MFMA32(kf[0], qf[0], zero16); else MFMA_C(X, kf[0], qf[0], negm); } else X = MFMA32(kf[i & 3], qf[i], X); PIN(X); \
                                   if (i < 4) kf[i] = lds_frag(kp + ((ODD) ? 0 : 16384) + (i + 4) * 1024); } \
                         if (PV && i >= 4) vf[i - 4] = lds_frag(vp + ((ODD) ? 0 : 16384) + VF(i - 4)); } \
            else { if (PV) { o[i & 3] = MFMA32(vf[i & 3], __builtin_bit_cast(bf16x8, PP[(i - 8) >> 2]), o[i & 3]); PIN(o[i & 3]); if (i < 12) vf[i & 3] = lds_frag(vp + ((ODD) ? 0 : 16384) + VF(i - 4)); } \
                   if (PFK && i >= 12) kf[i - 12] = lds_frag(kp + ((ODD) ? 16384 : 0) + (i - 12) * 1024); } \
            if (!(ODD) && i == 5) { kp = (lds_cptr)(uintptr_t)((unsigned)(uintptr_t)kp ^ 0x8000u); PIN(kp); } \
            if (!(ODD) && i == 13) { vp = (lds_cptr)(uintptr_t)((unsigned)(uintptr_t)vp ^ 0x8000u); PIN(vp); } \
            if (DM) { \
                if (i == 0) SETM0(dk, (ODD) ? 16384 : 0); \
                if (i == 1) GLDSC(uoff, Kw, 0); \
                if (i == 3) GLDSC(uoff, Kw, 1024); \
                if ((DM) & 2) { \
                    if (i == 4) SETM0(dv, (ODD) ? 16384 : 0); \
                    if (i == 5) GLDSC(uoffv, Vw, 0); \
                    if (i == 7) GLDSC(uoffv, Vw, 1024); \
                    if (i == 11) { uoffv += 16384u; PIN(uoffv); } } \
                if (i == 8) { uoff += (j + 5 == n) ? 16384u * (unsigned)(1 - n) : 16384u; PIN(uoff); } \
                if ((ODD) && i == 9) { dk ^= 0x8000u; dv ^= 0x8000u; PINS(dk); PINS(dv); } } \
            if (SM) { \
                if (i == 0) { Y[0] = EXP2(Y[0]); Y[1] = EXP2(Y[1]); } else if (i < 15) Y[i + 1] = EXP2(Y[i + 1]); \
                if (i == 1) l2 += Y[0]; \
                if (i >= 1) { if (i & 1) l += Y[i]; else l2 += Y[i]; PIN(l); PIN(l2); } \
                if (i & 1) { PN[i >> 3][(i >> 1) & 3] = cvtpk(Y[i - 1], Y[i]); PIN(PN[i >> 3]); } \
                if (i < 15) PIN(Y); } \
            if (QK) { \
                if (MASK && i == 9 && (MASK == 1 || j + 1 >= n - 2)) { int qq = qrel - 64 * (j + 1 - (n - 2)); PIN(qq); \
                    _Pragma("unroll") for (int r = 0; r < 16; ++r) { const int c = (r & 3) + 8 * (r >> 2); if (c > qq) X[r] = -INFINITY; } } \
                if (i == 9) { mxa = MX3(X[0], X[1], X[2]); PIN(mxa); } \
                if (i == 10) { mxb = MX3(X[3], X[4], X[5]); mxa = MX3(mxa, X[6], X[7]); PIN(mxb); PIN(mxa); } \
                if (i == 11) { mxb = MX3(mxb, X[8], X[9]); PIN(mxb); } \
                if (i == 12) { mxa = MX3(mxa, X[10], X[11]); mxb = MX3(mxb, X[12], X[13]); PIN(mxb); PIN(mxa); } \
                if (i == 13) { mxa = MX3(mxa, X[14], X[15]); PIN(mxa); } \
                if (i == 14) { mxa = MX3(mxa, mxb, X[0]); PIN(mxa); } \
                if (i == 15 && !(FIRST)) { fire = __ballot(mxa > THR); PINS(fire); } } \
            SBAR(); \
        } \
        if (__builtin_expect((fire | pend) != 0, 0) || FIRST) { \
            if (PV && pend) { _Pragma("unroll") for (int d = 0; d < 4; ++d) _Pragma("unroll") for (int r = 0; r < 16; ++r) o[d][r] *= alp; } \
            pend = 0; \
            if (QK && (FIRST || fire)) { \
                auto rr = __builtin_amdgcn_permlane32_swap(__float_as_uint(mxa), __float_as_uint(mxa), false, false); \
                const float rm = __builtin_fmaxf(__uint_as_float(rr[0]), __uint_as_float(rr[1])); \
                if (FIRST) { const float dl = __builtin_fmaxf(rm, -1e30f); m = dl; \
                    _Pragma("unroll") for (int r = 0; r < 16; ++r) { X[r] -= dl; negm[r] = -dl; } PIN(negm); } \
                else { const float dl = __builtin_fmaxf(rm, 0.f); m += dl; alp = EXP2(-dl); l *= alp; l2 *= alp; pend = 1; \
                    const float ndl = -dl, aone = hi ? 0.f : 1.f; \
                    asm volatile("s_nop 1\n\tv_mfma_f32_32x32x2_f32 %0, %2, %3, %0\n\tv_mfma_f32_32x32x2_f32 %1, %2, %3, %1\n\ts_nop 7\n\ts_nop 7\n\ts_nop 7" : "+v"(X), "+v"(negm) : "v"(aone), "v"(ndl)); } } } \
        SBAR(); \
        } while (0)
#define TBAR() wait_bar_v<4>()
    const f32x16 zero16 = {};
#pragma unroll 1
    for (int pass = 0; pass < 2; ++pass) {
        const int blk = pass ? blkB : blkA, n = 2 * (blk + 1);
        dma2c(vl0 + (g0 & 3) * 16384, tvoff, Vw); dma2c(vl0 + ((g0 + 1) & 3) * 16384, tvoff, Vw + 16384);
        if (pass == 0) wait_bar<4>();
        if (pass == 0) {
            const lds_cptr qr = (lds_cptr)lds + QA_OFF + rg * 8192 + lane * 16;
#pragma unroll
            for (int s = 0; s < 8; ++s) qf[s] = lds_frag(qr + s * 1024);
        }
        float m = -1e30f, l = 0.f, l2 = 0.f, alp = 1.f, mxa = 0.f, mxb = 0.f; unsigned long long pend = 0; f32x16 o[4] = {};
        u32x4 pk0[2] = {}, pk1[2] = {};
        f32x16 s0, s1, negm;
        bf16x8 kf[4], vf[4] = {};
        const int qrel = 32 * rg + r32 - 32 * par - 4 * hi;
        lds_cptr kp = krd + (g0 & 2) * 16384, vp = vrd + ((g0 & 2) ^ 2) * 16384;
        unsigned dk = kl0 + (g0 & 2) * 16384, dv = vl0 + ((g0 + 2) & 2) * 16384;
        unsigned uoffv = tvoff + 2 * 16384;
        unsigned uoff = tvoff + ((4 < n) ? 4 : 4 - n) * 16384;
#pragma unroll
        for (int i = 0; i < 4; ++i) kf[i] = lds_frag(kp + i * 1024);
        int j = -1;
        SLOT(s0, s1, pk1, pk0, true, true, false, false, true, false, true, 2);
        wait_bar_v<63>();
        j = 0;
        SLOT(s1, s0, pk0, pk1, false, true, true, false, false, 3, true, 2);
        TBAR();
        if (pass == 1 && tid == 0) flag_store(flags + FL_F3 + blkA * 32 + h, tag);
#pragma unroll 1
        for (j = 1; j < n - 3; j += 2) {
            SLOT(s0, s1, pk1, pk0, true, true, true, true, false, 3, true, 0);
            TBAR();
            ++j;
            SLOT(s1, s0, pk0, pk1, false, true, true, true, false, 3, true, 0);
            TBAR();
            --j;
        }
        if (n >= 4) {
            j = n - 3;
            SLOT(s0, s1, pk1, pk0, true, true, true, true, false, 3, true, 1);
            TBAR();
            j = n - 2;
            SLOT(s1, s0, pk0, pk1, false, true, true, true, false, 1, true, 1);
            wait_bar_v<2>();
        }
        j = n - 1;
        SLOT(s0, s1, pk1, pk0, true, false, true, true, false, 1, false, 0);
        wait_bar_v<2>();
        if (pass == 0) {
            const lds_cptr qr = (lds_cptr)lds + SPARE + rg * 8192 + lane * 16;
#pragma unroll
            for (int s = 0; s < 8; ++s) qf[s] = lds_frag(qr + s * 1024);
        }
        j = n;
        SLOT(s1, s0, pk0, pk1, false, false, false, true, false, false, false, 0);
        g0 += n;
        STAMP(4 + 2 * pass);
        {
            l += l2;
            auto rr = __builtin_amdgcn_permlane32_swap(__float_as_uint(l), __float_as_uint(l), false, false);
            l = __uint_as_float(rr[0]) + __uint_as_float(rr[1]);
            wait_bar<63>();
            LAS float* ml = (LAS float*)((lds_ptr)lds + SPARE);
            *(LAS f32x2*)(ml + (wid * 64 + lane) * 2) = (f32x2){m, l};
            lds_ptr sendp = (lds_ptr)lds + VRING + par * 32768 + rg * 8192 + lane * 16;
            lds_cptr recvp = (lds_cptr)lds + VRING + (par ^ 1) * 32768 + rg * 8192 + lane * 16;
            f32x16 keep[2];
            if (par) {
#pragma unroll
                for (int d = 0; d < 2; ++d) { keep[d] = o[2 + d];
#pragma unroll
                    for (int g = 0; g < 4; ++g) *(LAS f32x4*)(sendp + (d * 4 + g) * 1024) = (f32x4){o[d][4 * g], o[d][4 * g + 1], o[d][4 * g + 2], o[d][4 * g + 3]}; }
            } else {
#pragma unroll
                for (int d = 0; d < 2; ++d) { keep[d] = o[d];
#pragma unroll
                    for (int g = 0; g < 4; ++g) *(LAS f32x4*)(sendp + (d * 4 + g) * 1024) = (f32x4){o[2 + d][4 * g], o[2 + d][4 * g + 1], o[2 + d][4 * g + 2], o[2 + d][4 * g + 3]}; }
            }
            wait_bar<63>();
            const f32x2 mlo = *(const LAS f32x2*)(ml + ((wid ^ 4) * 64 + lane) * 2);
            const float mm = __builtin_fmaxf(m, mlo[0]), fo = EXP2(m - mm), fx = EXP2(mlo[0] - mm);
            const float inv = __builtin_amdgcn_rcpf(l * fo + mlo[1] * fx), sc = fo * inv, sx = fx * inv;
            const unsigned cdst = (unsigned)(WS_CTX + ((size_t)(blk * 4 + rg) * 32 + h) * 8192 + (par * 4) * 1024) + lane * 16;
#pragma unroll
            for (int d = 0; d < 2; ++d) {
#pragma unroll
                for (int g = 0; g < 4; ++g) {
                    const f32x4 v4 = *(const LAS f32x4*)(recvp + (d * 4 + g) * 1024);
#pragma unroll
                    for (int e = 0; e < 4; ++e) keep[d][4 * g + e] = keep[d][4 * g + e] * sc + v4[e] * sx;
                }
                st16(wr, cdst + (2 * d) * 1024, pack8(keep[d], 0));
                st16(wr, cdst + (2 * d + 1) * 1024, pack8(keep[d], 1));
            }
            if (pass == 0) wait_bar<63>();
            else signal_flag(flags + FL_F3 + blk * 32 + h, tag, tid);
        }
        STAMP(5 + 2 * pass);
    }
#undef SLOT
#undef TBAR
#undef VF
}

constexpr int CSTR = 4160;
constexpr int CCH = 4 * CSTR + 4608;
DI void p4_body(const Args& a, rsrc_t wr, int b, int h, int r, char* lds, int wid, int lane, int tid, GAS u64* flags, u64 tag) {
    const int ch = wid >> 1, c = 4 * b + ch, half = wid & 1;
    lds_ptr base = (lds_ptr)lds + ch * CCH;
    const lds_cptr ur = (lds_cptr)lds + STG_UR + ch * 4096, hs = (lds_cptr)lds + STG_H + ch * 4096;
    u32x4 w0[2], w1[2], hv[2];
#pragma unroll
    for (int it = 0; it < 2; ++it) { const int y8 = half * 128 + it * 64 + lane;
        w0[it] = *(const LAS u32x4*)(ur + 16 * y8); w1[it] = *(const LAS u32x4*)(ur + 16 * (y8 < 255 ? y8 + 1 : y8)); if (y8 == 255) w1[it] = (u32x4){0u, 0u, 0u, 0u};
        hv[it] = *(const LAS u32x4*)(hs + 16 * y8); }
    wait_bar<63>();
    const float Dc = bf2f(a.ssmD[c]);
#pragma unroll
    for (int it = 0; it < 2; ++it) {
        const int y8 = half * 128 + it * 64 + lane;
        unsigned w[6] = {w0[it][0], w0[it][1], w0[it][2], w0[it][3], w1[it][0], w1[it][1]}, o[5];
#pragma unroll
        for (int e = 0; e < 5; ++e) o[e] = __builtin_amdgcn_alignbit(w[e + 1], w[e], 16);
#pragma unroll
        for (int sg = 0; sg < 4; ++sg) { const int e = sg >> 1; u32x4 v;
            if (sg & 1) v = (u32x4){o[e], o[e + 1], o[e + 2], o[e + 3]}; else v = (u32x4){w[e], w[e + 1], w[e + 2], w[e + 3]};
            *(LAS u32x4*)(base + sg * CSTR + y8 * 16) = v; }
        *(LAS u32x4*)(base + 4 * CSTR + 512 + y8 * 16) = hv[it];
    }
    if (half == 1 && lane < 8) { const int sg = lane >> 1, y8 = 256 + (lane & 1); *(LAS u32x4*)(base + sg * CSTR + y8 * 16) = (u32x4){0u, 0u, 0u, 0u}; }
    if (half == 0 && lane < 32) *(LAS u32x4*)(base + 4 * CSTR + lane * 16) = (u32x4){0u, 0u, 0u, 0u};
    wait_bar<63>();
    const int j = lane & 15, g = lane >> 4;
    lds_cptr ard = (lds_cptr)base + ((3 - j) & 3) * CSTR + (2031 - j - ((3 - j) & 3) + 8 * g) * 2 - half * 64 - 62 * 64, ard2 = ard + 8;
    const lds_cptr brd = (lds_cptr)base + 4 * CSTR + (240 + 16 * j + 8 * g) * 2 - half * 64 - 6 * 64;
    PIN(ard2);
    f32x4 acc[8] = {};
#define P4LOAD(A, B, RC) do { _Pragma("unroll") for (int u = 0; u < 8; ++u) { const s16x4 lo = *(const LAS s16x4*)(ard + (62 - (RC) - 8 * u) * 64), hi2 = *(const LAS s16x4*)(ard2 + (62 - (RC) - 8 * u) * 64); \
            A[u] = __builtin_shufflevector(lo, hi2, 0, 1, 2, 3, 4, 5, 6, 7); B[u] = lds_frag(brd + (8 * u - (RC) + 6) * 64); } } while (0)
#define P4MM(A, B) do { _Pragma("unroll") for (int u = 0; u < 8; ++u) _Pragma("unroll") for (int v = 0; v + u < 8; ++v) { acc[u + v] = MFMA16(A[u], B[v], acc[u + v]); PIN(acc[u + v]); SBAR(); } } while (0)
    {
        bf16x8 A0[8], B0[8], A1[8], B1[8];
        P4LOAD(A0, B0, 0); SBAR(); P4LOAD(A1, B1, 2); SBAR();
        P4MM(A0, B0);
        P4LOAD(A0, B0, 4); SBAR();
        P4MM(A1, B1);
        P4LOAD(A1, B1, 6); SBAR();
        P4MM(A0, B0);
        P4MM(A1, B1);
    }
#undef P4LOAD
#undef P4MM
    wait_bar<0>();
    if (tid == 0) flag_store(flags + FL_F1 + h * 8 + r, tag);
    lds_ptr ex = base + CSTR;
    f32x4 keep[4];
    if (half) {
#pragma unroll
        for (int q = 0; q < 4; ++q) { *(LAS f32x4*)(ex + 4096 + q * 1024 + lane * 16) = acc[q]; keep[q] = acc[4 + q]; }
    } else {
#pragma unroll
        for (int q = 0; q < 4; ++q) { *(LAS f32x4*)(ex + q * 1024 + lane * 16) = acc[4 + q]; keep[q] = acc[q]; }
    }
    wait_bar<63>();
    u32x2 zp[4];
#pragma unroll
    for (int q = 0; q < 4; ++q) {
        const f32x4 o = *(const LAS f32x4*)(ex + (half ^ 1) * 4096 + q * 1024 + lane * 16);
        const f32x4 y = keep[q] + o;
        const int t = 256 * (half * 4 + q) + 16 * j + 4 * g;
        const s16x4 kvr = *(const LAS s16x4*)(base + (2047 - t - 3) * 2);
        float z[4];
#pragma unroll
        for (int r = 0; r < 4; ++r) { const float kv = bf2f((u16)kvr[3 - r]); z[r] = y[r] + kv * Dc; }
        zp[q] = (u32x2){cvtpk(z[0], z[1]), cvtpk(z[2], z[3])};
    }
#pragma unroll
    for (int q = 0; q < 4; q += 2) {
        auto s0 = __builtin_amdgcn_permlane16_swap(zp[q][0], zp[q + 1][0], false, false);
        auto s1 = __builtin_amdgcn_permlane16_swap(zp[q][1], zp[q + 1][1], false, false);
        const int t = 256 * (half * 4 + q + (g & 1)) + 16 * j + 8 * (g >> 1);
        st16(wr, (unsigned)(WS_Z + ((size_t)c * S + t) * 2), (u32x4){s0[0], s1[0], s0[1], s1[1]});
    }
}

constexpr int OCH = 40960;
DI u32x4 ld16c(rsrc_t r, unsigned off) { return __builtin_amdgcn_raw_buffer_load_b128(r, off, 0, 16); }
DI void p5_body(const Args& a, rsrc_t wr, int mt, int nt, int ks, char* lds, int wid, int lane, int tid, GAS u64* flags, u64 tag) {
    const unsigned lds0 = (unsigned)(uintptr_t)lds;
    const int rg = wid & 3, kh = wid >> 2;
    const int j = lane & 15, g = lane >> 4;
    const unsigned tv = lane * 16;
    const char* wg = a.ws + WS_WO + ((size_t)(2 * nt + ((wid >> 1) & 1)) * 32 + 8 * ks + 4 * (wid >> 2)) * 20480 + (wid & 1) * 5120;
    const unsigned wl = lds0 + (wid >> 1) * 10240 + (wid & 1) * 5120;
#define WLOAD(slot, st) do { const char* p_ = wg + (size_t)((st) >> 1) * 20480 + ((st) & 1) * 10240; \
        asm volatile("s_mov_b32 m0, %0\n\ts_nop 0\n\tglobal_load_lds_dwordx4 %1, %2 sc1\n\tglobal_load_lds_dwordx4 %1, %2 offset:1024 sc1\n\tglobal_load_lds_dwordx4 %1, %2 offset:2048 sc1\n\t" \
                     "global_load_lds_dwordx4 %1, %2 offset:3072 sc1" :: "s"(wl + (slot) * OCH), "v"(tv), "s"(p_) : "memory"); \
        dma1c(wl + (slot) * OCH + 4096, tv, p_ + 4096); } while (0)
    WLOAD(0, 0); WLOAD(1, 1);
    if (wid == 0) {
        poll_flags(flags, lane < 40 ? FL_F4 + nt * 40 + lane : (lane < 44 ? FL_F0 + (4 * (ks & 1) + lane - 40) * 32 + 2 * mt + (ks >> 1) : -1), tag);
        poll_flags(flags, lane < 8 ? FL_F3 + mt * 32 + 8 * ks + lane : -1, tag);
    }
    wait_bar<63>();
    const char* cg = a.ws + WS_CTX + ((size_t)(4 * mt + rg) * 32 + 8 * ks + 4 * kh) * 8192;
    const unsigned cv = (g >> 1) * 1024 + ((g & 1) * 32 + j) * 16;
    u32x4 cf[3][4];
#define CLOAD(slot, st) do { const char* p_ = cg + (size_t)((st) >> 1) * 8192 + ((st) & 1) * 4096; \
        asm volatile("global_load_dwordx4 %0, %4, %5 sc1\n\tglobal_load_dwordx4 %1, %4, %5 offset:256 sc1\n\tglobal_load_dwordx4 %2, %4, %5 offset:2048 sc1\n\tglobal_load_dwordx4 %3, %4, %5 offset:2304 sc1" \
                     : "=&v"(cf[slot][0]), "=&v"(cf[slot][1]), "=&v"(cf[slot][2]), "=&v"(cf[slot][3]) : "v"(cv), "s"(p_) : "memory"); } while (0)
    CLOAD(0, 0); CLOAD(1, 1);
    unsigned zr[5];
#pragma unroll
    for (int it = 0; it < 5; ++it) { const int e = tid + it * 512, cl2 = e >> 4, d = e & 15;
        zr[it] = ld4c(wr, (unsigned)(WS_Z + ((size_t)(nt * 160 + cl2) * S + mt * 128 + ks * 32 + 2 * d) * 2)); }
    u32x2 xv[3]; float gt[3];
#pragma unroll
    for (int k = 0; k < 3; ++k) { const int i = wid + 8 * k < 20 ? wid + 8 * k : wid, t = mt * 128 + ks * 32 + 16 * ((i % 10) / 5) + j, c = nt * 160 + 80 * (i / 10) + 16 * (i % 5) + 4 * g;
        xv[k] = *(const u32x2*)(a.x + (size_t)t * M + c); gt[k] = __uint_as_float(ld4c(wr, (unsigned)(WS_G + t * 4))); }
    const lds_cptr brd = (lds_cptr)lds + kh * 20480 + lane * 16;
    f32x4 acc[2][10] = {};
#pragma unroll
    for (int st = 0; st < 8; ++st) {
        if (st == 0) wait_bar<4>(); else if (st < 7) wait_bar<9>(); else wait_bar<0>();
        asm volatile("" : "+v"(cf[st % 3][0]), "+v"(cf[st % 3][1]), "+v"(cf[st % 3][2]), "+v"(cf[st % 3][3]));
        if (st == 0) { _Pragma("unroll") for (int k = 0; k < 3; ++k) { PIN(xv[k]); PIN(gt[k]); } _Pragma("unroll") for (int it = 0; it < 5; ++it) PIN(zr[it]); }
        const lds_cptr bb = brd + (st % 3) * OCH;
        bf16x8 wf[4];
#define WFI(i) lds_frag(bb + (((i) / 5) & 1) * 10240 + (((i) / 10) * 5 + (i) % 5) * 1024)
#pragma unroll
        for (int i = 0; i < 4; ++i) wf[i] = WFI(i);
        SBAR();
#pragma unroll
        for (int i = 0; i < 20; ++i) {
            const int ks2 = i / 10, c10 = i % 10;
            acc[0][c10] = MFMA16(wf[i & 3], __builtin_bit_cast(bf16x8, cf[st % 3][2 * ks2]), acc[0][c10]); PIN(acc[0][c10]); SBAR();
            acc[1][c10] = MFMA16(wf[i & 3], __builtin_bit_cast(bf16x8, cf[st % 3][2 * ks2 + 1]), acc[1][c10]); PIN(acc[1][c10]);
            if (i + 4 < 20) wf[i & 3] = WFI(i + 4);
            if (st + 2 < 8) { if (i == 1) WLOAD((st + 2) % 3, st + 2); if (i == 10) CLOAD((st + 2) % 3, st + 2); }
            SBAR();
        }
#undef WFI
    }
#undef WLOAD
#undef CLOAD
    wait_bar<0>();
    {
        lds_ptr sp = (lds_ptr)lds + wid * 10240 + lane * 16; lds_cptr rp = (lds_cptr)lds + (wid ^ 4) * 10240 + lane * 16;
        f32x4 keep[10];
        if (kh) {
#pragma unroll
            for (int i = 0; i < 10; ++i) { *(LAS f32x4*)(sp + i * 1024) = acc[i / 5][i % 5]; keep[i] = acc[i / 5][5 + i % 5]; }
        } else {
#pragma unroll
            for (int i = 0; i < 10; ++i) { *(LAS f32x4*)(sp + i * 1024) = acc[i / 5][5 + i % 5]; keep[i] = acc[i / 5][i % 5]; }
        }
        lds_ptr zb = (lds_ptr)lds + 81920;
#pragma unroll
        for (int it = 0; it < 5; ++it) { const int e = tid + it * 512, cl2 = e >> 4, d = e & 15; *(LAS unsigned*)(zb + cl2 * 68 + d * 4) = zr[it]; }
        wait_bar<63>();
#pragma unroll
        for (int i = 0; i < 10; ++i) keep[i] += *(const LAS f32x4*)(rp + i * 1024);
        const int tile = mt * 4 + nt;
        if (rg != ks) {
            const unsigned dst = (unsigned)(WS_SL + (((size_t)tile * 4 + rg) * 4 + ks) * 20480 + kh * 10240) + lane * 16;
#pragma unroll
            for (int i = 0; i < 10; ++i) st16(wr, dst + i * 1024, __builtin_bit_cast(u32x4, keep[i]));
        } else {
            lds_ptr op = (lds_ptr)lds + 98304 + kh * 10240 + lane * 16;
#pragma unroll
            for (int i = 0; i < 10; ++i) *(LAS f32x4*)(op + i * 1024) = keep[i];
        }
        signal_flag(flags + FL_F5 + tile * 4 + ks, tag, tid);
        if (wid == 0) poll_flags(flags, (lane < 4 && lane != ks) ? FL_F5 + tile * 4 + lane : -1, tag);
        wait_bar<63>();
        u32x4 pf[3][3];
#pragma unroll
        for (int k = 0; k < 3; ++k) { const int i = wid + 8 * k < 20 ? wid + 8 * k : wid;
            const unsigned src = (unsigned)(WS_SL + ((size_t)tile * 4 + ks) * 4 * 20480) + i * 1024 + lane * 16;
#pragma unroll
            for (int f = 0; f < 3; ++f) pf[k][f] = ld16c(wr, src + (unsigned)(f + (f >= ks ? 1 : 0)) * 20480u); }
#pragma unroll
        for (int k = 0; k < 3; ++k) if (wid + 8 * k < 20) {
            const int i = wid + 8 * k, tl = 16 * ((i % 10) / 5) + j, cl = 80 * (i / 10) + 16 * (i % 5) + 4 * g;
            f32x4 av = *(const LAS f32x4*)((lds_cptr)lds + 98304 + i * 1024 + lane * 16);
#pragma unroll
            for (int f = 0; f < 3; ++f) av += __builtin_bit_cast(f32x4, pf[k][f]);
            float r4[4];
#pragma unroll
            for (int e = 0; e < 4; ++e) { const float z = bf2f(*(const LAS u16*)((lds_cptr)lds + 81920 + (cl + e) * 68 + tl * 2));
                const float xe = (e & 1) ? bhi(xv[k][e >> 1]) : blo(xv[k][e >> 1]);
                r4[e] = gt[k] * av[e] + (1.f - gt[k]) * (xe * z); }
            *(u32x2*)(a.out + (size_t)(mt * 128 + ks * 32 + tl) * M + nt * 160 + cl) = (u32x2){cvtpk(r4[0], r4[1]), cvtpk(r4[2], r4[3])};
        }
    }
}

__global__ __launch_bounds__(512, 2) void k_fused(const u16* x, const u16* ckv, const u16* wq, const u16* wuk, const u16* wuv, const u16* wo, const u16* bk, const u16* bv,
                                                  const u16* shk, const u16* ssmk, const u16* ssmD, const u16* gw, u16* out, char* ws) {
    extern __shared__ __attribute__((aligned(16))) char lds[];
    const Args a = {x, ckv, wq, wuk, wuv, wo, bk, bv, shk, ssmk, ssmD, gw, out, ws};
    const int tid = threadIdx.x, lane = tid & 63, wid = rfl(tid >> 6);
    const int b = blockIdx.x, xcd = b & 7, idx = b >> 3, h = xcd * 4 + (idx >> 3), r = idx & 7;
    const unsigned lds0 = (unsigned)(uintptr_t)lds;
    const rsrc_t wr = __builtin_amdgcn_make_buffer_rsrc(ws, 0, (int)WS_END, 0x00020000);
    GAS u64* flags = (GAS u64*)(ws + WS_F);
    STAMP(0);
    const P1Ctx c1 = p1_setup(a, h, r, lds0, wid, lane);
    const u64 tag = *(const __attribute__((address_space(4))) u64*)(ws + WS_F) + 1;
    if (b >= 160) {
        u32x4 xg[3][2];
        const u32x4 g0 = *(const u32x4*)(gw + lane * 8), g1 = *(const u32x4*)(gw + 512 + (lane & 15) * 8);
#pragma unroll
        for (int k = 0; k < 3; ++k) { int jb = b - 160 + 96 * k; if (jb >= 256) jb = b - 160; const u16* xr = x + (size_t)(64 * (jb >> 3) + 8 * (jb & 7) + wid) * M;
            xg[k][0] = *(const u32x4*)(xr + lane * 8); xg[k][1] = *(const u32x4*)(xr + 512 + (lane & 15) * 8); }
        p0a_load(a, b - 160, lds0, wid, lane);
        p1_prime_t(c1);
        STAMP(11);
        wait_bar<4>();
#pragma unroll
        for (int k = 0; k < 3; ++k) { PIN(xg[k][0]); PIN(xg[k][1]); }
        STAMP(12);
        p0a_compute(b - 160, lds, tid);
        wait_bar<63>();
        STAMP(13);
        p1_prime_w(c1);
        p0a_store(wr, b - 160, lds, tid);
#pragma unroll
        for (int k = 0; k < 3; ++k) { const int jb = b - 160 + 96 * k; float sum = 0.f, s1 = 0.f;
#pragma unroll
            for (int e = 0; e < 4; ++e) { sum += blo(xg[k][0][e]) * blo(g0[e]) + bhi(xg[k][0][e]) * bhi(g0[e]); s1 += blo(xg[k][1][e]) * blo(g1[e]) + bhi(xg[k][1][e]) * bhi(g1[e]); }
            if (lane < 16) sum += s1;
            sum = wave_sum(sum);
            if (lane == 0 && jb < 256) st4(wr, (unsigned)(WS_G + (64 * (jb >> 3) + 8 * (jb & 7) + wid) * 4), __float_as_uint(1.f / (1.f + __expf(-sum)))); }
    } else { p1_prime_t(c1); p1_prime_w(c1); }
    STAMP(1);
    if (wid < 4) p1_body<0>(a, c1, wr, b, h, r, lds, wid, lane, tid, flags, tag); else p1_body<1>(a, c1, wr, b, h, r, lds, wid, lane, tid, flags, tag);
    STAMP(2);
    wait_bar<63>();
    p0b_finish(wr, b, lds, wid, lane);
    STAMP(14);
    if (b < 160) p4_body(a, wr, b, h, r, lds, wid, lane, tid, flags, tag);
    wait_bar<63>();
    STAMP(8);
    p2_body(a, b, h, r, 15 - r, r, lds, wid, lane, tid, flags, tag);
    STAMP(3);
    p3_body(wr, ws, h, 15 - r, r, lds, wid, lane, tid, flags, tag);
    {
        const int ks = xcd >> 1, mt = (xcd & 1) * 8 + (idx >> 2), nt = idx & 3;
        if (wid == 0) poll_flags(flags, lane < 16 ? FL_FW + (2 * nt + (lane >> 3)) * 32 + 8 * ks + (lane & 7) : -1, tag);
        wait_bar<0>();
        STAMP(9);
        p5_body(a, wr, mt, nt, ks, lds, wid, lane, tid, flags, tag);
    }
    if (b == 0 && tid == 0) flag_store(flags, tag);
    STAMP(10);
}

extern "C" void kernel_launch(void* const* d_in, const int* in_sizes, int n_in,
                              void* d_out, int out_size, void* d_ws, size_t ws_size,
                              hipStream_t stream) {
    static bool attr = false;
    if (!attr) { (void)hipFuncSetAttribute((const void*)k_fused, hipFuncAttributeMaxDynamicSharedMemorySize, LDS_BYTES); attr = true; }
    k_fused<<<256, 512, LDS_BYTES, stream>>>((const u16*)d_in[0], (const u16*)d_in[1], (const u16*)d_in[2], (const u16*)d_in[3], (const u16*)d_in[4], (const u16*)d_in[5],
                                             (const u16*)d_in[7], (const u16*)d_in[8], (const u16*)d_in[9], (const u16*)d_in[10], (const u16*)d_in[11], (const u16*)d_in[12],
                                             (u16*)d_out, (char*)d_ws);
}
```

```cpp
#include <hip/hip_runtime.h>
#include <hip/hip_bf16.h>
#include <stdint.h>

typedef unsigned short u16;
typedef unsigned long long u64;
typedef short bf16x8 __attribute__((ext_vector_type(8)));
typedef short s16x4 __attribute__((ext_vector_type(4)));
typedef float f32x16 __attribute__((ext_vector_type(16)));
typedef float f32x4 __attribute__((ext_vector_type(4)));
typedef float f32x2 __attribute__((ext_vector_type(2)));
typedef __bf16 bf16x2_t __attribute__((ext_vector_type(2)));
typedef unsigned u32x4 __attribute__((ext_vector_type(4)));
typedef unsigned u32x2 __attribute__((ext_vector_type(2)));
typedef short v4i16_t __attribute__((ext_vector_type(4)));
#define LAS __attribute__((address_space(3)))
#define GAS __attribute__((address_space(1)))
typedef LAS const char* lds_cptr;
typedef LAS char* lds_ptr;
#define DI __device__ __forceinline__
#define SBAR() __builtin_amdgcn_sched_barrier(0)
#define PIN(x) asm volatile("" : "+v"(x))
#define PINS(x) asm volatile("" : "+s"(x))
#define MFMA32(a, b, c) __builtin_amdgcn_mfma_f32_32x32x16_bf16(a, b, c, 0, 0, 0)
#define MFMA16(a, b, c) __builtin_amdgcn_mfma_f32_16x16x32_bf16(a, b, c, 0, 0, 0)
#define LGKM(n) __builtin_amdgcn_s_waitcnt(0xC07F | ((n) << 8))
#define EXP2(v) __builtin_amdgcn_exp2f(v)

constexpr int S = 2048, H = 32, D = 128, M = 640, DC = 512, HD = H * D;
constexpr int LDS_BYTES = 163840;
constexpr size_t WS_K = 0, WS_V = (size_t)S * HD * 2, WS_CTX = 2 * WS_V, WS_UR = 3 * WS_V, WS_Z = WS_UR + (size_t)S * M * 2, WS_WO = WS_Z + (size_t)S * M * 2,
                 WS_G = WS_WO + (size_t)HD * M * 2, WS_F = WS_G + 8192, WS_SL = WS_F + 32768, WS_END = WS_SL + (size_t)64 * 16 * 20480;
constexpr int FL_F0 = 16, FL_F1 = FL_F0 + 256, FL_F3 = FL_F1 + 256, FL_F4 = FL_F3 + 512, FL_FW = FL_F4 + 160, FL_F5 = FL_FW + 256;

#define STAMP(i)

DI float bf2f(u16 b) { return __uint_as_float(((unsigned)b) << 16); }
DI unsigned cvtpk(float lo, float hi) { f32x2 v = {lo, hi}; bf16x2_t b = __builtin_convertvector(v, bf16x2_t); return __builtin_bit_cast(unsigned, b); }
DI u16 f2bf(float f) { return (u16)cvtpk(f, f); }
DI float rb(float f) { return __uint_as_float(cvtpk(f, f) << 16); }
DI unsigned rfl(unsigned x) { return __builtin_amdgcn_readfirstlane(x); }

#define SETM0(base, imm) asm volatile("s_add_u32 m0, %0, %1" :: "s"(base), "i"(imm) : "scc")
#define GLDS(voff, sbase, imm) asm volatile("global_load_lds_dwordx4 %0, %1 offset:%c2" :: "v"(voff), "s"(sbase), "i"(imm) : "memory")
#define GLDSC(voff, sbase, imm) asm volatile("global_load_lds_dwordx4 %0, %1 offset:%c2 sc1" :: "v"(voff), "s"(sbase), "i"(imm) : "memory")
DI void dma1(unsigned ldsaddr, unsigned voff, const void* sbase) {
    asm volatile("s_mov_b32 m0, %0\n\ts_nop 0\n\tglobal_load_lds_dwordx4 %1, %2" :: "s"(ldsaddr), "v"(voff), "s"(sbase) : "memory");
}
DI void dma2(unsigned ldsaddr, unsigned voff, const void* sbase) {
    asm volatile("s_mov_b32 m0, %0\n\ts_nop 0\n\tglobal_load_lds_dwordx4 %1, %2\n\tglobal_load_lds_dwordx4 %1, %2 offset:1024" :: "s"(ldsaddr), "v"(voff), "s"(sbase) : "memory");
}
DI void dma1c(unsigned ldsaddr, unsigned voff, const void* sbase) {
    asm volatile("s_mov_b32 m0, %0\n\ts_nop 0\n\tglobal_load_lds_dwordx4 %1, %2 sc1" :: "s"(ldsaddr), "v"(voff), "s"(sbase) : "memory");
}
DI void dma2c(unsigned ldsaddr, unsigned voff, const void* sbase) {
    asm volatile("s_mov_b32 m0, %0\n\ts_nop 0\n\tglobal_load_lds_dwordx4 %1, %2 sc1\n\tglobal_load_lds_dwordx4 %1, %2 offset:1024 sc1" :: "s"(ldsaddr), "v"(voff), "s"(sbase) : "memory");
}
template <int N> DI void wait_bar_v() { asm volatile("s_waitcnt vmcnt(%0)\n\ts_barrier" :: "i"(N) : "memory"); }
template <int N> DI void wait_bar() { asm volatile("s_waitcnt vmcnt(%0) lgkmcnt(0)\n\ts_barrier" :: "i"(N) : "memory"); }
DI bf16x8 lds_frag(lds_cptr p) { return *(const LAS bf16x8*)p; }
DI s16x4 vtr(lds_cptr p) { return __builtin_bit_cast(s16x4, __builtin_amdgcn_ds_read_tr16_b64_v4i16((LAS v4i16_t*)p)); }
DI bf16x8 tr_frag(lds_cptr p, int second) { const s16x4 lo = vtr(p), hi = vtr(p + second); return __builtin_shufflevector(lo, hi, 0, 1, 2, 3, 4, 5, 6, 7); }
DI u32x4 pack8(const f32x16& x, int s) {
    u32x4 w; w[0] = cvtpk(x[8 * s + 0], x[8 * s + 1]); w[1] = cvtpk(x[8 * s + 2], x[8 * s + 3]); w[2] = cvtpk(x[8 * s + 4], x[8 * s + 5]); w[3] = cvtpk(x[8 * s + 6], x[8 * s + 7]); return w;
}
typedef __amdgpu_buffer_rsrc_t rsrc_t;
DI void st16(rsrc_t r, unsigned off, u32x4 v) { __builtin_amdgcn_raw_buffer_store_b128(v, r, off, 0, 16); }
DI void st8(rsrc_t r, unsigned off, u32x2 v) { __builtin_amdgcn_raw_buffer_store_b64(v, r, off, 0, 16); }
DI void st4(rsrc_t r, unsigned off, unsigned v) { __builtin_amdgcn_raw_buffer_store_b32(v, r, off, 0, 16); }
DI void signal_flag(GAS u64* flag, u64 tag, int tid) {
    asm volatile("s_waitcnt vmcnt(0) lgkmcnt(0)\n\ts_barrier" ::: "memory");
    if (tid == 0) __hip_atomic_store(flag, tag, __ATOMIC_RELAXED, __HIP_MEMORY_SCOPE_AGENT);
}
DI void poll_flags(GAS u64* flags, int idx, u64 tag) {
    for (unsigned spins = 0; spins < (1u << 16); ++spins) {
        const u64 v = idx >= 0 ? __hip_atomic_load(flags + idx, __ATOMIC_RELAXED, __HIP_MEMORY_SCOPE_AGENT) : tag;
        if (__all(v == tag)) break;
        __builtin_amdgcn_s_sleep(2);
    }
}
DI unsigned ld4c(rsrc_t r, unsigned off) { return __builtin_amdgcn_raw_buffer_load_b32(r, off, 0, 16); }
DI void flag_store(GAS u64* flag, u64 tag) { __hip_atomic_store(flag, tag, __ATOMIC_RELAXED, __HIP_MEMORY_SCOPE_AGENT); }

struct Args { const u16 *x, *ckv, *wq, *wuk, *wuv, *wo, *bk, *bv, *shk, *ssmk, *ssmD, *gw; u16* out; char* ws; };

constexpr int P0_OFF = 133120;
DI float blo(unsigned w) { return __uint_as_float(w << 16); }
DI float bhi(unsigned w) { return __uint_as_float(w & 0xffff0000u); }
template <int CTRL> DI float dpp_add(float v) { return v + __builtin_bit_cast(float, __builtin_amdgcn_update_dpp(0, __builtin_bit_cast(int, v), CTRL, 0xf, 0xf, true)); }
DI float wave_sum(float v) {
    v = dpp_add<0x128>(v); v = dpp_add<0x124>(v); v = dpp_add<0x122>(v); v = dpp_add<0x121>(v);
    auto a = __builtin_amdgcn_permlane16_swap(__float_as_uint(v), __float_as_uint(v), false, false); v = __uint_as_float(a[0]) + __uint_as_float(a[1]);
    auto b = __builtin_amdgcn_permlane32_swap(__float_as_uint(v), __float_as_uint(v), false, false); return __uint_as_float(b[0]) + __uint_as_float(b[1]);
}
DI int div10(int x) { return (x * 6554) >> 16; }
DI void p0a_load(const Args& a, int jb0, unsigned lds0, int wid, int lane) {
#pragma unroll
    for (int i = 0; i < 9; ++i) {
        const int p = wid + 8 * i;
        if (p < 66) {
            const int k = p >= 44 ? 2 : (p >= 22 ? 1 : 0), q = p - 22 * k;
            int jb = jb0 + 96 * k; if (jb >= 256) jb = jb0;
            const int cb = jb & 7, tb = jb >> 3;
            if (q < 11) { const int ci = q * 64 + lane, cj = ci < 670 ? ci : 669, row = div10(cj); int t = tb * 64 - 3 + row; t = t < 0 ? 0 : t;
                dma1(lds0 + p * 1024, (unsigned)(t * 1280 + (cj - 10 * row) * 16), (const char*)a.bk + cb * 160); }
            else if (q < 21) { const int ci = (q - 11) * 64 + lane, row = div10(ci);
                dma1(lds0 + p * 1024, (unsigned)(row * 1280 + (ci - 10 * row) * 16), (const char*)a.bv + (size_t)tb * 64 * 1280 + cb * 160); }
            else dma1(lds0 + p * 1024, (unsigned)((lane < 40 ? lane : 39) * 16), (const char*)a.shk + cb * 640);
        }
    }
}
DI void p0a_compute(int jb0, char* lds, int tid) {
    const int nj = jb0 + 192 < 256 ? 3 : 2;
#pragma unroll
    for (int it = 0; it < 2; ++it) {
        const int item = tid + it * 512, k = item >= 640 ? 2 : (item >= 320 ? 1 : 0), e = item - k * 320, tp = div10(e), cc = e - 10 * tp;
        if (k < nj && item < 960) {
            const int t0 = ((jb0 + 96 * k) >> 3) * 64;
            const lds_cptr in = (lds_cptr)lds + k * 22528;
            LAS char* tile = (LAS char*)lds + P0_OFF + k * 10240;
            u32x4 kk[5], vv[2], sh[4];
#pragma unroll
            for (int i = 0; i < 5; ++i) kk[i] = *(const LAS u32x4*)(in + (2 * tp + i) * 160 + cc * 16);
#pragma unroll
            for (int i = 0; i < 2; ++i) vv[i] = *(const LAS u32x4*)(in + 11264 + (2 * tp + i) * 160 + cc * 16);
#pragma unroll
            for (int i = 0; i < 4; ++i) sh[i] = *(const LAS u32x4*)(in + 21504 + cc * 64 + i * 16);
            if (t0 + 2 * tp < 3) {
#pragma unroll
                for (int i = 0; i < 3; ++i) if (t0 + 2 * tp + i - 3 < 0) kk[i] = (u32x4){0u, 0u, 0u, 0u};
            }
            const int dw = 31 - tp, pos = (((dw >> 2) ^ (cc & 7)) << 4) + (dw & 3) * 4;
#pragma unroll
            for (int qp = 0; qp < 4; ++qp) {
                f32x2 kx[5], r2[2];
#pragma unroll
                for (int i = 0; i < 5; ++i) kx[i] = (f32x2){blo(kk[i][qp]), bhi(kk[i][qp])};
                const f32x2 t0_ = {blo(sh[qp][0]), blo(sh[qp][2])}, t1_ = {bhi(sh[qp][0]), bhi(sh[qp][2])}, t2_ = {blo(sh[qp][1]), blo(sh[qp][3])}, t3_ = {bhi(sh[qp][1]), bhi(sh[qp][3])};
#pragma unroll
                for (int r = 0; r < 2; ++r)
                    r2[r] = (kx[r] * t3_ + kx[r + 1] * t2_ + kx[r + 2] * t1_ + kx[r + 3] * t0_) * (f32x2){blo(vv[r][qp]), bhi(vv[r][qp])};
                *(LAS unsigned*)(tile + (8 * cc + 2 * qp) * 128 + pos) = cvtpk(r2[1][0], r2[0][0]);
                *(LAS unsigned*)(tile + (8 * cc + 2 * qp + 1) * 128 + pos) = cvtpk(r2[1][1], r2[0][1]);
            }
        }
    }
}
DI void p0a_store(rsrc_t wr, int jb0, char* lds, int tid) {
    const int nj = jb0 + 192 < 256 ? 3 : 2;
#pragma unroll
    for (int it = 0; it < 4; ++it) { const int e = tid + it * 512, k = e >= 1280 ? 2 : (e >= 640 ? 1 : 0), f = e - k * 640, cl = f >> 3, d = f & 7;
        if (k < nj && e < 1920) { const int jb = jb0 + 96 * k;
            st16(wr, (unsigned)(WS_UR + ((size_t)((jb & 7) * 80 + cl) * S + (2047 - (jb >> 3) * 64 - 63) + 8 * d) * 2),
                 *(const LAS u32x4*)((lds_cptr)lds + P0_OFF + k * 10240 + cl * 128 + ((d ^ ((cl >> 3) & 7)) << 4))); } }
}
DI void p0b_load(const Args& a, int b, unsigned lds0, int wid, int lane) {
    const int cb = b & 7, tb = b >> 3;
#pragma unroll
    for (int i = 0; i < 3; ++i) { const int p = wid + 8 * i;
        if (p < 20) { const int ci = p * 64 + lane, row = div10(ci);
            dma1(lds0 + P0_OFF + p * 1024, (unsigned)(row * 1280 + (ci - 10 * row) * 16), (const char*)a.wo + (size_t)tb * 128 * 1280 + cb * 160); }
    }
}
DI void p0b_finish(rsrc_t wr, int b, char* lds, int wid, int lane) {
    const int cb = b & 7, tb = b >> 3;
    const int j = lane & 15, g = lane >> 4, q = j >> 2, p = lane & 3;
    const lds_cptr brd = (lds_cptr)lds + P0_OFF + (16 * (g >> 1) + 4 * (g & 1) + q) * 160 + p * 8;
#pragma unroll
    for (int i = 0; i < 3; ++i) if (wid + 8 * i < 20) { const int f = wid + 8 * i, kq = f / 5, cbk = f % 5;
        const bf16x8 w = tr_frag(brd + kq * 32 * 160 + cbk * 32, 1280);
        st16(wr, (unsigned)(WS_WO + ((size_t)(cb * 32 + tb) * 20 + f) * 1024 + lane * 16), __builtin_bit_cast(u32x4, w)); }
}

#define PF_TILE 6
constexpr int TSLOT = 32 * 1040;
struct P1Ctx { const char* wsrc; const char* tsrc; unsigned wvoff, wlds, tvoff, tlds; };
DI P1Ctx p1_setup(const Args& a, int h, int r, unsigned lds0, int wid, int lane) {
    P1Ctx c;
    c.wsrc = (const char*)((wid >> 2) ? a.wuv : a.wuk) + (size_t)h * 256 + ((wid >> 1) & 1) * 128 + (size_t)(wid & 1) * 4 * 8 * 8192;
    c.wvoff = (unsigned)(lane >> 3) * 8192u + (unsigned)(((lane & 7) ^ (((lane >> 4) & 1) << 2)) * 16);
    c.wlds = lds0 + ((wid >> 2) * 2 + ((wid >> 1) & 1)) * 8192 + (wid & 1) * 4 * 1024;
    c.tsrc = (const char*)a.ckv + (size_t)(256 * r + 4 * wid) * 1024;
    c.tvoff = lane * 16; c.tlds = lds0 + 4 * wid * 1040;
    return c;
}
DI void p1_dma_w1(const P1Ctx& c, int ch, int slot, int i) { dma1(c.wlds + slot * TSLOT + i * 1024, c.wvoff, c.wsrc + (size_t)ch * 64 * 8192 + (size_t)i * 8 * 8192); }
template <int J> DI void p1_dma_t1(const P1Ctx& c, int t, int slot) {
    asm volatile("s_mov_b32 m0, %0\n\ts_nop 0\n\tglobal_load_lds_dwordx4 %1, %2 offset:%c3" :: "s"(c.tlds + slot * TSLOT + 16 * J), "v"(c.tvoff), "s"(c.tsrc + (size_t)t * 32 * 1024), "i"(J * 1024) : "memory");
}
DI void p1_prime_t(const P1Ctx& c) { p1_dma_t1<0>(c, 0, 3); p1_dma_t1<1>(c, 0, 3); p1_dma_t1<2>(c, 0, 3); p1_dma_t1<3>(c, 0, 3); }
DI void p1_prime_w(const P1Ctx& c) {
#pragma unroll
    for (int ch = 0; ch < 3; ++ch)
#pragma unroll
        for (int i = 0; i < 4; ++i) p1_dma_w1(c, ch, ch, i);
}
constexpr int STG_UR = 3 * TSLOT, STG_H = STG_UR + 16384;
template <int MAT>
DI void p1_body(const Args& a, const P1Ctx& c, rsrc_t wr, int b, int h, int r, char* lds, int wid, int lane, int tid, GAS u64* flags, u64 tag) {
    const int g4 = lane >> 4, q = (lane & 15) >> 2, p = lane & 3;
    const lds_cptr wrd = (lds_cptr)lds + (MAT * 2 + ((wid >> 1) & 1)) * 8192 + (8 * (g4 >> 1) + q) * 128 + (((wid & 1) ^ (q >> 1)) * 64) + (g4 & 1) * 32 + p * 8;
    const lds_cptr trd = (lds_cptr)lds + (lane & 31) * 1040 + (lane >> 5) * 16;
    const unsigned dst = (unsigned)((MAT ? WS_V : WS_K) + ((size_t)(h * 64 + 8 * r) * 8 + 2 * (wid & 3)) * 1024) + lane * 16;
    unsigned pfd = 0;
    bf16x8 Wf[32];
    f32x16 acc = f32x16{};
    const lds_cptr t0p = trd + 3 * TSLOT;
#define P1M(A_, W_) (MAT == 0 ? MFMA32(W_, A_, acc) : MFMA32(A_, W_, acc))
#pragma unroll
    for (int ch = 0; ch < 8; ++ch) {
        if (ch == 0) wait_bar<8>(); else wait_bar<4>();
        if (ch == 3 && b >= 160 && tid < 3) { const int jb = b - 160 + 96 * tid; if (jb < 256) flag_store(flags + FL_F0 + (jb & 7) * 32 + (jb >> 3), tag); }
        const lds_cptr wb = wrd + (ch % 3) * TSLOT;
        bf16x8 af[4];
#pragma unroll
        for (int s = 0; s < 4; ++s) { Wf[4 * ch + s] = tr_frag(wb + s * 2048, 512); af[s] = lds_frag(t0p + (4 * ch + s) * 32); }
        SBAR();
#pragma unroll
        for (int s = 0; s < 4; ++s) {
            if (ch >= 1) { if (ch + 2 <= 7) p1_dma_w1(c, ch + 2, (ch + 2) % 3, s);
                           else { if (s == 0) p1_dma_t1<0>(c, ch - 5, (ch + 2) % 3); if (s == 1) p1_dma_t1<1>(c, ch - 5, (ch + 2) % 3); if (s == 2) p1_dma_t1<2>(c, ch - 5, (ch + 2) % 3); if (s == 3) p1_dma_t1<3>(c, ch - 5, (ch + 2) % 3); } }
            acc = P1M(af[s], Wf[4 * ch + s]); PIN(acc);
            SBAR();
        }
    }
    f32x16 accp = acc;
    bf16x8 af[4];
    wait_bar<4>();
#pragma unroll
    for (int s = 0; s < 4; ++s) af[s] = lds_frag(trd + (8 % 3) * TSLOT + s * 32);
    SBAR();
#pragma unroll
    for (int i = 1; i < 8; ++i) {
        const int j = 7 + i;
        const lds_cptr tb = trd + (j % 3) * TSLOT, tn = trd + ((j + 1) % 3) * TSLOT;
#pragma unroll
        for (int s = 0; s < 32; ++s) {
            if (s == 28 && i < 7) {
                if (b < 160 && i == 4 && wid == 0) poll_flags(flags, lane < 32 ? FL_F0 + (b / 20) * 32 + lane : -1, tag);
                if (i + 2 <= 7) wait_bar<6>(); else wait_bar<2>();
                const unsigned lds0 = (unsigned)(uintptr_t)lds;
                if (i == 1) p0b_load(a, b, lds0, wid, lane);
                if (i == PF_TILE) {
                    const int id = (b * 8 + wid) * 60 + (lane < 59 ? lane : 59);
                    const char* p = id < 40960 ? (const char*)a.x + (size_t)id * 64 : (const char*)a.wq + (size_t)(id - 40960) * 64;
                    asm volatile("global_load_dword %0, %1, off" : "+v"(pfd) : "v"(p) : "memory"); }
                if (b < 160) {
                    if (i == 1) dma2(lds0 + STG_H + wid * 2048, lane * 16, (const char*)a.ssmk + (size_t)(4 * b) * 4096 + wid * 2048);
                    if (i == 4) dma2c(lds0 + STG_UR + wid * 2048, lane * 16, a.ws + WS_UR + (size_t)(4 * b) * 4096 + wid * 2048);
                }
                SBAR();
            }
            if (!(s & 1) && s + 4 < 32) { LGKM(2); SBAR(); }
            if (s == 0) acc = MAT == 0 ? MFMA32(Wf[0], af[0], (f32x16{})) : MFMA32(af[0], Wf[0], (f32x16{})); else acc = P1M(af[s & 3], Wf[s]);
            PIN(acc);
            if (s + 4 < 32) af[s & 3] = lds_frag(tb + (s + 4) * 32); else if (i < 7) af[s & 3] = lds_frag(tn + (s - 28) * 32);
            if (s == 2) st16(wr, dst + (i - 1) * 8192, pack8(accp, 0));
            if (s == 4) st16(wr, dst + (i - 1) * 8192 + 1024, pack8(accp, 1));
            if (i + 2 <= 7) {
                if (s == 6) p1_dma_t1<0>(c, i + 2, (j + 2) % 3);
                if (s == 11) p1_dma_t1<1>(c, i + 2, (j + 2) % 3);
                if (s == 16) p1_dma_t1<2>(c, i + 2, (j + 2) % 3);
                if (s == 21) p1_dma_t1<3>(c, i + 2, (j + 2) % 3);
            }
            SBAR();
        }
        accp = acc;
    }
    asm volatile("" : "+v"(pfd));
    st16(wr, dst + 7 * 8192, pack8(accp, 0)); st16(wr, dst + 7 * 8192 + 1024, pack8(accp, 1));
#undef P1M
}

constexpr float QSCALE = 0.08838834764831845f * 1.4426950408889634f;
constexpr float THR = 8.f;
constexpr int QCH = 49152;
constexpr int KRING = 0, VRING = 65536, SPARE = 131072, QA_OFF = VRING + 32768;
#define MX3(a, b, c) __builtin_fmaxf(__builtin_fmaxf((a), (b)), (c))

DI void p2_body(const Args& a, int b, int h, int r, int blkA, int blkB, char* lds, int wid, int lane, int tid, GAS u64* flags, u64 tag) {
    const unsigned lds0 = (unsigned)(uintptr_t)lds;
    const int r32 = lane & 31, hi = lane >> 5, rg = wid & 3, par = wid >> 2;
    const int myblk = par ? blkB : blkA;
    const char* xg = (const char*)a.x + (size_t)(128 * myblk + 32 * rg) * 1280;
    const unsigned xvoff = (unsigned)(lane >> 3) * 1280u + (unsigned)(((lane & 7) ^ ((lane >> 4) & 7)) * 16);
    const unsigned xvoff2 = (unsigned)(lane >> 3) * 1280u + 8 * 1280u + (unsigned)(((lane & 7) ^ ((4 + (lane >> 4)) & 7)) * 16);
    const unsigned xlds = lds0 + 16384 + wid * 4096;
    const char* wg = (const char*)a.wq + (size_t)h * 256 + (wid >> 2) * 128 + (size_t)(wid & 3) * 2 * 8 * 8192;
    const unsigned wvoff = (unsigned)(lane >> 3) * 8192u + (unsigned)(((lane & 7) ^ (((lane >> 4) & 1) << 2)) * 16);
    const unsigned wvoff2 = wvoff + 8 * 8192u;
    const unsigned wlds = lds0 + 16384 + 32768 + (wid >> 2) * 8192 + (wid & 3) * 2048;
    const int g4 = lane >> 4, q = (lane & 15) >> 2, p = lane & 3;
    const int hsw = (q >> 1) & 1, ysw = hi ^ ((r32 >> 1) & 7);
    const lds_cptr wrd0 = (lds_cptr)lds + 16384 + 32768 + (8 * (g4 >> 1) + q) * 128 + (g4 & 1) * 32 + p * 8 + hsw * 64;
    const lds_cptr wrd1 = (lds_cptr)lds + 16384 + 32768 + (8 * (g4 >> 1) + q) * 128 + (g4 & 1) * 32 + p * 8 + (hsw ^ 1) * 64;
    lds_cptr xrd[4];
#pragma unroll
    for (int s = 0; s < 4; ++s) xrd[s] = (lds_cptr)lds + 16384 + wid * 4096 + r32 * 128 + (((2 * s) ^ ysw) << 4);
    f32x16 acc[4] = {};
#pragma unroll
    for (int c = 0; c < 2; ++c) { const char* xp = xg + c * 128; const char* wp = wg + (size_t)c * 64 * 8192; const unsigned so_ = ((c + 2) % 3) * QCH;
        dma1(xlds + so_, xvoff, xp); dma1(xlds + so_ + 1024, xvoff2, xp); dma1(xlds + so_ + 2048, xvoff, xp + 16 * 1280); dma1(xlds + so_ + 3072, xvoff2, xp + 16 * 1280);
        dma1(wlds + so_, wvoff, wp); dma1(wlds + so_ + 1024, wvoff2, wp); }
    unsigned so = 2 * QCH;
    const char* xp = xg + 2 * 128; const char* wp = wg + (size_t)2 * 64 * 8192;
    const unsigned kl0 = lds0 + KRING + wid * 2048, tvoff = lane * 16;
    const char* Kw = a.ws + WS_K + (size_t)h * 64 * 8192 + wid * 2048;
#define WFR(B0, B1, i) tr_frag((((i) & 1) ? (B1) : (B0)) + (((i) >> 1) & 1) * 8192 + ((i) >> 2) * 2048, 512)
    bf16x8 wf[4], xf[2];
#define P2CHUNK(MODE, EXTRA, LAST) do { \
        const unsigned sn = so == 0 ? 2 * QCH : so - QCH, sx = so == 2 * QCH ? 0 : so + QCH;        \
        const unsigned xl_ = xlds + sn, wl_ = wlds + sn; const char* xp2 = xp + 16 * 1280; \
        const lds_cptr wb0 = wrd0 + so, wb1 = wrd1 + so, wn0 = wrd0 + sx, wn1 = wrd1 + sx; \
        _Pragma("unroll") for (int i = 0; i < 16; ++i) { \
            if (i == 12 && !(LAST)) { \
                if ((EXTRA) == 2 && wid == 0) poll_flags(flags, lane < 8 ? FL_F1 + h * 8 + lane : -1, tag); \
                if ((MODE) == 0) wait_bar<6>(); else wait_bar<4>(); \
                if ((EXTRA) == 1 && tid == 0) { flag_store(flags + FL_FW + (b & 7) * 32 + (b >> 3), tag); if (b < 160) flag_store(flags + FL_F4 + b, tag); else flag_store(flags + FL_F1 + h * 8 + r, tag); } \
                SBAR(); } \
            acc[i & 3] = MFMA32(wf[i & 3], xf[(i >> 2) & 1], acc[i & 3]); PIN(acc[i & 3]); \
            if (i + 4 < 16) wf[i & 3] = WFR(wb0, wb1, i + 4); else if (!(LAST)) wf[i & 3] = WFR(wn0, wn1, i - 12); \
            if ((i & 3) == 0 && i + 4 < 16) xf[((i >> 2) + 1) & 1] = lds_frag(xrd[(i >> 2) + 1] + so); \
            if (i == 12 && !(LAST)) xf[0] = lds_frag(xrd[0] + sx); \
            if ((MODE) == 0) { \
                if (i == 0) { SETM0(xl_, 0); } if (i == 1) { GLDS(xvoff, xp, 0); SETM0(xl_, 1024); } \
                if (i == 3) { GLDS(xvoff2, xp, 0); SETM0(xl_, 2048); } if (i == 5) { GLDS(xvoff, xp2, 0); SETM0(xl_, 3072); } \
                if (i == 7) { GLDS(xvoff2, xp2, 0); SETM0(wl_, 0); } if (i == 9) { GLDS(wvoff, wp, 0); SETM0(wl_, 1024); } \
                if (i == 11) { GLDS(wvoff2, wp, 0); } \
            } else { \
                const char* ku_ = Kw + (size_t)(2 * ((MODE) - 1)) * 16384; \
                if (i == 0) { SETM0(kl0, (2 * ((MODE) - 1)) * 16384); } if (i == 1) GLDSC(tvoff, ku_, 0); if (i == 4) GLDSC(tvoff, ku_, 1024); \
                if (i == 6) { SETM0(kl0, (2 * ((MODE) - 1) + 1) * 16384); } if (i == 8) GLDSC(tvoff, ku_ + 16384, 0); if (i == 11) GLDSC(tvoff, ku_ + 16384, 1024); \
            } \
            SBAR(); \
        } \
        so = sx; xp += 128; wp += (size_t)64 * 8192; } while (0)
    wait_bar<6>();
    xf[0] = lds_frag(xrd[0] + so);
#pragma unroll
    for (int i = 0; i < 4; ++i) wf[i] = WFR(wrd0 + so, wrd1 + so, i);
    SBAR();
    P2CHUNK(0, 0, false);
    P2CHUNK(0, 1, false);
#pragma unroll 1
    for (int c = 2; c < 7; ++c) P2CHUNK(0, 0, false);
    P2CHUNK(0, 2, false);
    P2CHUNK(1, 0, false);
    P2CHUNK(2, 0, true);
#undef P2CHUNK
    wait_bar<63>();
    lds_ptr qw = (lds_ptr)lds + (par ? SPARE : QA_OFF) + rg * 8192 + lane * 16;
#pragma unroll
    for (int db = 0; db < 4; ++db) {
#pragma unroll
        for (int e = 0; e < 16; ++e) acc[db][e] *= QSCALE;
        *(LAS u32x4*)(qw + (2 * db) * 1024) = pack8(acc[db], 0);
        *(LAS u32x4*)(qw + (2 * db + 1) * 1024) = pack8(acc[db], 1);
    }
#undef WFR
}

DI void p3_body(rsrc_t wr, const char* ws, int h, int blkA, int blkB, char* lds, int wid, int lane, int tid, GAS u64* flags, u64 tag) {
    const unsigned lds0 = (unsigned)(uintptr_t)lds;
    const int r32 = lane & 31, hi = lane >> 5, rg = wid & 3, par = wid >> 2;
    const unsigned tvoff = lane * 16;
    const unsigned kl0 = lds0 + KRING + wid * 2048, vl0 = lds0 + VRING + wid * 2048;
    const char* Kw = ws + WS_K + (size_t)h * 64 * 8192 + wid * 2048; const char* Vw = ws + WS_V + (size_t)h * 64 * 8192 + wid * 2048;
    const lds_cptr krd = (lds_cptr)lds + KRING + par * 8192 + lane * 16;
    const lds_cptr vrd = (lds_cptr)lds + VRING + par * 8192 + lane * 16;
    int g0 = 0;
    bf16x8 qf[8];
#define VF(i) ((((i) & 3) * 2 + ((i) >> 2)) * 1024)
#define MFMA_C(D, A, B, C) asm volatile("v_mfma_f32_32x32x16_bf16 %0, %2, %3, %1" : "=&v"(D), "+v"(C) : "v"(A), "v"(B))
#define SLOT(X, Y, PN, PP, ODD, QK, SM, PV, FIRST, DM, PFK, MASK) do { \
        unsigned long long fire = 0; \
        _Pragma("unroll") for (int i = 0; i < 16; ++i) { \
            if (i < 8 && QK && !(i & 1)) { LGKM(2); SBAR(); } \
            if (i >= 8 && PV && !(i & 1)) { LGKM(2); SBAR(); } \
            if (i < 8) { if (QK) { if (i == 0) { if (FIRST) X = MFMA32(kf[0], qf[0], zero16); else MFMA_C(X, kf[0], qf[0], negm); } else X = MFMA32(kf[i & 3], qf[i], X); PIN(X); \
                                   if (i < 4) kf[i] = lds_frag(kp + ((ODD) ? 0 : 16384) + (i + 4) * 1024); } \
                         if (PV && i >= 4) vf[i - 4] = lds_frag(vp + ((ODD) ? 0 : 16384) + VF(i - 4)); } \
            else { if (PV) { o[i & 3] = MFMA32(vf[i & 3], __builtin_bit_cast(bf16x8, PP[(i - 8) >> 2]), o[i & 3]); PIN(o[i & 3]); if (i < 12) vf[i & 3] = lds_frag(vp + ((ODD) ? 0 : 16384) + VF(i - 4)); } \
                   if (PFK && i >= 12) kf[i - 12] = lds_frag(kp + ((ODD) ? 16384 : 0) + (i - 12) * 1024); } \
            if (!(ODD) && i == 5) { kp = (lds_cptr)(uintptr_t)((unsigned)(uintptr_t)kp ^ 0x8000u); PIN(kp); } \
            if (!(ODD) && i == 13) { vp = (lds_cptr)(uintptr_t)((unsigned)(uintptr_t)vp ^ 0x8000u); PIN(vp); } \
            if (DM) { \
                if (i == 0) SETM0(dk, (ODD) ? 16384 : 0); \
                if (i == 1) GLDSC(uoff, Kw, 0); \
                if (i == 3) GLDSC(uoff, Kw, 1024); \
                if ((DM) & 2) { \
                    if (i == 4) SETM0(dv, (ODD) ? 16384 : 0); \
                    if (i == 5) GLDSC(uoffv, Vw, 0); \
                    if (i == 7) GLDSC(uoffv, Vw, 1024); \
                    if (i == 11) { uoffv += 16384u; PIN(uoffv); } } \
                if (i == 8) { uoff += (j + 5 == n) ? 16384u * (unsigned)(1 - n) : 16384u; PIN(uoff); } \
                if ((ODD) && i == 9) { dk ^= 0x8000u; dv ^= 0x8000u; PINS(dk); PINS(dv); } } \
            if (SM) { \
                if (i == 0) { Y[0] = EXP2(Y[0]); Y[1] = EXP2(Y[1]); } else if (i < 15) Y[i + 1] = EXP2(Y[i + 1]); \
                if (i == 1) l2 += Y[0]; \
                if (i >= 1) { if (i & 1) l += Y[i]; else l2 += Y[i]; PIN(l); PIN(l2); } \
                if (i & 1) { PN[i >> 3][(i >> 1) & 3] = cvtpk(Y[i - 1], Y[i]); PIN(PN[i >> 3]); } \
                if (i < 15) PIN(Y); } \
            if (QK) { \
                if (MASK && i == 9 && (MASK == 1 || j + 1 >= n - 2)) { int qq = qrel - 64 * (j + 1 - (n - 2)); PIN(qq); \
                    _Pragma("unroll") for (int r = 0; r < 16; ++r) { const int c = (r & 3) + 8 * (r >> 2); if (c > qq) X[r] = -INFINITY; } } \
                if (i == 9) { mxa = MX3(X[0], X[1], X[2]); PIN(mxa); } \
                if (i == 10) { mxb = MX3(X[3], X[4], X[5]); mxa = MX3(mxa, X[6], X[7]); PIN(mxb); PIN(mxa); } \
                if (i == 11) { mxb = MX3(mxb, X[8], X[9]); PIN(mxb); } \
                if (i == 12) { mxa = MX3(mxa, X[10], X[11]); mxb = MX3(mxb, X[12], X[13]); PIN(mxb); PIN(mxa); } \
                if (i == 13) { mxa = MX3(mxa, X[14], X[15]); PIN(mxa); } \
                if (i == 14) { mxa = MX3(mxa, mxb, X[0]); PIN(mxa); } \
                if (i == 15 && !(FIRST)) { fire = __ballot(mxa > THR); PINS(fire); } } \
            SBAR(); \
        } \
        if (__builtin_expect((fire | pend) != 0, 0) || FIRST) { \
            if (PV && pend) { _Pragma("unroll") for (int d = 0; d < 4; ++d) _Pragma("unroll") for (int r = 0; r < 16; ++r) o[d][r] *= alp; } \
            pend = 0; \
            if (QK && (FIRST || fire)) { \
                auto rr = __builtin_amdgcn_permlane32_swap(__float_as_uint(mxa), __float_as_uint(mxa), false, false); \
                const float rm = __builtin_fmaxf(__uint_as_float(rr[0]), __uint_as_float(rr[1])); \
                if (FIRST) { const float dl = __builtin_fmaxf(rm, -1e30f); m = dl; \
                    _Pragma("unroll") for (int r = 0; r < 16; ++r) { X[r] -= dl; negm[r] = -dl; } PIN(negm); } \
                else { const float dl = __builtin_fmaxf(rm, 0.f); m += dl; alp = EXP2(-dl); l *= alp; l2 *= alp; pend = 1; \
                    const float ndl = -dl, aone = hi ? 0.f : 1.f; \
                    asm volatile("s_nop 1\n\tv_mfma_f32_32x32x2_f32 %0, %2, %3, %0\n\tv_mfma_f32_32x32x2_f32 %1, %2, %3, %1\n\ts_nop 7\n\ts_nop 7\n\ts_nop 7" : "+v"(X), "+v"(negm) : "v"(aone), "v"(ndl)); } } } \
        SBAR(); \
        } while (0)
#define TBAR() wait_bar_v<4>()
    const f32x16 zero16 = {};
#pragma unroll 1
    for (int pass = 0; pass < 2; ++pass) {
        const int blk = pass ? blkB : blkA, n = 2 * (blk + 1);
        dma2c(vl0 + (g0 & 3) * 16384, tvoff, Vw); dma2c(vl0 + ((g0 + 1) & 3) * 16384, tvoff, Vw + 16384);
        if (pass == 0) wait_bar<4>();
        if (pass == 0) {
            const lds_cptr qr = (lds_cptr)lds + QA_OFF + rg * 8192 + lane * 16;
#pragma unroll
            for (int s = 0; s < 8; ++s) qf[s] = lds_frag(qr + s * 1024);
        }
        float m = -1e30f, l = 0.f, l2 = 0.f, alp = 1.f, mxa = 0.f, mxb = 0.f; unsigned long long pend = 0; f32x16 o[4] = {};
        u32x4 pk0[2] = {}, pk1[2] = {};
        f32x16 s0, s1, negm;
        bf16x8 kf[4], vf[4] = {};
        const int qrel = 32 * rg + r32 - 32 * par - 4 * hi;
        lds_cptr kp = krd + (g0 & 2) * 16384, vp = vrd + ((g0 & 2) ^ 2) * 16384;
        unsigned dk = kl0 + (g0 & 2) * 16384, dv = vl0 + ((g0 + 2) & 2) * 16384;
        unsigned uoffv = tvoff + 2 * 16384;
        unsigned uoff = tvoff + ((4 < n) ? 4 : 4 - n) * 16384;
#pragma unroll
        for (int i = 0; i < 4; ++i) kf[i] = lds_frag(kp + i * 1024);
        int j = -1;
        SLOT(s0, s1, pk1, pk0, true, true, false, false, true, false, true, 2);
        wait_bar_v<63>();
        j = 0;
        SLOT(s1, s0, pk0, pk1, false, true, true, false, false, 3, true, 2);
        TBAR();
        if (pass == 1 && tid == 0) flag_store(flags + FL_F3 + blkA * 32 + h, tag);
#pragma unroll 1
        for (j = 1; j < n - 3; j += 2) {
            SLOT(s0, s1, pk1, pk0, true, true, true, true, false, 3, true, 0);
            TBAR();
            ++j;
            SLOT(s1, s0, pk0, pk1, false, true, true, true, false, 3, true, 0);
            TBAR();
            --j;
        }
        if (n >= 4) {
            j = n - 3;
            SLOT(s0, s1, pk1, pk0, true, true, true, true, false, 3, true, 1);
            TBAR();
            j = n - 2;
            SLOT(s1, s0, pk0, pk1, false, true, true, true, false, 1, true, 1);
            wait_bar_v<2>();
        }
        j = n - 1;
        SLOT(s0, s1, pk1, pk0, true, false, true, true, false, 1, false, 0);
        wait_bar_v<2>();
        if (pass == 0) {
            const lds_cptr qr = (lds_cptr)lds + SPARE + rg * 8192 + lane * 16;
#pragma unroll
            for (int s = 0; s < 8; ++s) qf[s] = lds_frag(qr + s * 1024);
        }
        j = n;
        SLOT(s1, s0, pk0, pk1, false, false, false, true, false, false, false, 0);
        g0 += n;
        STAMP(4 + 2 * pass);
        {
            l += l2;
            auto rr = __builtin_amdgcn_permlane32_swap(__float_as_uint(l), __float_as_uint(l), false, false);
            l = __uint_as_float(rr[0]) + __uint_as_float(rr[1]);
            wait_bar<63>();
            LAS float* ml = (LAS float*)((lds_ptr)lds + SPARE);
            *(LAS f32x2*)(ml + (wid * 64 + lane) * 2) = (f32x2){m, l};
            lds_ptr sendp = (lds_ptr)lds + VRING + par * 32768 + rg * 8192 + lane * 16;
            lds_cptr recvp = (lds_cptr)lds + VRING + (par ^ 1) * 32768 + rg * 8192 + lane * 16;
            f32x16 keep[2];
            if (par) {
#pragma unroll
                for (int d = 0; d < 2; ++d) { keep[d] = o[2 + d];
#pragma unroll
                    for (int g = 0; g < 4; ++g) *(LAS f32x4*)(sendp + (d * 4 + g) * 1024) = (f32x4){o[d][4 * g], o[d][4 * g + 1], o[d][4 * g + 2], o[d][4 * g + 3]}; }
            } else {
#pragma unroll
                for (int d = 0; d < 2; ++d) { keep[d] = o[d];
#pragma unroll
                    for (int g = 0; g < 4; ++g) *(LAS f32x4*)(sendp + (d * 4 + g) * 1024) = (f32x4){o[2 + d][4 * g], o[2 + d][4 * g + 1], o[2 + d][4 * g + 2], o[2 + d][4 * g + 3]}; }
            }
            wait_bar<63>();
            const f32x2 mlo = *(const LAS f32x2*)(ml + ((wid ^ 4) * 64 + lane) * 2);
            const float mm = __builtin_fmaxf(m, mlo[0]), fo = EXP2(m - mm), fx = EXP2(mlo[0] - mm);
            const float inv = __builtin_amdgcn_rcpf(l * fo + mlo[1] * fx), sc = fo * inv, sx = fx * inv;
            const unsigned cdst = (unsigned)(WS_CTX + ((size_t)(blk * 4 + rg) * 32 + h) * 8192 + (par * 4) * 1024) + lane * 16;
#pragma unroll
            for (int d = 0; d < 2; ++d) {
#pragma unroll
                for (int g = 0; g < 4; ++g) {
                    const f32x4 v4 = *(const LAS f32x4*)(recvp + (d * 4 + g) * 1024);
#pragma unroll
                    for (int e = 0; e < 4; ++e) keep[d][4 * g + e] = keep[d][4 * g + e] * sc + v4[e] * sx;
                }
                st16(wr, cdst + (2 * d) * 1024, pack8(keep[d], 0));
                st16(wr, cdst + (2 * d + 1) * 1024, pack8(keep[d], 1));
            }
            if (pass == 0) wait_bar<63>();
            else signal_flag(flags + FL_F3 + blk * 32 + h, tag, tid);
        }
        STAMP(5 + 2 * pass);
    }
#undef SLOT
#undef TBAR
#undef VF
}

constexpr int CSTR = 4160;
constexpr int CCH = 4 * CSTR + 4608;
DI void p4_body(const Args& a, rsrc_t wr, int b, int h, int r, char* lds, int wid, int lane, int tid, GAS u64* flags, u64 tag) {
    const int ch = wid >> 1, c = 4 * b + ch, half = wid & 1;
    lds_ptr base = (lds_ptr)lds + ch * CCH;
    const lds_cptr ur = (lds_cptr)lds + STG_UR + ch * 4096, hs = (lds_cptr)lds + STG_H + ch * 4096;
    u32x4 w0[2], w1[2], hv[2];
#pragma unroll
    for (int it = 0; it < 2; ++it) { const int y8 = half * 128 + it * 64 + lane;
        w0[it] = *(const LAS u32x4*)(ur + 16 * y8); w1[it] = *(const LAS u32x4*)(ur + 16 * (y8 < 255 ? y8 + 1 : y8)); if (y8 == 255) w1[it] = (u32x4){0u, 0u, 0u, 0u};
        hv[it] = *(const LAS u32x4*)(hs + 16 * y8); }
    wait_bar<63>();
    const float Dc = bf2f(a.ssmD[c]);
#pragma unroll
    for (int it = 0; it < 2; ++it) {
        const int y8 = half * 128 + it * 64 + lane;
        unsigned w[6] = {w0[it][0], w0[it][1], w0[it][2], w0[it][3], w1[it][0], w1[it][1]}, o[5];
#pragma unroll
        for (int e = 0; e < 5; ++e) o[e] = __builtin_amdgcn_alignbit(w[e + 1], w[e], 16);
#pragma unroll
        for (int sg = 0; sg < 4; ++sg) { const int e = sg >> 1; u32x4 v;
            if (sg & 1) v = (u32x4){o[e], o[e + 1], o[e + 2], o[e + 3]}; else v = (u32x4){w[e], w[e + 1], w[e + 2], w[e + 3]};
            *(LAS u32x4*)(base + sg * CSTR + y8 * 16) = v; }
        *(LAS u32x4*)(base + 4 * CSTR + 512 + y8 * 16) = hv[it];
    }
    if (half == 1 && lane < 8) { const int sg = lane >> 1, y8 = 256 + (lane & 1); *(LAS u32x4*)(base + sg * CSTR + y8 * 16) = (u32x4){0u, 0u, 0u, 0u}; }
    if (half == 0 && lane < 32) *(LAS u32x4*)(base + 4 * CSTR + lane * 16) = (u32x4){0u, 0u, 0u, 0u};
    wait_bar<63>();
    const int j = lane & 15, g = lane >> 4;
    lds_cptr ard = (lds_cptr)base + ((3 - j) & 3) * CSTR + (2031 - j - ((3 - j) & 3) + 8 * g) * 2 - half * 64 - 62 * 64, ard2 = ard + 8;
    const lds_cptr brd = (lds_cptr)base + 4 * CSTR + (240 + 16 * j + 8 * g) * 2 - half * 64 - 6 * 64;
    PIN(ard2);
    f32x4 acc[8] = {};
#define P4LOAD(A, B, RC) do { _Pragma("unroll") for (int u = 0; u < 8; ++u) { const s16x4 lo = *(const LAS s16x4*)(ard + (62 - (RC) - 8 * u) * 64), hi2 = *(const LAS s16x4*)(ard2 + (62 - (RC) - 8 * u) * 64); \
            A[u] = __builtin_shufflevector(lo, hi2, 0, 1, 2, 3, 4, 5, 6, 7); B[u] = lds_frag(brd + (8 * u - (RC) + 6) * 64); } } while (0)
#define P4MM(A, B) do { _Pragma("unroll") for (int u = 0; u < 8; ++u) _Pragma("unroll") for (int v = 0; v + u < 8; ++v) { acc[u + v] = MFMA16(A[u], B[v], acc[u + v]); PIN(acc[u + v]); SBAR(); } } while (0)
    {
        bf16x8 A0[8], B0[8], A1[8], B1[8];
        P4LOAD(A0, B0, 0); SBAR(); P4LOAD(A1, B1, 2); SBAR();
        P4MM(A0, B0);
        P4LOAD(A0, B0, 4); SBAR();
        P4MM(A1, B1);
        P4LOAD(A1, B1, 6); SBAR();
        P4MM(A0, B0);
        P4MM(A1, B1);
    }
#undef P4LOAD
#undef P4MM
    wait_bar<0>();
    if (tid == 0) flag_store(flags + FL_F1 + h * 8 + r, tag);
    lds_ptr ex = base + CSTR;
    f32x4 keep[4];
    if (half) {
#pragma unroll
        for (int q = 0; q < 4; ++q) { *(LAS f32x4*)(ex + 4096 + q * 1024 + lane * 16) = acc[q]; keep[q] = acc[4 + q]; }
    } else {
#pragma unroll
        for (int q = 0; q < 4; ++q) { *(LAS f32x4*)(ex + q * 1024 + lane * 16) = acc[4 + q]; keep[q] = acc[q]; }
    }
    wait_bar<63>();
    u32x2 zp[4];
#pragma unroll
    for (int q = 0; q < 4; ++q) {
        const f32x4 o = *(const LAS f32x4*)(ex + (half ^ 1) * 4096 + q * 1024 + lane * 16);
        const f32x4 y = keep[q] + o;
        const int t = 256 * (half * 4 + q) + 16 * j + 4 * g;
        const s16x4 kvr = *(const LAS s16x4*)(base + (2047 - t - 3) * 2);
        float z[4];
#pragma unroll
        for (int r = 0; r < 4; ++r) { const float kv = bf2f((u16)kvr[3 - r]); z[r] = y[r] + kv * Dc; }
        zp[q] = (u32x2){cvtpk(z[0], z[1]), cvtpk(z[2], z[3])};
    }
#pragma unroll
    for (int q = 0; q < 4; q += 2) {
        auto s0 = __builtin_amdgcn_permlane16_swap(zp[q][0], zp[q + 1][0], false, false);
        auto s1 = __builtin_amdgcn_permlane16_swap(zp[q][1], zp[q + 1][1], false, false);
        const int t = 256 * (half * 4 + q + (g & 1)) + 16 * j + 8 * (g >> 1);
        st16(wr, (unsigned)(WS_Z + ((size_t)c * S + t) * 2), (u32x4){s0[0], s1[0], s0[1], s1[1]});
    }
}

constexpr int OCH = 40960;
DI u32x4 ld16c(rsrc_t r, unsigned off) { return __builtin_amdgcn_raw_buffer_load_b128(r, off, 0, 16); }
DI void p5_body(const Args& a, rsrc_t wr, int mt, int nt, int ks, char* lds, int wid, int lane, int tid, GAS u64* flags, u64 tag) {
    const unsigned lds0 = (unsigned)(uintptr_t)lds;
    const int rg = wid & 3, kh = wid >> 2;
    const int j = lane & 15, g = lane >> 4;
    const unsigned tv = lane * 16;
    const char* wg = a.ws + WS_WO + ((size_t)(2 * nt + ((wid >> 1) & 1)) * 32 + 8 * ks + 4 * (wid >> 2)) * 20480 + (wid & 1) * 5120;
    const unsigned wl = lds0 + (wid >> 1) * 10240 + (wid & 1) * 5120;
#define WLOAD(slot, st) do { const char* p_ = wg + (size_t)((st) >> 1) * 20480 + ((st) & 1) * 10240; \
        asm volatile("s_mov_b32 m0, %0\n\ts_nop 0\n\tglobal_load_lds_dwordx4 %1, %2 sc1\n\tglobal_load_lds_dwordx4 %1, %2 offset:1024 sc1\n\tglobal_load_lds_dwordx4 %1, %2 offset:2048 sc1\n\t" \
                     "global_load_lds_dwordx4 %1, %2 offset:3072 sc1" :: "s"(wl + (slot) * OCH), "v"(tv), "s"(p_) : "memory"); \
        dma1c(wl + (slot) * OCH + 4096, tv, p_ + 4096); } while (0)
    WLOAD(0, 0); WLOAD(1, 1);
    if (wid == 0) {
        poll_flags(flags, lane < 40 ? FL_F4 + nt * 40 + lane : (lane < 44 ? FL_F0 + (4 * (ks & 1) + lane - 40) * 32 + 2 * mt + (ks >> 1) : -1), tag);
        poll_flags(flags, lane < 8 ? FL_F3 + mt * 32 + 8 * ks + lane : -1, tag);
    }
    wait_bar<63>();
    const char* cg = a.ws + WS_CTX + ((size_t)(4 * mt + rg) * 32 + 8 * ks + 4 * kh) * 8192;
    const unsigned cv = (g >> 1) * 1024 + ((g & 1) * 32 + j) * 16;
    u32x4 cf[3][4];
#define CLOAD(slot, st) do { const char* p_ = cg + (size_t)((st) >> 1) * 8192 + ((st) & 1) * 4096; \
        asm volatile("global_load_dwordx4 %0, %4, %5 sc1\n\tglobal_load_dwordx4 %1, %4, %5 offset:256 sc1\n\tglobal_load_dwordx4 %2, %4, %5 offset:2048 sc1\n\tglobal_load_dwordx4 %3, %4, %5 offset:2304 sc1" \
                     : "=&v"(cf[slot][0]), "=&v"(cf[slot][1]), "=&v"(cf[slot][2]), "=&v"(cf[slot][3]) : "v"(cv), "s"(p_) : "memory"); } while (0)
    CLOAD(0, 0); CLOAD(1, 1);
    unsigned zr[5];
#pragma unroll
    for (int it = 0; it < 5; ++it) { const int e = tid + it * 512, cl2 = e >> 4, d = e & 15;
        zr[it] = ld4c(wr, (unsigned)(WS_Z + ((size_t)(nt * 160 + cl2) * S + mt * 128 + ks * 32 + 2 * d) * 2)); }
    u32x2 xv[3]; float gt[3];
#pragma unroll
    for (int k = 0; k < 3; ++k) { const int i = wid + 8 * k < 20 ? wid + 8 * k : wid, t = mt * 128 + ks * 32 + 16 * ((i % 10) / 5) + j, c = nt * 160 + 80 * (i / 10) + 16 * (i % 5) + 4 * g;
        xv[k] = *(const u32x2*)(a.x + (size_t)t * M + c); gt[k] = __uint_as_float(ld4c(wr, (unsigned)(WS_G + t * 4))); }
    const lds_cptr brd = (lds_cptr)lds + kh * 20480 + lane * 16;
    f32x4 acc[2][10] = {};
#pragma unroll
    for (int st = 0; st < 8; ++st) {
        if (st == 0) wait_bar<4>(); else if (st < 7) wait_bar<9>(); else wait_bar<0>();
        asm volatile("" : "+v"(cf[st % 3][0]), "+v"(cf[st % 3][1]), "+v"(cf[st % 3][2]), "+v"(cf[st % 3][3]));
        if (st == 0) { _Pragma("unroll") for (int k = 0; k < 3; ++k) { PIN(xv[k]); PIN(gt[k]); } _Pragma("unroll") for (int it = 0; it < 5; ++it) PIN(zr[it]); }
        const lds_cptr bb = brd + (st % 3) * OCH;
        bf16x8 wf[4];
#define WFI(i) lds_frag(bb + (((i) / 5) & 1) * 10240 + (((i) / 10) * 5 + (i) % 5) * 1024)
#pragma unroll
        for (int i = 0; i < 4; ++i) wf[i] = WFI(i);
        SBAR();
#pragma unroll
        for (int i = 0; i < 20; ++i) {
            const int ks2 = i / 10, c10 = i % 10;
            acc[0][c10] = MFMA16(wf[i & 3], __builtin_bit_cast(bf16x8, cf[st % 3][2 * ks2]), acc[0][c10]); PIN(acc[0][c10]); SBAR();
            acc[1][c10] = MFMA16(wf[i & 3], __builtin_bit_cast(bf16x8, cf[st % 3][2 * ks2 + 1]), acc[1][c10]); PIN(acc[1][c10]);
            if (i + 4 < 20) wf[i & 3] = WFI(i + 4);
            if (st + 2 < 8) { if (i == 1) WLOAD((st + 2) % 3, st + 2); if (i == 10) CLOAD((st + 2) % 3, st + 2); }
            SBAR();
        }
#undef WFI
    }
#undef WLOAD
#undef CLOAD
    wait_bar<0>();
    {
        lds_ptr sp = (lds_ptr)lds + wid * 10240 + lane * 16; lds_cptr rp = (lds_cptr)lds + (wid ^ 4) * 10240 + lane * 16;
        f32x4 keep[10];
        if (kh) {
#pragma unroll
            for (int i = 0; i < 10; ++i) { *(LAS f32x4*)(sp + i * 1024) = acc[i / 5][i % 5]; keep[i] = acc[i / 5][5 + i % 5]; }
        } else {
#pragma unroll
            for (int i = 0; i < 10; ++i) { *(LAS f32x4*)(sp + i * 1024) = acc[i / 5][5 + i % 5]; keep[i] = acc[i / 5][i % 5]; }
        }
        lds_ptr zb = (lds_ptr)lds + 81920;
#pragma unroll
        for (int it = 0; it < 5; ++it) { const int e = tid + it * 512, cl2 = e >> 4, d = e & 15; *(LAS unsigned*)(zb + cl2 * 68 + d * 4) = zr[it]; }
        wait_bar<63>();
#pragma unroll
        for (int i = 0; i < 10; ++i) keep[i] += *(const LAS f32x4*)(rp + i * 1024);
        const int tile = mt * 4 + nt;
        if (rg != ks) {
            const unsigned dst = (unsigned)(WS_SL + (((size_t)tile * 4 + rg) * 4 + ks) * 20480 + kh * 10240) + lane * 16;
#pragma unroll
            for (int i = 0; i < 10; ++i) st16(wr, dst + i * 1024, __builtin_bit_cast(u32x4, keep[i]));
        } else {
            lds_ptr op = (lds_ptr)lds + 98304 + kh * 10240 + lane * 16;
#pragma unroll
            for (int i = 0; i < 10; ++i) *(LAS f32x4*)(op + i * 1024) = keep[i];
        }
        signal_flag(flags + FL_F5 + tile * 4 + ks, tag, tid);
        if (wid == 0) poll_flags(flags, (lane < 4 && lane != ks) ? FL_F5 + tile * 4 + lane : -1, tag);
        wait_bar<63>();
        u32x4 pf[3][3];
#pragma unroll
        for (int k = 0; k < 3; ++k) { const int i = wid + 8 * k < 20 ? wid + 8 * k : wid;
            const unsigned src = (unsigned)(WS_SL + ((size_t)tile * 4 + ks) * 4 * 20480) + i * 1024 + lane * 16;
#pragma unroll
            for (int f = 0; f < 3; ++f) pf[k][f] = ld16c(wr, src + (unsigned)(f + (f >= ks ? 1 : 0)) * 20480u); }
#pragma unroll
        for (int k = 0; k < 3; ++k) if (wid + 8 * k < 20) {
            const int i = wid + 8 * k, tl = 16 * ((i % 10) / 5) + j, cl = 80 * (i / 10) + 16 * (i % 5) + 4 * g;
            f32x4 av = *(const LAS f32x4*)((lds_cptr)lds + 98304 + i * 1024 + lane * 16);
#pragma unroll
            for (int f = 0; f < 3; ++f) av += __builtin_bit_cast(f32x4, pf[k][f]);
            float r4[4];
#pragma unroll
            for (int e = 0; e < 4; ++e) { const float z = bf2f(*(const LAS u16*)((lds_cptr)lds + 81920 + (cl + e) * 68 + tl * 2));
                const float xe = (e & 1) ? bhi(xv[k][e >> 1]) : blo(xv[k][e >> 1]);
                r4[e] = gt[k] * av[e] + (1.f - gt[k]) * (xe * z); }
            *(u32x2*)(a.out + (size_t)(mt * 128 + ks * 32 + tl) * M + nt * 160 + cl) = (u32x2){cvtpk(r4[0], r4[1]), cvtpk(r4[2], r4[3])};
        }
    }
}

__global__ __launch_bounds__(512, 2) void k_fused(const u16* x, const u16* ckv, const u16* wq, const u16* wuk, const u16* wuv, const u16* wo, const u16* bk, const u16* bv,
                                                  const u16* shk, const u16* ssmk, const u16* ssmD, const u16* gw, u16* out, char* ws) {
    extern __shared__ __attribute__((aligned(16))) char lds[];
    const Args a = {x, ckv, wq, wuk, wuv, wo, bk, bv, shk, ssmk, ssmD, gw, out, ws};
    const int tid = threadIdx.x, lane = tid & 63, wid = rfl(tid >> 6);
    const int b = blockIdx.x, xcd = b & 7, idx = b >> 3, h = xcd * 4 + (idx >> 3), r = idx & 7;
    const unsigned lds0 = (unsigned)(uintptr_t)lds;
    const rsrc_t wr = __builtin_amdgcn_make_buffer_rsrc(ws, 0, (int)WS_END, 0x00020000);
    GAS u64* flags = (GAS u64*)(ws + WS_F);
    STAMP(0);
    unsigned pfc = 0;
    if (wid == 7) { const char* pc; asm volatile("s_getpc_b64 %0" : "=s"(pc)); int off = (idx * 28 + (lane < 28 ? lane : 27)) * 64; off = off < 56832 ? off : 56832;
        asm volatile("global_load_dword %0, %1, %2" : "+v"(pfc) : "v"(off), "s"(pc) : "memory"); }
    const P1Ctx c1 = p1_setup(a, h, r, lds0, wid, lane);
    const u64 tag = *(const __attribute__((address_space(4))) u64*)(ws + WS_F) + 1;
    if (b >= 160) {
        u32x4 xg[3][2];
        const u32x4 g0 = *(const u32x4*)(gw + lane * 8), g1 = *(const u32x4*)(gw + 512 + (lane & 15) * 8);
#pragma unroll
        for (int k = 0; k < 3; ++k) { int jb = b - 160 + 96 * k; if (jb >= 256) jb = b - 160; const u16* xr = x + (size_t)(64 * (jb >> 3) + 8 * (jb & 7) + wid) * M;
            xg[k][0] = *(const u32x4*)(xr + lane * 8); xg[k][1] = *(const u32x4*)(xr + 512 + (lane & 15) * 8); }
        p0a_load(a, b - 160, lds0, wid, lane);
        p1_prime_t(c1);
        STAMP(11);
        wait_bar<4>();
#pragma unroll
        for (int k = 0; k < 3; ++k) { PIN(xg[k][0]); PIN(xg[k][1]); }
        STAMP(12);
        p0a_compute(b - 160, lds, tid);
        wait_bar<63>();
        STAMP(13);
        p1_prime_w(c1);
        p0a_store(wr, b - 160, lds, tid);
#pragma unroll
        for (int k = 0; k < 3; ++k) { const int jb = b - 160 + 96 * k; float sum = 0.f, s1 = 0.f;
#pragma unroll
            for (int e = 0; e < 4; ++e) { sum += blo(xg[k][0][e]) * blo(g0[e]) + bhi(xg[k][0][e]) * bhi(g0[e]); s1 += blo(xg[k][1][e]) * blo(g1[e]) + bhi(xg[k][1][e]) * bhi(g1[e]); }
            if (lane < 16) sum += s1;
            sum = wave_sum(sum);
            if (lane == 0 && jb < 256) st4(wr, (unsigned)(WS_G + (64 * (jb >> 3) + 8 * (jb & 7) + wid) * 4), __float_as_uint(1.f / (1.f + __expf(-sum)))); }
    } else { p1_prime_t(c1); p1_prime_w(c1); }
    STAMP(1);
    if (wid < 4) p1_body<0>(a, c1, wr, b, h, r, lds, wid, lane, tid, flags, tag); else p1_body<1>(a, c1, wr, b, h, r, lds, wid, lane, tid, flags, tag);
    STAMP(2);
    asm volatile("" : "+v"(pfc));
    wait_bar<63>();
    p0b_finish(wr, b, lds, wid, lane);
    STAMP(14);
    if (b < 160) p4_body(a, wr, b, h, r, lds, wid, lane, tid, flags, tag);
    wait_bar<63>();
    STAMP(8);
    p2_body(a, b, h, r, 15 - r, r, lds, wid, lane, tid, flags, tag);
    STAMP(3);
    p3_body(wr, ws, h, 15 - r, r, lds, wid, lane, tid, flags, tag);
    {
        const int ks = xcd >> 1, mt = (xcd & 1) * 8 + (idx >> 2), nt = idx & 3;
        if (wid == 0) poll_flags(flags, lane < 16 ? FL_FW + (2 * nt + (lane >> 3)) * 32 + 8 * ks + (lane & 7) : -1, tag);
        wait_bar<0>();
        STAMP(9);
        p5_body(a, wr, mt, nt, ks, lds, wid, lane, tid, flags, tag);
    }
    if (b == 0 && tid == 0) flag_store(flags, tag);
    STAMP(10);
}

extern "C" void kernel_launch(void* const* d_in, const int* in_sizes, int n_in,
                              void* d_out, int out_size, void* d_ws, size_t ws_size,
                              hipStream_t stream) {
    static bool attr = false;
    if (!attr) { (void)hipFuncSetAttribute((const void*)k_fused, hipFuncAttributeMaxDynamicSharedMemorySize, LDS_BYTES); attr = true; }
    k_fused<<<256, 512, LDS_BYTES, stream>>>((const u16*)d_in[0], (const u16*)d_in[1], (const u16*)d_in[2], (const u16*)d_in[3], (const u16*)d_in[4], (const u16*)d_in[5],
                                             (const u16*)d_in[7], (const u16*)d_in[8], (const u16*)d_in[9], (const u16*)d_in[10], (const u16*)d_in[11], (const u16*)d_in[12],
                                             (u16*)d_out, (char*)d_ws);
}
```

```cpp
#include <hip/hip_runtime.h>
#include <hip/hip_bf16.h>
#include <stdint.h>

typedef unsigned short u16;
typedef unsigned long long u64;
typedef short bf16x8 __attribute__((ext_vector_type(8)));
typedef short s16x4 __attribute__((ext_vector_type(4)));
typedef float f32x16 __attribute__((ext_vector_type(16)));
typedef float f32x4 __attribute__((ext_vector_type(4)));
typedef float f32x2 __attribute__((ext_vector_type(2)));
typedef __bf16 bf16x2_t __attribute__((ext_vector_type(2)));
typedef unsigned u32x4 __attribute__((ext_vector_type(4)));
typedef unsigned u32x2 __attribute__((ext_vector_type(2)));
typedef short v4i16_t __attribute__((ext_vector_type(4)));
#define LAS __attribute__((address_space(3)))
#define GAS __attribute__((address_space(1)))
typedef LAS const char* lds_cptr;
typedef LAS char* lds_ptr;
#define DI __device__ __forceinline__
#define SBAR() __builtin_amdgcn_sched_barrier(0)
#define PIN(x) asm volatile("" : "+v"(x))
#define PINS(x) asm volatile("" : "+s"(x))
#define MFMA32(a, b, c) __builtin_amdgcn_mfma_f32_32x32x16_bf16(a, b, c, 0, 0, 0)
#define MFMA16(a, b, c) __builtin_amdgcn_mfma_f32_16x16x32_bf16(a, b, c, 0, 0, 0)
#define LGKM(n) __builtin_amdgcn_s_waitcnt(0xC07F | ((n) << 8))
#define EXP2(v) __builtin_amdgcn_exp2f(v)

constexpr int S = 2048, H = 32, D = 128, M = 640, DC = 512, HD = H * D;
constexpr int LDS_BYTES = 163840;
constexpr size_t WS_K = 0, WS_V = (size_t)S * HD * 2, WS_CTX = 2 * WS_V, WS_UR = 3 * WS_V, WS_Z = WS_UR + (size_t)S * M * 2, WS_WO = WS_Z + (size_t)S * M * 2,
                 WS_G = WS_WO + (size_t)HD * M * 2, WS_F = WS_G + 8192, WS_SL = WS_F + 32768, WS_END = WS_SL + (size_t)64 * 16 * 20480;
constexpr int FL_F0 = 16, FL_F1 = FL_F0 + 256, FL_F3 = FL_F1 + 256, FL_F4 = FL_F3 + 512, FL_FW = FL_F4 + 160, FL_F5 = FL_FW + 256;

#define STAMP(i)

DI float bf2f(u16 b) { return __uint_as_float(((unsigned)b) << 16); }
DI unsigned cvtpk(float lo, float hi) { f32x2 v = {lo, hi}; bf16x2_t b = __builtin_convertvector(v, bf16x2_t); return __builtin_bit_cast(unsigned, b); }
DI u16 f2bf(float f) { return (u16)cvtpk(f, f); }
DI float rb(float f) { return __uint_as_float(cvtpk(f, f) << 16); }
DI unsigned rfl(unsigned x) { return __builtin_amdgcn_readfirstlane(x); }

#define SETM0(base, imm) asm volatile("s_add_u32 m0, %0, %1" :: "s"(base), "i"(imm) : "scc")
#define GLDS(voff, sbase, imm) asm volatile("global_load_lds_dwordx4 %0, %1 offset:%c2" :: "v"(voff), "s"(sbase), "i"(imm) : "memory")
#define GLDSC(voff, sbase, imm) asm volatile("global_load_lds_dwordx4 %0, %1 offset:%c2 sc1" :: "v"(voff), "s"(sbase), "i"(imm) : "memory")
DI void dma1(unsigned ldsaddr, unsigned voff, const void* sbase) {
    asm volatile("s_mov_b32 m0, %0\n\ts_nop 0\n\tglobal_load_lds_dwordx4 %1, %2" :: "s"(ldsaddr), "v"(voff), "s"(sbase) : "memory");
}
DI void dma2(unsigned ldsaddr, unsigned voff, const void* sbase) {
    asm volatile("s_mov_b32 m0, %0\n\ts_nop 0\n\tglobal_load_lds_dwordx4 %1, %2\n\tglobal_load_lds_dwordx4 %1, %2 offset:1024" :: "s"(ldsaddr), "v"(voff), "s"(sbase) : "memory");
}
DI void dma1c(unsigned ldsaddr, unsigned voff, const void* sbase) {
    asm volatile("s_mov_b32 m0, %0\n\ts_nop 0\n\tglobal_load_lds_dwordx4 %1, %2 sc1" :: "s"(ldsaddr), "v"(voff), "s"(sbase) : "memory");
}
DI void dma2c(unsigned ldsaddr, unsigned voff, const void* sbase) {
    asm volatile("s_mov_b32 m0, %0\n\ts_nop 0\n\tglobal_load_lds_dwordx4 %1, %2 sc1\n\tglobal_load_lds_dwordx4 %1, %2 offset:1024 sc1" :: "s"(ldsaddr), "v"(voff), "s"(sbase) : "memory");
}
template <int N> DI void wait_bar_v() { asm volatile("s_waitcnt vmcnt(%0)\n\ts_barrier" :: "i"(N) : "memory"); }
template <int N> DI void wait_bar() { asm volatile("s_waitcnt vmcnt(%0) lgkmcnt(0)\n\ts_barrier" :: "i"(N) : "memory"); }
DI bf16x8 lds_frag(lds_cptr p) { return *(const LAS bf16x8*)p; }
DI s16x4 vtr(lds_cptr p) { return __builtin_bit_cast(s16x4, __builtin_amdgcn_ds_read_tr16_b64_v4i16((LAS v4i16_t*)p)); }
DI bf16x8 tr_frag(lds_cptr p, int second) { const s16x4 lo = vtr(p), hi = vtr(p + second); return __builtin_shufflevector(lo, hi, 0, 1, 2, 3, 4, 5, 6, 7); }
DI u32x4 pack8(const f32x16& x, int s) {
    u32x4 w; w[0] = cvtpk(x[8 * s + 0], x[8 * s + 1]); w[1] = cvtpk(x[8 * s + 2], x[8 * s + 3]); w[2] = cvtpk(x[8 * s + 4], x[8 * s + 5]); w[3] = cvtpk(x[8 * s + 6], x[8 * s + 7]); return w;
}
typedef __amdgpu_buffer_rsrc_t rsrc_t;
DI void st16(rsrc_t r, unsigned off, u32x4 v) { __builtin_amdgcn_raw_buffer_store_b128(v, r, off, 0, 17); }
DI void st8(rsrc_t r, unsigned off, u32x2 v) { __builtin_amdgcn_raw_buffer_store_b64(v, r, off, 0, 16); }
DI void st4(rsrc_t r, unsigned off, unsigned v) { __builtin_amdgcn_raw_buffer_store_b32(v, r, off, 0, 16); }
DI void signal_flag(GAS u64* flag, u64 tag, int tid) {
    asm volatile("s_waitcnt vmcnt(0) lgkmcnt(0)\n\ts_barrier" ::: "memory");
    if (tid == 0) __hip_atomic_store(flag, tag, __ATOMIC_RELAXED, __HIP_MEMORY_SCOPE_AGENT);
}
DI void poll_flags(GAS u64* flags, int idx, u64 tag) {
    for (unsigned spins = 0; spins < (1u << 16); ++spins) {
        const u64 v = idx >= 0 ? __hip_atomic_load(flags + idx, __ATOMIC_RELAXED, __HIP_MEMORY_SCOPE_AGENT) : tag;
        if (__all(v == tag)) break;
        __builtin_amdgcn_s_sleep(2);
    }
}
DI unsigned ld4c(rsrc_t r, unsigned off) { return __builtin_amdgcn_raw_buffer_load_b32(r, off, 0, 16); }
DI void flag_store(GAS u64* flag, u64 tag) { __hip_atomic_store(flag, tag, __ATOMIC_RELAXED, __HIP_MEMORY_SCOPE_AGENT); }

struct Args { const u16 *x, *ckv, *wq, *wuk, *wuv, *wo, *bk, *bv, *shk, *ssmk, *ssmD, *gw; u16* out; char* ws; };

constexpr int P0_OFF = 133120;
DI float blo(unsigned w) { return __uint_as_float(w << 16); }
DI float bhi(unsigned w) { return __uint_as_float(w & 0xffff0000u); }
template <int CTRL> DI float dpp_add(float v) { return v + __builtin_bit_cast(float, __builtin_amdgcn_update_dpp(0, __builtin_bit_cast(int, v), CTRL, 0xf, 0xf, true)); }
DI float wave_sum(float v) {
    v = dpp_add<0x128>(v); v = dpp_add<0x124>(v); v = dpp_add<0x122>(v); v = dpp_add<0x121>(v);
    auto a = __builtin_amdgcn_permlane16_swap(__float_as_uint(v), __float_as_uint(v), false, false); v = __uint_as_float(a[0]) + __uint_as_float(a[1]);
    auto b = __builtin_amdgcn_permlane32_swap(__float_as_uint(v), __float_as_uint(v), false, false); return __uint_as_float(b[0]) + __uint_as_float(b[1]);
}
DI int div10(int x) { return (x * 6554) >> 16; }
DI void p0a_load(const Args& a, int jb0, unsigned lds0, int wid, int lane) {
#pragma unroll
    for (int i = 0; i < 9; ++i) {
        const int p = wid + 8 * i;
        if (p < 66) {
            const int k = p >= 44 ? 2 : (p >= 22 ? 1 : 0), q = p - 22 * k;
            int jb = jb0 + 96 * k; if (jb >= 256) jb = jb0;
            const int cb = jb & 7, tb = jb >> 3;
            if (q < 11) { const int ci = q * 64 + lane, cj = ci < 670 ? ci : 669, row = div10(cj); int t = tb * 64 - 3 + row; t = t < 0 ? 0 : t;
                dma1(lds0 + p * 1024, (unsigned)(t * 1280 + (cj - 10 * row) * 16), (const char*)a.bk + cb * 160); }
            else if (q < 21) { const int ci = (q - 11) * 64 + lane, row = div10(ci);
                dma1(lds0 + p * 1024, (unsigned)(row * 1280 + (ci - 10 * row) * 16), (const char*)a.bv + (size_t)tb * 64 * 1280 + cb * 160); }
            else dma1(lds0 + p * 1024, (unsigned)((lane < 40 ? lane : 39) * 16), (const char*)a.shk + cb * 640);
        }
    }
}
DI void p0a_compute(int jb0, char* lds, int tid) {
    const int nj = jb0 + 192 < 256 ? 3 : 2;
#pragma unroll
    for (int it = 0; it < 2; ++it) {
        const int item = tid + it * 512, k = item >= 640 ? 2 : (item >= 320 ? 1 : 0), e = item - k * 320, tp = div10(e), cc = e - 10 * tp;
        if (k < nj && item < 960) {
            const int t0 = ((jb0 + 96 * k) >> 3) * 64;
            const lds_cptr in = (lds_cptr)lds + k * 22528;
            LAS char* tile = (LAS char*)lds + P0_OFF + k * 10240;
            u32x4 kk[5], vv[2], sh[4];
#pragma unroll
            for (int i = 0; i < 5; ++i) kk[i] = *(const LAS u32x4*)(in + (2 * tp + i) * 160 + cc * 16);
#pragma unroll
            for (int i = 0; i < 2; ++i) vv[i] = *(const LAS u32x4*)(in + 11264 + (2 * tp + i) * 160 + cc * 16);
#pragma unroll
            for (int i = 0; i < 4; ++i) sh[i] = *(const LAS u32x4*)(in + 21504 + cc * 64 + i * 16);
            if (t0 + 2 * tp < 3) {
#pragma unroll
                for (int i = 0; i < 3; ++i) if (t0 + 2 * tp + i - 3 < 0) kk[i] = (u32x4){0u, 0u, 0u, 0u};
            }
            const int dw = 31 - tp, pos = (((dw >> 2) ^ (cc & 7)) << 4) + (dw & 3) * 4;
#pragma unroll
            for (int qp = 0; qp < 4; ++qp) {
                f32x2 kx[5], r2[2];
#pragma unroll
                for (int i = 0; i < 5; ++i) kx[i] = (f32x2){blo(kk[i][qp]), bhi(kk[i][qp])};
                const f32x2 t0_ = {blo(sh[qp][0]), blo(sh[qp][2])}, t1_ = {bhi(sh[qp][0]), bhi(sh[qp][2])}, t2_ = {blo(sh[qp][1]), blo(sh[qp][3])}, t3_ = {bhi(sh[qp][1]), bhi(sh[qp][3])};
#pragma unroll
                for (int r = 0; r < 2; ++r)
                    r2[r] = (kx[r] * t3_ + kx[r + 1] * t2_ + kx[r + 2] * t1_ + kx[r + 3] * t0_) * (f32x2){blo(vv[r][qp]), bhi(vv[r][qp])};
                *(LAS unsigned*)(tile + (8 * cc + 2 * qp) * 128 + pos) = cvtpk(r2[1][0], r2[0][0]);
                *(LAS unsigned*)(tile + (8 * cc + 2 * qp + 1) * 128 + pos) = cvtpk(r2[1][1], r2[0][1]);
            }
        }
    }
}
DI void p0a_store(rsrc_t wr, int jb0, char* lds, int tid) {
    const int nj = jb0 + 192 < 256 ? 3 : 2;
#pragma unroll
    for (int it = 0; it < 4; ++it) { const int e = tid + it * 512, k = e >= 1280 ? 2 : (e >= 640 ? 1 : 0), f = e - k * 640, cl = f >> 3, d = f & 7;
        if (k < nj && e < 1920) { const int jb = jb0 + 96 * k;
            st16(wr, (unsigned)(WS_UR + ((size_t)((jb & 7) * 80 + cl) * S + (2047 - (jb >> 3) * 64 - 63) + 8 * d) * 2),
                 *(const LAS u32x4*)((lds_cptr)lds + P0_OFF + k * 10240 + cl * 128 + ((d ^ ((cl >> 3) & 7)) << 4))); } }
}
DI void p0b_load(const Args& a, int b, unsigned lds0, int wid, int lane) {
    const int cb = b & 7, tb = b >> 3;
#pragma unroll
    for (int i = 0; i < 3; ++i) { const int p = wid + 8 * i;
        if (p < 20) { const int ci = p * 64 + lane, row = div10(ci);
            dma1(lds0 + P0_OFF + p * 1024, (unsigned)(row * 1280 + (ci - 10 * row) * 16), (const char*)a.wo + (size_t)tb * 128 * 1280 + cb * 160); }
    }
}
DI void p0b_finish(rsrc_t wr, int b, char* lds, int wid, int lane) {
    const int cb = b & 7, tb = b >> 3;
    const int j = lane & 15, g = lane >> 4, q = j >> 2, p = lane & 3;
    const lds_cptr brd = (lds_cptr)lds + P0_OFF + (16 * (g >> 1) + 4 * (g & 1) + q) * 160 + p * 8;
#pragma unroll
    for (int i = 0; i < 3; ++i) if (wid + 8 * i < 20) { const int f = wid + 8 * i, kq = f / 5, cbk = f % 5;
        const bf16x8 w = tr_frag(brd + kq * 32 * 160 + cbk * 32, 1280);
        st16(wr, (unsigned)(WS_WO + ((size_t)(cb * 32 + tb) * 20 + f) * 1024 + lane * 16), __builtin_bit_cast(u32x4, w)); }
}

#define PF_TILE 6
constexpr int TSLOT = 32 * 1040;
struct P1Ctx { const char* wsrc; const char* tsrc; unsigned wvoff, wlds, tvoff, tlds; };
DI P1Ctx p1_setup(const Args& a, int h, int r, unsigned lds0, int wid, int lane) {
    P1Ctx c;
    c.wsrc = (const char*)((wid >> 2) ? a.wuv : a.wuk) + (size_t)h * 256 + ((wid >> 1) & 1) * 128 + (size_t)(wid & 1) * 4 * 8 * 8192;
    c.wvoff = (unsigned)(lane >> 3) * 8192u + (unsigned)(((lane & 7) ^ (((lane >> 4) & 1) << 2)) * 16);
    c.wlds = lds0 + ((wid >> 2) * 2 + ((wid >> 1) & 1)) * 8192 + (wid & 1) * 4 * 1024;
    c.tsrc = (const char*)a.ckv + (size_t)(256 * r + 4 * wid) * 1024;
    c.tvoff = lane * 16; c.tlds = lds0 + 4 * wid * 1040;
    return c;
}
DI void p1_dma_w1(const P1Ctx& c, int ch, int slot, int i) { dma1(c.wlds + slot * TSLOT + i * 1024, c.wvoff, c.wsrc + (size_t)ch * 64 * 8192 + (size_t)i * 8 * 8192); }
template <int J> DI void p1_dma_t1(const P1Ctx& c, int t, int slot) {
    asm volatile("s_mov_b32 m0, %0\n\ts_nop 0\n\tglobal_load_lds_dwordx4 %1, %2 offset:%c3" :: "s"(c.tlds + slot * TSLOT + 16 * J), "v"(c.tvoff), "s"(c.tsrc + (size_t)t * 32 * 1024), "i"(J * 1024) : "memory");
}
DI void p1_prime_t(const P1Ctx& c) { p1_dma_t1<0>(c, 0, 3); p1_dma_t1<1>(c, 0, 3); p1_dma_t1<2>(c, 0, 3); p1_dma_t1<3>(c, 0, 3); }
DI void p1_prime_w(const P1Ctx& c) {
#pragma unroll
    for (int ch = 0; ch < 3; ++ch)
#pragma unroll
        for (int i = 0; i < 4; ++i) p1_dma_w1(c, ch, ch, i);
}
constexpr int STG_UR = 3 * TSLOT, STG_H = STG_UR + 16384;
template <int MAT>
DI void p1_body(const Args& a, const P1Ctx& c, rsrc_t wr, int b, int h, int r, char* lds, int wid, int lane, int tid, GAS u64* flags, u64 tag) {
    const int g4 = lane >> 4, q = (lane & 15) >> 2, p = lane & 3;
    const lds_cptr wrd = (lds_cptr)lds + (MAT * 2 + ((wid >> 1) & 1)) * 8192 + (8 * (g4 >> 1) + q) * 128 + (((wid & 1) ^ (q >> 1)) * 64) + (g4 & 1) * 32 + p * 8;
    const lds_cptr trd = (lds_cptr)lds + (lane & 31) * 1040 + (lane >> 5) * 16;
    const unsigned dst = (unsigned)((MAT ? WS_V : WS_K) + ((size_t)(h * 64 + 8 * r) * 8 + 2 * (wid & 3)) * 1024) + lane * 16;
    unsigned pfd = 0;
    bf16x8 Wf[32];
    f32x16 acc = f32x16{};
    const lds_cptr t0p = trd + 3 * TSLOT;
#define P1M(A_, W_) (MAT == 0 ? MFMA32(W_, A_, acc) : MFMA32(A_, W_, acc))
#pragma unroll
    for (int ch = 0; ch < 8; ++ch) {
        if (ch == 0) wait_bar<8>(); else wait_bar<4>();
        if (ch == 3 && b >= 160 && tid < 3) { const int jb = b - 160 + 96 * tid; if (jb < 256) flag_store(flags + FL_F0 + (jb & 7) * 32 + (jb >> 3), tag); }
        const lds_cptr wb = wrd + (ch % 3) * TSLOT;
        bf16x8 af[4];
#pragma unroll
        for (int s = 0; s < 4; ++s) { Wf[4 * ch + s] = tr_frag(wb + s * 2048, 512); af[s] = lds_frag(t0p + (4 * ch + s) * 32); }
        SBAR();
#pragma unroll
        for (int s = 0; s < 4; ++s) {
            if (ch >= 1) { if (ch + 2 <= 7) p1_dma_w1(c, ch + 2, (ch + 2) % 3, s);
                           else { if (s == 0) p1_dma_t1<0>(c, ch - 5, (ch + 2) % 3); if (s == 1) p1_dma_t1<1>(c, ch - 5, (ch + 2) % 3); if (s == 2) p1_dma_t1<2>(c, ch - 5, (ch + 2) % 3); if (s == 3) p1_dma_t1<3>(c, ch - 5, (ch + 2) % 3); } }
            acc = P1M(af[s], Wf[4 * ch + s]); PIN(acc);
            SBAR();
        }
    }
    f32x16 accp = acc;
    bf16x8 af[4];
    wait_bar<4>();
#pragma unroll
    for (int s = 0; s < 4; ++s) af[s] = lds_frag(trd + (8 % 3) * TSLOT + s * 32);
    SBAR();
#pragma unroll
    for (int i = 1; i < 8; ++i) {
        const int j = 7 + i;
        const lds_cptr tb = trd + (j % 3) * TSLOT, tn = trd + ((j + 1) % 3) * TSLOT;
#pragma unroll
        for (int s = 0; s < 32; ++s) {
            if (s == 28 && i < 7) {
                if (b < 160 && i == 4 && wid == 0) poll_flags(flags, lane < 32 ? FL_F0 + (b / 20) * 32 + lane : -1, tag);
                if (i + 2 <= 7) wait_bar<6>(); else wait_bar<2>();
                const unsigned lds0 = (unsigned)(uintptr_t)lds;
                if (i == 1) p0b_load(a, b, lds0, wid, lane);
                if (i == PF_TILE) {
                    const int id = (b * 8 + wid) * 60 + (lane < 59 ? lane : 59);
                    const char* p = id < 40960 ? (const char*)a.x + (size_t)id * 64 : (const char*)a.wq + (size_t)(id - 40960) * 64;
                    asm volatile("global_load_dword %0, %1, off" : "+v"(pfd) : "v"(p) : "memory"); }
                if (b < 160) {
                    if (i == 1) dma2(lds0 + STG_H + wid * 2048, lane * 16, (const char*)a.ssmk + (size_t)(4 * b) * 4096 + wid * 2048);
                    if (i == 4) dma2c(lds0 + STG_UR + wid * 2048, lane * 16, a.ws + WS_UR + (size_t)(4 * b) * 4096 + wid * 2048);
                }
                SBAR();
            }
            if (!(s & 1) && s + 4 < 32) { LGKM(2); SBAR(); }
            if (s == 0) acc = MAT == 0 ? MFMA32(Wf[0], af[0], (f32x16{})) : MFMA32(af[0], Wf[0], (f32x16{})); else acc = P1M(af[s & 3], Wf[s]);
            PIN(acc);
            if (s + 4 < 32) af[s & 3] = lds_frag(tb + (s + 4) * 32); else if (i < 7) af[s & 3] = lds_frag(tn + (s - 28) * 32);
            if (s == 2) st16(wr, dst + (i - 1) * 8192, pack8(accp, 0));
            if (s == 4) st16(wr, dst + (i - 1) * 8192 + 1024, pack8(accp, 1));
            if (i + 2 <= 7) {
                if (s == 6) p1_dma_t1<0>(c, i + 2, (j + 2) % 3);
                if (s == 11) p1_dma_t1<1>(c, i + 2, (j + 2) % 3);
                if (s == 16) p1_dma_t1<2>(c, i + 2, (j + 2) % 3);
                if (s == 21) p1_dma_t1<3>(c, i + 2, (j + 2) % 3);
            }
            SBAR();
        }
        accp = acc;
    }
    asm volatile("" : "+v"(pfd));
    st16(wr, dst + 7 * 8192, pack8(accp, 0)); st16(wr, dst + 7 * 8192 + 1024, pack8(accp, 1));
#undef P1M
}

constexpr float QSCALE = 0.08838834764831845f * 1.4426950408889634f;
constexpr float THR = 8.f;
constexpr int QCH = 49152;
constexpr int KRING = 0, VRING = 65536, SPARE = 131072, QA_OFF = VRING + 32768;
#define MX3(a, b, c) __builtin_fmaxf(__builtin_fmaxf((a), (b)), (c))

DI void p2_body(const Args& a, int b, int h, int r, int blkA, int blkB, char* lds, int wid, int lane, int tid, GAS u64* flags, u64 tag) {
    const unsigned lds0 = (unsigned)(uintptr_t)lds;
    const int r32 = lane & 31, hi = lane >> 5, rg = wid & 3, par = wid >> 2;
    const int myblk = par ? blkB : blkA;
    const char* xg = (const char*)a.x + (size_t)(128 * myblk + 32 * rg) * 1280;
    const unsigned xvoff = (unsigned)(lane >> 3) * 1280u + (unsigned)(((lane & 7) ^ ((lane >> 4) & 7)) * 16);
    const unsigned xvoff2 = (unsigned)(lane >> 3) * 1280u + 8 * 1280u + (unsigned)(((lane & 7) ^ ((4 + (lane >> 4)) & 7)) * 16);
    const unsigned xlds = lds0 + 16384 + wid * 4096;
    const char* wg = (const char*)a.wq + (size_t)h * 256 + (wid >> 2) * 128 + (size_t)(wid & 3) * 2 * 8 * 8192;
    const unsigned wvoff = (unsigned)(lane >> 3) * 8192u + (unsigned)(((lane & 7) ^ (((lane >> 4) & 1) << 2)) * 16);
    const unsigned wvoff2 = wvoff + 8 * 8192u;
    const unsigned wlds = lds0 + 16384 + 32768 + (wid >> 2) * 8192 + (wid & 3) * 2048;
    const int g4 = lane >> 4, q = (lane & 15) >> 2, p = lane & 3;
    const int hsw = (q >> 1) & 1, ysw = hi ^ ((r32 >> 1) & 7);
    const lds_cptr wrd0 = (lds_cptr)lds + 16384 + 32768 + (8 * (g4 >> 1) + q) * 128 + (g4 & 1) * 32 + p * 8 + hsw * 64;
    const lds_cptr wrd1 = (lds_cptr)lds + 16384 + 32768 + (8 * (g4 >> 1) + q) * 128 + (g4 & 1) * 32 + p * 8 + (hsw ^ 1) * 64;
    lds_cptr xrd[4];
#pragma unroll
    for (int s = 0; s < 4; ++s) xrd[s] = (lds_cptr)lds + 16384 + wid * 4096 + r32 * 128 + (((2 * s) ^ ysw) << 4);
    f32x16 acc[4] = {};
#pragma unroll
    for (int c = 0; c < 2; ++c) { const char* xp = xg + c * 128; const char* wp = wg + (size_t)c * 64 * 8192; const unsigned so_ = ((c + 2) % 3) * QCH;
        dma1(xlds + so_, xvoff, xp); dma1(xlds + so_ + 1024, xvoff2, xp); dma1(xlds + so_ + 2048, xvoff, xp + 16 * 1280); dma1(xlds + so_ + 3072, xvoff2, xp + 16 * 1280);
        dma1(wlds + so_, wvoff, wp); dma1(wlds + so_ + 1024, wvoff2, wp); }
    unsigned so = 2 * QCH;
    const char* xp = xg + 2 * 128; const char* wp = wg + (size_t)2 * 64 * 8192;
    const unsigned kl0 = lds0 + KRING + wid * 2048, tvoff = lane * 16;
    const char* Kw = a.ws + WS_K + (size_t)h * 64 * 8192 + wid * 2048;
#define WFR(B0, B1, i) tr_frag((((i) & 1) ? (B1) : (B0)) + (((i) >> 1) & 1) * 8192 + ((i) >> 2) * 2048, 512)
    bf16x8 wf[4], xf[2];
#define P2CHUNK(MODE, EXTRA, LAST) do { \
        const unsigned sn = so == 0 ? 2 * QCH : so - QCH, sx = so == 2 * QCH ? 0 : so + QCH;        \
        const unsigned xl_ = xlds + sn, wl_ = wlds + sn; const char* xp2 = xp + 16 * 1280; \
        const lds_cptr wb0 = wrd0 + so, wb1 = wrd1 + so, wn0 = wrd0 + sx, wn1 = wrd1 + sx; \
        _Pragma("unroll") for (int i = 0; i < 16; ++i) { \
            if (i == 12 && !(LAST)) { \
                if ((EXTRA) == 2 && wid == 0) poll_flags(flags, lane < 8 ? FL_F1 + h * 8 + lane : -1, tag); \
                if ((MODE) == 0) wait_bar<6>(); else wait_bar<4>(); \
                if ((EXTRA) == 1 && tid == 0) { flag_store(flags + FL_FW + (b & 7) * 32 + (b >> 3), tag); if (b < 160) flag_store(flags + FL_F4 + b, tag); else flag_store(flags + FL_F1 + h * 8 + r, tag); } \
                SBAR(); } \
            acc[i & 3] = MFMA32(wf[i & 3], xf[(i >> 2) & 1], acc[i & 3]); PIN(acc[i & 3]); \
            if (i + 4 < 16) wf[i & 3] = WFR(wb0, wb1, i + 4); else if (!(LAST)) wf[i & 3] = WFR(wn0, wn1, i - 12); \
            if ((i & 3) == 0 && i + 4 < 16) xf[((i >> 2) + 1) & 1] = lds_frag(xrd[(i >> 2) + 1] + so); \
            if (i == 12 && !(LAST)) xf[0] = lds_frag(xrd[0] + sx); \
            if ((MODE) == 0) { \
                if (i == 0) { SETM0(xl_, 0); } if (i == 1) { GLDS(xvoff, xp, 0); SETM0(xl_, 1024); } \
                if (i == 3) { GLDS(xvoff2, xp, 0); SETM0(xl_, 2048); } if (i == 5) { GLDS(xvoff, xp2, 0); SETM0(xl_, 3072); } \
                if (i == 7) { GLDS(xvoff2, xp2, 0); SETM0(wl_, 0); } if (i == 9) { GLDS(wvoff, wp, 0); SETM0(wl_, 1024); } \
                if (i == 11) { GLDS(wvoff2, wp, 0); } \
            } else { \
                const char* ku_ = Kw + (size_t)(2 * ((MODE) - 1)) * 16384; \
                if (i == 0) { SETM0(kl0, (2 * ((MODE) - 1)) * 16384); } if (i == 1) GLDSC(tvoff, ku_, 0); if (i == 4) GLDSC(tvoff, ku_, 1024); \
                if (i == 6) { SETM0(kl0, (2 * ((MODE) - 1) + 1) * 16384); } if (i == 8) GLDSC(tvoff, ku_ + 16384, 0); if (i == 11) GLDSC(tvoff, ku_ + 16384, 1024); \
            } \
            SBAR(); \
        } \
        so = sx; xp += 128; wp += (size_t)64 * 8192; } while (0)
    wait_bar<6>();
    xf[0] = lds_frag(xrd[0] + so);
#pragma unroll
    for (int i = 0; i < 4; ++i) wf[i] = WFR(wrd0 + so, wrd1 + so, i);
    SBAR();
    P2CHUNK(0, 0, false);
    P2CHUNK(0, 1, false);
#pragma unroll 1
    for (int c = 2; c < 7; ++c) P2CHUNK(0, 0, false);
    P2CHUNK(0, 2, false);
    P2CHUNK(1, 0, false);
    P2CHUNK(2, 0, true);
#undef P2CHUNK
    wait_bar<63>();
    lds_ptr qw = (lds_ptr)lds + (par ? SPARE : QA_OFF) + rg * 8192 + lane * 16;
#pragma unroll
    for (int db = 0; db < 4; ++db) {
#pragma unroll
        for (int e = 0; e < 16; ++e) acc[db][e] *= QSCALE;
        *(LAS u32x4*)(qw + (2 * db) * 1024) = pack8(acc[db], 0);
        *(LAS u32x4*)(qw + (2 * db + 1) * 1024) = pack8(acc[db], 1);
    }
#undef WFR
}

DI void p3_body(rsrc_t wr, const char* ws, int h, int blkA, int blkB, char* lds, int wid, int lane, int tid, GAS u64* flags, u64 tag) {
    const unsigned lds0 = (unsigned)(uintptr_t)lds;
    const int r32 = lane & 31, hi = lane >> 5, rg = wid & 3, par = wid >> 2;
    const unsigned tvoff = lane * 16;
    const unsigned kl0 = lds0 + KRING + wid * 2048, vl0 = lds0 + VRING + wid * 2048;
    const char* Kw = ws + WS_K + (size_t)h * 64 * 8192 + wid * 2048; const char* Vw = ws + WS_V + (size_t)h * 64 * 8192 + wid * 2048;
    const lds_cptr krd = (lds_cptr)lds + KRING + par * 8192 + lane * 16;
    const lds_cptr vrd = (lds_cptr)lds + VRING + par * 8192 + lane * 16;
    int g0 = 0;
    bf16x8 qf[8];
#define VF(i) ((((i) & 3) * 2 + ((i) >> 2)) * 1024)
#define MFMA_C(D, A, B, C) asm volatile("v_mfma_f32_32x32x16_bf16 %0, %2, %3, %1" : "=&v"(D), "+v"(C) : "v"(A), "v"(B))
#define SLOT(X, Y, PN, PP, ODD, QK, SM, PV, FIRST, DM, PFK, MASK) do { \
        unsigned long long fire = 0; \
        _Pragma("unroll") for (int i = 0; i < 16; ++i) { \
            if (i < 8 && QK && !(i & 1)) { LGKM(2); SBAR(); } \
            if (i >= 8 && PV && !(i & 1)) { LGKM(2); SBAR(); } \
            if (i < 8) { if (QK) { if (i == 0) { if (FIRST) X = MFMA32(kf[0], qf[0], zero16); else MFMA_C(X, kf[0], qf[0], negm); } else X = MFMA32(kf[i & 3], qf[i], X); PIN(X); \
                                   if (i < 4) kf[i] = lds_frag(kp + ((ODD) ? 0 : 16384) + (i + 4) * 1024); } \
                         if (PV && i >= 4) vf[i - 4] = lds_frag(vp + ((ODD) ? 0 : 16384) + VF(i - 4)); } \
            else { if (PV) { o[i & 3] = MFMA32(vf[i & 3], __builtin_bit_cast(bf16x8, PP[(i - 8) >> 2]), o[i & 3]); PIN(o[i & 3]); if (i < 12) vf[i & 3] = lds_frag(vp + ((ODD) ? 0 : 16384) + VF(i - 4)); } \
                   if (PFK && i >= 12) kf[i - 12] = lds_frag(kp + ((ODD) ? 16384 : 0) + (i - 12) * 1024); } \
            if (!(ODD) && i == 5) { kp = (lds_cptr)(uintptr_t)((unsigned)(uintptr_t)kp ^ 0x8000u); PIN(kp); } \
            if (!(ODD) && i == 13) { vp = (lds_cptr)(uintptr_t)((unsigned)(uintptr_t)vp ^ 0x8000u); PIN(vp); } \
            if (DM) { \
                if (i == 0) SETM0(dk, (ODD) ? 16384 : 0); \
                if (i == 1) GLDSC(uoff, Kw, 0); \
                if (i == 3) GLDSC(uoff, Kw, 1024); \
                if ((DM) & 2) { \
                    if (i == 4) SETM0(dv, (ODD) ? 16384 : 0); \
                    if (i == 5) GLDSC(uoffv, Vw, 0); \
                    if (i == 7) GLDSC(uoffv, Vw, 1024); \
                    if (i == 11) { uoffv += 16384u; PIN(uoffv); } } \
                if (i == 8) { uoff += (j + 5 == n) ? 16384u * (unsigned)(1 - n) : 16384u; PIN(uoff); } \
                if ((ODD) && i == 9) { dk ^= 0x8000u; dv ^= 0x8000u; PINS(dk); PINS(dv); } } \
            if (SM) { \
                if (i == 0) { Y[0] = EXP2(Y[0]); Y[1] = EXP2(Y[1]); } else if (i < 15) Y[i + 1] = EXP2(Y[i + 1]); \
                if (i == 1) l2 += Y[0]; \
                if (i >= 1) { if (i & 1) l += Y[i]; else l2 += Y[i]; PIN(l); PIN(l2); } \
                if (i & 1) { PN[i >> 3][(i >> 1) & 3] = cvtpk(Y[i - 1], Y[i]); PIN(PN[i >> 3]); } \
                if (i < 15) PIN(Y); } \
            if (QK) { \
                if (MASK && i == 9 && (MASK == 1 || j + 1 >= n - 2)) { int qq = qrel - 64 * (j + 1 - (n - 2)); PIN(qq); \
                    _Pragma("unroll") for (int r = 0; r < 16; ++r) { const int c = (r & 3) + 8 * (r >> 2); if (c > qq) X[r] = -INFINITY; } } \
                if (i == 9) { mxa = MX3(X[0], X[1], X[2]); PIN(mxa); } \
                if (i == 10) { mxb = MX3(X[3], X[4], X[5]); mxa = MX3(mxa, X[6], X[7]); PIN(mxb); PIN(mxa); } \
                if (i == 11) { mxb = MX3(mxb, X[8], X[9]); PIN(mxb); } \
                if (i == 12) { mxa = MX3(mxa, X[10], X[11]); mxb = MX3(mxb, X[12], X[13]); PIN(mxb); PIN(mxa); } \
                if (i == 13) { mxa = MX3(mxa, X[14], X[15]); PIN(mxa); } \
                if (i == 14) { mxa = MX3(mxa, mxb, X[0]); PIN(mxa); } \
                if (i == 15 && !(FIRST)) { fire = __ballot(mxa > THR); PINS(fire); } } \
            SBAR(); \
        } \
        if (__builtin_expect((fire | pend) != 0, 0) || FIRST) { \
            if (PV && pend) { _Pragma("unroll") for (int d = 0; d < 4; ++d) _Pragma("unroll") for (int r = 0; r < 16; ++r) o[d][r] *= alp; } \
            pend = 0; \
            if (QK && (FIRST || fire)) { \
                auto rr = __builtin_amdgcn_permlane32_swap(__float_as_uint(mxa), __float_as_uint(mxa), false, false); \
                const float rm = __builtin_fmaxf(__uint_as_float(rr[0]), __uint_as_float(rr[1])); \
                if (FIRST) { const float dl = __builtin_fmaxf(rm, -1e30f); m = dl; \
                    _Pragma("unroll") for (int r = 0; r < 16; ++r) { X[r] -= dl; negm[r] = -dl; } PIN(negm); } \
                else { const float dl = __builtin_fmaxf(rm, 0.f); m += dl; alp = EXP2(-dl); l *= alp; l2 *= alp; pend = 1; \
                    const float ndl = -dl, aone = hi ? 0.f : 1.f; \
                    asm volatile("s_nop 1\n\tv_mfma_f32_32x32x2_f32 %0, %2, %3, %0\n\tv_mfma_f32_32x32x2_f32 %1, %2, %3, %1\n\ts_nop 7\n\ts_nop 7\n\ts_nop 7" : "+v"(X), "+v"(negm) : "v"(aone), "v"(ndl)); } } } \
        SBAR(); \
        } while (0)
#define TBAR() wait_bar_v<4>()
    const f32x16 zero16 = {};
#pragma unroll 1
    for (int pass = 0; pass < 2; ++pass) {
        const int blk = pass ? blkB : blkA, n = 2 * (blk + 1);
        dma2c(vl0 + (g0 & 3) * 16384, tvoff, Vw); dma2c(vl0 + ((g0 + 1) & 3) * 16384, tvoff, Vw + 16384);
        if (pass == 0) wait_bar<4>();
        if (pass == 0) {
            const lds_cptr qr = (lds_cptr)lds + QA_OFF + rg * 8192 + lane * 16;
#pragma unroll
            for (int s = 0; s < 8; ++s) qf[s] = lds_frag(qr + s * 1024);
        }
        float m = -1e30f, l = 0.f, l2 = 0.f, alp = 1.f, mxa = 0.f, mxb = 0.f; unsigned long long pend = 0; f32x16 o[4] = {};
        u32x4 pk0[2] = {}, pk1[2] = {};
        f32x16 s0, s1, negm;
        bf16x8 kf[4], vf[4] = {};
        const int qrel = 32 * rg + r32 - 32 * par - 4 * hi;
        lds_cptr kp = krd + (g0 & 2) * 16384, vp = vrd + ((g0 & 2) ^ 2) * 16384;
        unsigned dk = kl0 + (g0 & 2) * 16384, dv = vl0 + ((g0 + 2) & 2) * 16384;
        unsigned uoffv = tvoff + 2 * 16384;
        unsigned uoff = tvoff + ((4 < n) ? 4 : 4 - n) * 16384;
#pragma unroll
        for (int i = 0; i < 4; ++i) kf[i] = lds_frag(kp + i * 1024);
        int j = -1;
        SLOT(s0, s1, pk1, pk0, true, true, false, false, true, false, true, 2);
        wait_bar_v<63>();
        j = 0;
        SLOT(s1, s0, pk0, pk1, false, true, true, false, false, 3, true, 2);
        TBAR();
        if (pass == 1 && tid == 0) flag_store(flags + FL_F3 + blkA * 32 + h, tag);
#pragma unroll 1
        for (j = 1; j < n - 3; j += 2) {
            SLOT(s0, s1, pk1, pk0, true, true, true, true, false, 3, true, 0);
            TBAR();
            ++j;
            SLOT(s1, s0, pk0, pk1, false, true, true, true, false, 3, true, 0);
            TBAR();
            --j;
        }
        if (n >= 4) {
            j = n - 3;
            SLOT(s0, s1, pk1, pk0, true, true, true, true, false, 3, true, 1);
            TBAR();
            j = n - 2;
            SLOT(s1, s0, pk0, pk1, false, true, true, true, false, 1, true, 1);
            wait_bar_v<2>();
        }
        j = n - 1;
        SLOT(s0, s1, pk1, pk0, true, false, true, true, false, 1, false, 0);
        wait_bar_v<2>();
        if (pass == 0) {
            const lds_cptr qr = (lds_cptr)lds + SPARE + rg * 8192 + lane * 16;
#pragma unroll
            for (int s = 0; s < 8; ++s) qf[s] = lds_frag(qr + s * 1024);
        }
        j = n;
        SLOT(s1, s0, pk0, pk1, false, false, false, true, false, false, false, 0);
        g0 += n;
        STAMP(4 + 2 * pass);
        {
            l += l2;
            auto rr = __builtin_amdgcn_permlane32_swap(__float_as_uint(l), __float_as_uint(l), false, false);
            l = __uint_as_float(rr[0]) + __uint_as_float(rr[1]);
            wait_bar<63>();
            LAS float* ml = (LAS float*)((lds_ptr)lds + SPARE);
            *(LAS f32x2*)(ml + (wid * 64 + lane) * 2) = (f32x2){m, l};
            lds_ptr sendp = (lds_ptr)lds + VRING + par * 32768 + rg * 8192 + lane * 16;
            lds_cptr recvp = (lds_cptr)lds + VRING + (par ^ 1) * 32768 + rg * 8192 + lane * 16;
            f32x16 keep[2];
            if (par) {
#pragma unroll
                for (int d = 0; d < 2; ++d) { keep[d] = o[2 + d];
#pragma unroll
                    for (int g = 0; g < 4; ++g) *(LAS f32x4*)(sendp + (d * 4 + g) * 1024) = (f32x4){o[d][4 * g], o[d][4 * g + 1], o[d][4 * g + 2], o[d][4 * g + 3]}; }
            } else {
#pragma unroll
                for (int d = 0; d < 2; ++d) { keep[d] = o[d];
#pragma unroll
                    for (int g = 0; g < 4; ++g) *(LAS f32x4*)(sendp + (d * 4 + g) * 1024) = (f32x4){o[2 + d][4 * g], o[2 + d][4 * g + 1], o[2 + d][4 * g + 2], o[2 + d][4 * g + 3]}; }
            }
            wait_bar<63>();
            const f32x2 mlo = *(const LAS f32x2*)(ml + ((wid ^ 4) * 64 + lane) * 2);
            const float mm = __builtin_fmaxf(m, mlo[0]), fo = EXP2(m - mm), fx = EXP2(mlo[0] - mm);
            const float inv = __builtin_amdgcn_rcpf(l * fo + mlo[1] * fx), sc = fo * inv, sx = fx * inv;
            const unsigned cdst = (unsigned)(WS_CTX + ((size_t)(blk * 4 + rg) * 32 + h) * 8192 + (par * 4) * 1024) + lane * 16;
#pragma unroll
            for (int d = 0; d < 2; ++d) {
#pragma unroll
                for (int g = 0; g < 4; ++g) {
                    const f32x4 v4 = *(const LAS f32x4*)(recvp + (d * 4 + g) * 1024);
#pragma unroll
                    for (int e = 0; e < 4; ++e) keep[d][4 * g + e] = keep[d][4 * g + e] * sc + v4[e] * sx;
                }
                st16(wr, cdst + (2 * d) * 1024, pack8(keep[d], 0));
                st16(wr, cdst + (2 * d + 1) * 1024, pack8(keep[d], 1));
            }
            if (pass == 0) wait_bar<63>();
            else signal_flag(flags + FL_F3 + blk * 32 + h, tag, tid);
        }
        STAMP(5 + 2 * pass);
    }
#undef SLOT
#undef TBAR
#undef VF
}

constexpr int CSTR = 4160;
constexpr int CCH = 4 * CSTR + 4608;
DI void p4_body(const Args& a, rsrc_t wr, int b, int h, int r, char* lds, int wid, int lane, int tid, GAS u64* flags, u64 tag) {
    const int ch = wid >> 1, c = 4 * b + ch, half = wid & 1;
    lds_ptr base = (lds_ptr)lds + ch * CCH;
    const lds_cptr ur = (lds_cptr)lds + STG_UR + ch * 4096, hs = (lds_cptr)lds + STG_H + ch * 4096;
    u32x4 w0[2], w1[2], hv[2];
#pragma unroll
    for (int it = 0; it < 2; ++it) { const int y8 = half * 128 + it * 64 + lane;
        w0[it] = *(const LAS u32x4*)(ur + 16 * y8); w1[it] = *(const LAS u32x4*)(ur + 16 * (y8 < 255 ? y8 + 1 : y8)); if (y8 == 255) w1[it] = (u32x4){0u, 0u, 0u, 0u};
        hv[it] = *(const LAS u32x4*)(hs + 16 * y8); }
    wait_bar<63>();
    const float Dc = bf2f(a.ssmD[c]);
#pragma unroll
    for (int it = 0; it < 2; ++it) {
        const int y8 = half * 128 + it * 64 + lane;
        unsigned w[6] = {w0[it][0], w0[it][1], w0[it][2], w0[it][3], w1[it][0], w1[it][1]}, o[5];
#pragma unroll
        for (int e = 0; e < 5; ++e) o[e] = __builtin_amdgcn_alignbit(w[e + 1], w[e], 16);
#pragma unroll
        for (int sg = 0; sg < 4; ++sg) { const int e = sg >> 1; u32x4 v;
            if (sg & 1) v = (u32x4){o[e], o[e + 1], o[e + 2], o[e + 3]}; else v = (u32x4){w[e], w[e + 1], w[e + 2], w[e + 3]};
            *(LAS u32x4*)(base + sg * CSTR + y8 * 16) = v; }
        *(LAS u32x4*)(base + 4 * CSTR + 512 + y8 * 16) = hv[it];
    }
    if (half == 1 && lane < 8) { const int sg = lane >> 1, y8 = 256 + (lane & 1); *(LAS u32x4*)(base + sg * CSTR + y8 * 16) = (u32x4){0u, 0u, 0u, 0u}; }
    if (half == 0 && lane < 32) *(LAS u32x4*)(base + 4 * CSTR + lane * 16) = (u32x4){0u, 0u, 0u, 0u};
    wait_bar<63>();
    const int j = lane & 15, g = lane >> 4;
    lds_cptr ard = (lds_cptr)base + ((3 - j) & 3) * CSTR + (2031 - j - ((3 - j) & 3) + 8 * g) * 2 - half * 64 - 62 * 64, ard2 = ard + 8;
    const lds_cptr brd = (lds_cptr)base + 4 * CSTR + (240 + 16 * j + 8 * g) * 2 - half * 64 - 6 * 64;
    PIN(ard2);
    f32x4 acc[8] = {};
#define P4LOAD(A, B, RC) do { _Pragma("unroll") for (int u = 0; u < 8; ++u) { const s16x4 lo = *(const LAS s16x4*)(ard + (62 - (RC) - 8 * u) * 64), hi2 = *(const LAS s16x4*)(ard2 + (62 - (RC) - 8 * u) * 64); \
            A[u] = __builtin_shufflevector(lo, hi2, 0, 1, 2, 3, 4, 5, 6, 7); B[u] = lds_frag(brd + (8 * u - (RC) + 6) * 64); } } while (0)
#define P4MM(A, B) do { _Pragma("unroll") for (int u = 0; u < 8; ++u) _Pragma("unroll") for (int v = 0; v + u < 8; ++v) { acc[u + v] = MFMA16(A[u], B[v], acc[u + v]); PIN(acc[u + v]); SBAR(); } } while (0)
    {
        bf16x8 A0[8], B0[8], A1[8], B1[8];
        P4LOAD(A0, B0, 0); SBAR(); P4LOAD(A1, B1, 2); SBAR();
        P4MM(A0, B0);
        P4LOAD(A0, B0, 4); SBAR();
        P4MM(A1, B1);
        P4LOAD(A1, B1, 6); SBAR();
        P4MM(A0, B0);
        P4MM(A1, B1);
    }
#undef P4LOAD
#undef P4MM
    wait_bar<0>();
    if (tid == 0) flag_store(flags + FL_F1 + h * 8 + r, tag);
    lds_ptr ex = base + CSTR;
    f32x4 keep[4];
    if (half) {
#pragma unroll
        for (int q = 0; q < 4; ++q) { *(LAS f32x4*)(ex + 4096 + q * 1024 + lane * 16) = acc[q]; keep[q] = acc[4 + q]; }
    } else {
#pragma unroll
        for (int q = 0; q < 4; ++q) { *(LAS f32x4*)(ex + q * 1024 + lane * 16) = acc[4 + q]; keep[q] = acc[q]; }
    }
    wait_bar<63>();
    u32x2 zp[4];
#pragma unroll
    for (int q = 0; q < 4; ++q) {
        const f32x4 o = *(const LAS f32x4*)(ex + (half ^ 1) * 4096 + q * 1024 + lane * 16);
        const f32x4 y = keep[q] + o;
        const int t = 256 * (half * 4 + q) + 16 * j + 4 * g;
        const s16x4 kvr = *(const LAS s16x4*)(base + (2047 - t - 3) * 2);
        float z[4];
#pragma unroll
        for (int r = 0; r < 4; ++r) { const float kv = bf2f((u16)kvr[3 - r]); z[r] = y[r] + kv * Dc; }
        zp[q] = (u32x2){cvtpk(z[0], z[1]), cvtpk(z[2], z[3])};
    }
#pragma unroll
    for (int q = 0; q < 4; q += 2) {
        auto s0 = __builtin_amdgcn_permlane16_swap(zp[q][0], zp[q + 1][0], false, false);
        auto s1 = __builtin_amdgcn_permlane16_swap(zp[q][1], zp[q + 1][1], false, false);
        const int t = 256 * (half * 4 + q + (g & 1)) + 16 * j + 8 * (g >> 1);
        st16(wr, (unsigned)(WS_Z + ((size_t)c * S + t) * 2), (u32x4){s0[0], s1[0], s0[1], s1[1]});
    }
}

constexpr int OCH = 40960;
DI u32x4 ld16c(rsrc_t r, unsigned off) { return __builtin_amdgcn_raw_buffer_load_b128(r, off, 0, 16); }
DI void p5_body(const Args& a, rsrc_t wr, int mt, int nt, int ks, char* lds, int wid, int lane, int tid, GAS u64* flags, u64 tag) {
    const unsigned lds0 = (unsigned)(uintptr_t)lds;
    const int rg = wid & 3, kh = wid >> 2;
    const int j = lane & 15, g = lane >> 4;
    const unsigned tv = lane * 16;
    const char* wg = a.ws + WS_WO + ((size_t)(2 * nt + ((wid >> 1) & 1)) * 32 + 8 * ks + 4 * (wid >> 2)) * 20480 + (wid & 1) * 5120;
    const unsigned wl = lds0 + (wid >> 1) * 10240 + (wid & 1) * 5120;
#define WLOAD(slot, st) do { const char* p_ = wg + (size_t)((st) >> 1) * 20480 + ((st) & 1) * 10240; \
        asm volatile("s_mov_b32 m0, %0\n\ts_nop 0\n\tglobal_load_lds_dwordx4 %1, %2 sc1\n\tglobal_load_lds_dwordx4 %1, %2 offset:1024 sc1\n\tglobal_load_lds_dwordx4 %1, %2 offset:2048 sc1\n\t" \
                     "global_load_lds_dwordx4 %1, %2 offset:3072 sc1" :: "s"(wl + (slot) * OCH), "v"(tv), "s"(p_) : "memory"); \
        dma1c(wl + (slot) * OCH + 4096, tv, p_ + 4096); } while (0)
    WLOAD(0, 0); WLOAD(1, 1);
    if (wid == 0) {
        poll_flags(flags, lane < 40 ? FL_F4 + nt * 40 + lane : (lane < 44 ? FL_F0 + (4 * (ks & 1) + lane - 40) * 32 + 2 * mt + (ks >> 1) : -1), tag);
        poll_flags(flags, lane < 8 ? FL_F3 + mt * 32 + 8 * ks + lane : -1, tag);
    }
    wait_bar<63>();
    const char* cg = a.ws + WS_CTX + ((size_t)(4 * mt + rg) * 32 + 8 * ks + 4 * kh) * 8192;
    const unsigned cv = (g >> 1) * 1024 + ((g & 1) * 32 + j) * 16;
    u32x4 cf[3][4];
#define CLOAD(slot, st) do { const char* p_ = cg + (size_t)((st) >> 1) * 8192 + ((st) & 1) * 4096; \
        asm volatile("global_load_dwordx4 %0, %4, %5 sc1\n\tglobal_load_dwordx4 %1, %4, %5 offset:256 sc1\n\tglobal_load_dwordx4 %2, %4, %5 offset:2048 sc1\n\tglobal_load_dwordx4 %3, %4, %5 offset:2304 sc1" \
                     : "=&v"(cf[slot][0]), "=&v"(cf[slot][1]), "=&v"(cf[slot][2]), "=&v"(cf[slot][3]) : "v"(cv), "s"(p_) : "memory"); } while (0)
    CLOAD(0, 0); CLOAD(1, 1);
    unsigned zr[5];
#pragma unroll
    for (int it = 0; it < 5; ++it) { const int e = tid + it * 512, cl2 = e >> 4, d = e & 15;
        zr[it] = ld4c(wr, (unsigned)(WS_Z + ((size_t)(nt * 160 + cl2) * S + mt * 128 + ks * 32 + 2 * d) * 2)); }
    u32x2 xv[3]; float gt[3];
#pragma unroll
    for (int k = 0; k < 3; ++k) { const int i = wid + 8 * k < 20 ? wid + 8 * k : wid, t = mt * 128 + ks * 32 + 16 * ((i % 10) / 5) + j, c = nt * 160 + 80 * (i / 10) + 16 * (i % 5) + 4 * g;
        xv[k] = *(const u32x2*)(a.x + (size_t)t * M + c); gt[k] = __uint_as_float(ld4c(wr, (unsigned)(WS_G + t * 4))); }
    const lds_cptr brd = (lds_cptr)lds + kh * 20480 + lane * 16;
    f32x4 acc[2][10] = {};
#pragma unroll
    for (int st = 0; st < 8; ++st) {
        if (st == 0) wait_bar<4>(); else if (st < 7) wait_bar<9>(); else wait_bar<0>();
        asm volatile("" : "+v"(cf[st % 3][0]), "+v"(cf[st % 3][1]), "+v"(cf[st % 3][2]), "+v"(cf[st % 3][3]));
        if (st == 0) { _Pragma("unroll") for (int k = 0; k < 3; ++k) { PIN(xv[k]); PIN(gt[k]); } _Pragma("unroll") for (int it = 0; it < 5; ++it) PIN(zr[it]); }
        const lds_cptr bb = brd + (st % 3) * OCH;
        bf16x8 wf[4];
#define WFI(i) lds_frag(bb + (((i) / 5) & 1) * 10240 + (((i) / 10) * 5 + (i) % 5) * 1024)
#pragma unroll
        for (int i = 0; i < 4; ++i) wf[i] = WFI(i);
        SBAR();
#pragma unroll
        for (int i = 0; i < 20; ++i) {
            const int ks2 = i / 10, c10 = i % 10;
            acc[0][c10] = MFMA16(wf[i & 3], __builtin_bit_cast(bf16x8, cf[st % 3][2 * ks2]), acc[0][c10]); PIN(acc[0][c10]); SBAR();
            acc[1][c10] = MFMA16(wf[i & 3], __builtin_bit_cast(bf16x8, cf[st % 3][2 * ks2 + 1]), acc[1][c10]); PIN(acc[1][c10]);
            if (i + 4 < 20) wf[i & 3] = WFI(i + 4);
            if (st + 2 < 8) { if (i == 1) WLOAD((st + 2) % 3, st + 2); if (i == 10) CLOAD((st + 2) % 3, st + 2); }
            SBAR();
        }
#undef WFI
    }
#undef WLOAD
#undef CLOAD
    wait_bar<0>();
    {
        lds_ptr sp = (lds_ptr)lds + wid * 10240 + lane * 16; lds_cptr rp = (lds_cptr)lds + (wid ^ 4) * 10240 + lane * 16;
        f32x4 keep[10];
        if (kh) {
#pragma unroll
            for (int i = 0; i < 10; ++i) { *(LAS f32x4*)(sp + i * 1024) = acc[i / 5][i % 5]; keep[i] = acc[i / 5][5 + i % 5]; }
        } else {
#pragma unroll
            for (int i = 0; i < 10; ++i) { *(LAS f32x4*)(sp + i * 1024) = acc[i / 5][5 + i % 5]; keep[i] = acc[i / 5][i % 5]; }
        }
        lds_ptr zb = (lds_ptr)lds + 81920;
#pragma unroll
        for (int it = 0; it < 5; ++it) { const int e = tid + it * 512, cl2 = e >> 4, d = e & 15; *(LAS unsigned*)(zb + cl2 * 68 + d * 4) = zr[it]; }
        wait_bar<63>();
#pragma unroll
        for (int i = 0; i < 10; ++i) keep[i] += *(const LAS f32x4*)(rp + i * 1024);
        const int tile = mt * 4 + nt;
        if (rg != ks) {
            const unsigned dst = (unsigned)(WS_SL + (((size_t)tile * 4 + rg) * 4 + ks) * 20480 + kh * 10240) + lane * 16;
#pragma unroll
            for (int i = 0; i < 10; ++i) st16(wr, dst + i * 1024, __builtin_bit_cast(u32x4, keep[i]));
        } else {
            lds_ptr op = (lds_ptr)lds + 98304 + kh * 10240 + lane * 16;
#pragma unroll
            for (int i = 0; i < 10; ++i) *(LAS f32x4*)(op + i * 1024) = keep[i];
        }
        signal_flag(flags + FL_F5 + tile * 4 + ks, tag, tid);
        if (wid == 0) poll_flags(flags, (lane < 4 && lane != ks) ? FL_F5 + tile * 4 + lane : -1, tag);
        wait_bar<63>();
        u32x4 pf[3][3];
#pragma unroll
        for (int k = 0; k < 3; ++k) { const int i = wid + 8 * k < 20 ? wid + 8 * k : wid;
            const unsigned src = (unsigned)(WS_SL + ((size_t)tile * 4 + ks) * 4 * 20480) + i * 1024 + lane * 16;
#pragma unroll
            for (int f = 0; f < 3; ++f) pf[k][f] = ld16c(wr, src + (unsigned)(f + (f >= ks ? 1 : 0)) * 20480u); }
#pragma unroll
        for (int k = 0; k < 3; ++k) if (wid + 8 * k < 20) {
            const int i = wid + 8 * k, tl = 16 * ((i % 10) / 5) + j, cl = 80 * (i / 10) + 16 * (i % 5) + 4 * g;
            f32x4 av = *(const LAS f32x4*)((lds_cptr)lds + 98304 + i * 1024 + lane * 16);
#pragma unroll
            for (int f = 0; f < 3; ++f) av += __builtin_bit_cast(f32x4, pf[k][f]);
            float r4[4];
#pragma unroll
            for (int e = 0; e < 4; ++e) { const float z = bf2f(*(const LAS u16*)((lds_cptr)lds + 81920 + (cl + e) * 68 + tl * 2));
                const float xe = (e & 1) ? bhi(xv[k][e >> 1]) : blo(xv[k][e >> 1]);
                r4[e] = gt[k] * av[e] + (1.f - gt[k]) * (xe * z); }
            *(u32x2*)(a.out + (size_t)(mt * 128 + ks * 32 + tl) * M + nt * 160 + cl) = (u32x2){cvtpk(r4[0], r4[1]), cvtpk(r4[2], r4[3])};
        }
    }
}

__global__ __launch_bounds__(512, 2) void k_fused(const u16* x, const u16* ckv, const u16* wq, const u16* wuk, const u16* wuv, const u16* wo, const u16* bk, const u16* bv,
                                                  const u16* shk, const u16* ssmk, const u16* ssmD, const u16* gw, u16* out, char* ws) {
    extern __shared__ __attribute__((aligned(16))) char lds[];
    const Args a = {x, ckv, wq, wuk, wuv, wo, bk, bv, shk, ssmk, ssmD, gw, out, ws};
    const int tid = threadIdx.x, lane = tid & 63, wid = rfl(tid >> 6);
    const int b = blockIdx.x, xcd = b & 7, idx = b >> 3, h = xcd * 4 + (idx >> 3), r = idx & 7;
    const unsigned lds0 = (unsigned)(uintptr_t)lds;
    const rsrc_t wr = __builtin_amdgcn_make_buffer_rsrc(ws, 0, (int)WS_END, 0x00020000);
    GAS u64* flags = (GAS u64*)(ws + WS_F);
    STAMP(0);
    const P1Ctx c1 = p1_setup(a, h, r, lds0, wid, lane);
    const u64 tag = *(const __attribute__((address_space(4))) u64*)(ws + WS_F) + 1;
    if (b >= 160) {
        u32x4 xg[3][2];
        const u32x4 g0 = *(const u32x4*)(gw + lane * 8), g1 = *(const u32x4*)(gw + 512 + (lane & 15) * 8);
#pragma unroll
        for (int k = 0; k < 3; ++k) { int jb = b - 160 + 96 * k; if (jb >= 256) jb = b - 160; const u16* xr = x + (size_t)(64 * (jb >> 3) + 8 * (jb & 7) + wid) * M;
            xg[k][0] = *(const u32x4*)(xr + lane * 8); xg[k][1] = *(const u32x4*)(xr + 512 + (lane & 15) * 8); }
        p0a_load(a, b - 160, lds0, wid, lane);
        p1_prime_t(c1);
        STAMP(11);
        wait_bar<4>();
#pragma unroll
        for (int k = 0; k < 3; ++k) { PIN(xg[k][0]); PIN(xg[k][1]); }
        STAMP(12);
        p0a_compute(b - 160, lds, tid);
        wait_bar<63>();
        STAMP(13);
        p1_prime_w(c1);
        p0a_store(wr, b - 160, lds, tid);
#pragma unroll
        for (int k = 0; k < 3; ++k) { const int jb = b - 160 + 96 * k; float sum = 0.f, s1 = 0.f;
#pragma unroll
            for (int e = 0; e < 4; ++e) { sum += blo(xg[k][0][e]) * blo(g0[e]) + bhi(xg[k][0][e]) * bhi(g0[e]); s1 += blo(xg[k][1][e]) * blo(g1[e]) + bhi(xg[k][1][e]) * bhi(g1[e]); }
            if (lane < 16) sum += s1;
            sum = wave_sum(sum);
            if (lane == 0 && jb < 256) st4(wr, (unsigned)(WS_G + (64 * (jb >> 3) + 8 * (jb & 7) + wid) * 4), __float_as_uint(1.f / (1.f + __expf(-sum)))); }
    } else { p1_prime_t(c1); p1_prime_w(c1); }
    STAMP(1);
    if (wid < 4) p1_body<0>(a, c1, wr, b, h, r, lds, wid, lane, tid, flags, tag); else p1_body<1>(a, c1, wr, b, h, r, lds, wid, lane, tid, flags, tag);
    STAMP(2);
    wait_bar<63>();
    p0b_finish(wr, b, lds, wid, lane);
    STAMP(14);
    if (b < 160) p4_body(a, wr, b, h, r, lds, wid, lane, tid, flags, tag);
    wait_bar<63>();
    STAMP(8);
    p2_body(a, b, h, r, 15 - r, r, lds, wid, lane, tid, flags, tag);
    STAMP(3);
    p3_body(wr, ws, h, 15 - r, r, lds, wid, lane, tid, flags, tag);
    {
        const int ks = xcd >> 1, mt = (xcd & 1) * 8 + (idx >> 2), nt = idx & 3;
        if (wid == 0) poll_flags(flags, lane < 16 ? FL_FW + (2 * nt + (lane >> 3)) * 32 + 8 * ks + (lane & 7) : -1, tag);
        wait_bar<0>();
        STAMP(9);
        p5_body(a, wr, mt, nt, ks, lds, wid, lane, tid, flags, tag);
    }
    if (b == 0 && tid == 0) flag_store(flags, tag);
    STAMP(10);
}

extern "C" void kernel_launch(void* const* d_in, const int* in_sizes, int n_in,
                              void* d_out, int out_size, void* d_ws, size_t ws_size,
                              hipStream_t stream) {
    static bool attr = false;
    if (!attr) { (void)hipFuncSetAttribute((const void*)k_fused, hipFuncAttributeMaxDynamicSharedMemorySize, LDS_BYTES); attr = true; }
    k_fused<<<256, 512, LDS_BYTES, stream>>>((const u16*)d_in[0], (const u16*)d_in[1], (const u16*)d_in[2], (const u16*)d_in[3], (const u16*)d_in[4], (const u16*)d_in[5],
                                             (const u16*)d_in[7], (const u16*)d_in[8], (const u16*)d_in[9], (const u16*)d_in[10], (const u16*)d_in[11], (const u16*)d_in[12],
                                             (u16*)d_out, (char*)d_ws);
}
```

```cpp
#include <hip/hip_runtime.h>
#include <hip/hip_bf16.h>
#include <stdint.h>

typedef unsigned short u16;
typedef unsigned long long u64;
typedef short bf16x8 __attribute__((ext_vector_type(8)));
typedef short s16x4 __attribute__((ext_vector_type(4)));
typedef float f32x16 __attribute__((ext_vector_type(16)));
typedef float f32x4 __attribute__((ext_vector_type(4)));
typedef float f32x2 __attribute__((ext_vector_type(2)));
typedef __bf16 bf16x2_t __attribute__((ext_vector_type(2)));
typedef unsigned u32x4 __attribute__((ext_vector_type(4)));
typedef unsigned u32x2 __attribute__((ext_vector_type(2)));
typedef short v4i16_t __attribute__((ext_vector_type(4)));
#define LAS __attribute__((address_space(3)))
#define GAS __attribute__((address_space(1)))
typedef LAS const char* lds_cptr;
typedef LAS char* lds_ptr;
#define DI __device__ __forceinline__
#define SBAR() __builtin_amdgcn_sched_barrier(0)
#define PIN(x) asm volatile("" : "+v"(x))
#define PINS(x) asm volatile("" : "+s"(x))
#define MFMA32(a, b, c) __builtin_amdgcn_mfma_f32_32x32x16_bf16(a, b, c, 0, 0, 0)
#define MFMA16(a, b, c) __builtin_amdgcn_mfma_f32_16x16x32_bf16(a, b, c, 0, 0, 0)
#define LGKM(n) __builtin_amdgcn_s_waitcnt(0xC07F | ((n) << 8))
#define EXP2(v) __builtin_amdgcn_exp2f(v)

constexpr int S = 2048, H = 32, D = 128, M = 640, DC = 512, HD = H * D;
constexpr int LDS_BYTES = 163840;
constexpr size_t WS_K = 0, WS_V = (size_t)S * HD * 2, WS_CTX = 2 * WS_V, WS_UR = 3 * WS_V, WS_Z = WS_UR + (size_t)S * M * 2, WS_WO = WS_Z + (size_t)S * M * 2,
                 WS_G = WS_WO + (size_t)HD * M * 2, WS_F = WS_G + 8192, WS_SL = WS_F + 32768, WS_END = WS_SL + (size_t)64 * 16 * 20480;
constexpr int FL_F0 = 16, FL_F1 = FL_F0 + 256, FL_F3 = FL_F1 + 256, FL_F4 = FL_F3 + 512, FL_FW = FL_F4 + 160, FL_F5 = FL_FW + 256;

DI float bf2f(u16 b) { return __uint_as_float(((unsigned)b) << 16); }
DI unsigned cvtpk(float lo, float hi) { f32x2 v = {lo, hi}; bf16x2_t b = __builtin_convertvector(v, bf16x2_t); return __builtin_bit_cast(unsigned, b); }
DI u16 f2bf(float f) { return (u16)cvtpk(f, f); }
DI float rb(float f) { return __uint_as_float(cvtpk(f, f) << 16); }
DI unsigned rfl(unsigned x) { return __builtin_amdgcn_readfirstlane(x); }

#define SETM0(base, imm) asm volatile("s_add_u32 m0, %0, %1" :: "s"(base), "i"(imm) : "scc")
#define GLDS(voff, sbase, imm) asm volatile("global_load_lds_dwordx4 %0, %1 offset:%c2" :: "v"(voff), "s"(sbase), "i"(imm) : "memory")
#define GLDSC(voff, sbase, imm) asm volatile("global_load_lds_dwordx4 %0, %1 offset:%c2 sc1" :: "v"(voff), "s"(sbase), "i"(imm) : "memory")
DI void dma1(unsigned ldsaddr, unsigned voff, const void* sbase) {
    asm volatile("s_mov_b32 m0, %0\n\ts_nop 0\n\tglobal_load_lds_dwordx4 %1, %2" :: "s"(ldsaddr), "v"(voff), "s"(sbase) : "memory");
}
DI void dma2(unsigned ldsaddr, unsigned voff, const void* sbase) {
    asm volatile("s_mov_b32 m0, %0\n\ts_nop 0\n\tglobal_load_lds_dwordx4 %1, %2\n\tglobal_load_lds_dwordx4 %1, %2 offset:1024" :: "s"(ldsaddr), "v"(voff), "s"(sbase) : "memory");
}
DI void dma1c(unsigned ldsaddr, unsigned voff, const void* sbase) {
    asm volatile("s_mov_b32 m0, %0\n\ts_nop 0\n\tglobal_load_lds_dwordx4 %1, %2 sc1" :: "s"(ldsaddr), "v"(voff), "s"(sbase) : "memory");
}
DI void dma2c(unsigned ldsaddr, unsigned voff, const void* sbase) {
    asm volatile("s_mov_b32 m0, %0\n\ts_nop 0\n\tglobal_load_lds_dwordx4 %1, %2 sc1\n\tglobal_load_lds_dwordx4 %1, %2 offset:1024 sc1" :: "s"(ldsaddr), "v"(voff), "s"(sbase) : "memory");
}
template <int N> DI void wait_bar_v() { asm volatile("s_waitcnt vmcnt(%0)\n\ts_barrier" :: "i"(N) : "memory"); }
template <int N> DI void wait_bar() { asm volatile("s_waitcnt vmcnt(%0) lgkmcnt(0)\n\ts_barrier" :: "i"(N) : "memory"); }
DI bf16x8 lds_frag(lds_cptr p) { return *(const LAS bf16x8*)p; }
DI s16x4 vtr(lds_cptr p) { return __builtin_bit_cast(s16x4, __builtin_amdgcn_ds_read_tr16_b64_v4i16((LAS v4i16_t*)p)); }
DI bf16x8 tr_frag(lds_cptr p, int second) { const s16x4 lo = vtr(p), hi = vtr(p + second); return __builtin_shufflevector(lo, hi, 0, 1, 2, 3, 4, 5, 6, 7); }
DI u32x4 pack8(const f32x16& x, int s) {
    u32x4 w; w[0] = cvtpk(x[8 * s + 0], x[8 * s + 1]); w[1] = cvtpk(x[8 * s + 2], x[8 * s + 3]); w[2] = cvtpk(x[8 * s + 4], x[8 * s + 5]); w[3] = cvtpk(x[8 * s + 6], x[8 * s + 7]); return w;
}
typedef __amdgpu_buffer_rsrc_t rsrc_t;
DI void st16(rsrc_t r, unsigned off, u32x4 v) { __builtin_amdgcn_raw_buffer_store_b128(v, r, off, 0, 16); }
DI void st8(rsrc_t r, unsigned off, u32x2 v) { __builtin_amdgcn_raw_buffer_store_b64(v, r, off, 0, 16); }
DI void st4(rsrc_t r, unsigned off, unsigned v) { __builtin_amdgcn_raw_buffer_store_b32(v, r, off, 0, 16); }
DI void signal_flag(GAS u64* flag, u64 tag, int tid) {
    asm volatile("s_waitcnt vmcnt(0) lgkmcnt(0)\n\ts_barrier" ::: "memory");
    if (tid == 0) __hip_atomic_store(flag, tag, __ATOMIC_RELAXED, __HIP_MEMORY_SCOPE_AGENT);
}
DI void poll_flags(GAS u64* flags, int idx, u64 tag) {
    for (unsigned spins = 0; spins < (1u << 16); ++spins) {
        const u64 v = idx >= 0 ? __hip_atomic_load(flags + idx, __ATOMIC_RELAXED, __HIP_MEMORY_SCOPE_AGENT) : tag;
        if (__all(v == tag)) break;
        __builtin_amdgcn_s_sleep(2);
    }
}
DI unsigned ld4c(rsrc_t r, unsigned off) { return __builtin_amdgcn_raw_buffer_load_b32(r, off, 0, 16); }
DI void flag_store(GAS u64* flag, u64 tag) { __hip_atomic_store(flag, tag, __ATOMIC_RELAXED, __HIP_MEMORY_SCOPE_AGENT); }

struct Args { const u16 *x, *ckv, *wq, *wuk, *wuv, *wo, *bk, *bv, *shk, *ssmk, *ssmD, *gw; u16* out; char* ws; };

constexpr int P0_OFF = 133120;
DI float blo(unsigned w) { return __uint_as_float(w << 16); }
DI float bhi(unsigned w) { return __uint_as_float(w & 0xffff0000u); }
template <int CTRL> DI float dpp_add(float v) { return v + __builtin_bit_cast(float, __builtin_amdgcn_update_dpp(0, __builtin_bit_cast(int, v), CTRL, 0xf, 0xf, true)); }
DI float wave_sum(float v) {
    v = dpp_add<0x128>(v); v = dpp_add<0x124>(v); v = dpp_add<0x122>(v); v = dpp_add<0x121>(v);
    auto a = __builtin_amdgcn_permlane16_swap(__float_as_uint(v), __float_as_uint(v), false, false); v = __uint_as_float(a[0]) + __uint_as_float(a[1]);
    auto b = __builtin_amdgcn_permlane32_swap(__float_as_uint(v), __float_as_uint(v), false, false); return __uint_as_float(b[0]) + __uint_as_float(b[1]);
}
DI int div10(int x) { return (x * 6554) >> 16; }
DI void p0a_load(const Args& a, int jb0, unsigned lds0, int wid, int lane) {
#pragma unroll
    for (int i = 0; i < 9; ++i) {
        const int p = wid + 8 * i;
        if (p < 66) {
            const int k = p >= 44 ? 2 : (p >= 22 ? 1 : 0), q = p - 22 * k;
            int jb = jb0 + 96 * k; if (jb >= 256) jb = jb0;
            const int cb = jb & 7, tb = jb >> 3;
            if (q < 11) { const int ci = q * 64 + lane, cj = ci < 670 ? ci : 669, row = div10(cj); int t = tb * 64 - 3 + row; t = t < 0 ? 0 : t;
                dma1(lds0 + p * 1024, (unsigned)(t * 1280 + (cj - 10 * row) * 16), (const char*)a.bk + cb * 160); }
            else if (q < 21) { const int ci = (q - 11) * 64 + lane, row = div10(ci);
                dma1(lds0 + p * 1024, (unsigned)(row * 1280 + (ci - 10 * row) * 16), (const char*)a.bv + (size_t)tb * 64 * 1280 + cb * 160); }
            else dma1(lds0 + p * 1024, (unsigned)((lane < 40 ? lane : 39) * 16), (const char*)a.shk + cb * 640);
        }
    }
}
DI void p0a_compute(int jb0, char* lds, int tid) {
    const int nj = jb0 + 192 < 256 ? 3 : 2;
#pragma unroll
    for (int it = 0; it < 2; ++it) {
        const int item = tid + it * 512, k = item >= 640 ? 2 : (item >= 320 ? 1 : 0), e = item - k * 320, tp = div10(e), cc = e - 10 * tp;
        if (k < nj && item < 960) {
            const int t0 = ((jb0 + 96 * k) >> 3) * 64;
            const lds_cptr in = (lds_cptr)lds + k * 22528;
            LAS char* tile = (LAS char*)lds + P0_OFF + k * 10240;
            u32x4 kk[5], vv[2], sh[4];
#pragma unroll
            for (int i = 0; i < 5; ++i) kk[i] = *(const LAS u32x4*)(in + (2 * tp + i) * 160 + cc * 16);
#pragma unroll
            for (int i = 0; i < 2; ++i) vv[i] = *(const LAS u32x4*)(in + 11264 + (2 * tp + i) * 160 + cc * 16);
#pragma unroll
            for (int i = 0; i < 4; ++i) sh[i] = *(const LAS u32x4*)(in + 21504 + cc * 64 + i * 16);
            if (t0 + 2 * tp < 3) {
#pragma unroll
                for (int i = 0; i < 3; ++i) if (t0 + 2 * tp + i - 3 < 0) kk[i] = (u32x4){0u, 0u, 0u, 0u};
            }
            const int dw = 31 - tp, pos = (((dw >> 2) ^ (cc & 7)) << 4) + (dw & 3) * 4;
#pragma unroll
            for (int qp = 0; qp < 4; ++qp) {
                f32x2 kx[5], r2[2];
#pragma unroll
                for (int i = 0; i < 5; ++i) kx[i] = (f32x2){blo(kk[i][qp]), bhi(kk[i][qp])};
                const f32x2 t0_ = {blo(sh[qp][0]), blo(sh[qp][2])}, t1_ = {bhi(sh[qp][0]), bhi(sh[qp][2])}, t2_ = {blo(sh[qp][1]), blo(sh[qp][3])}, t3_ = {bhi(sh[qp][1]), bhi(sh[qp][3])};
#pragma unroll
                for (int r = 0; r < 2; ++r)
                    r2[r] = (kx[r] * t3_ + kx[r + 1] * t2_ + kx[r + 2] * t1_ + kx[r + 3] * t0_) * (f32x2){blo(vv[r][qp]), bhi(vv[r][qp])};
                *(LAS unsigned*)(tile + (8 * cc + 2 * qp) * 128 + pos) = cvtpk(r2[1][0], r2[0][0]);
                *(LAS unsigned*)(tile + (8 * cc + 2 * qp + 1) * 128 + pos) = cvtpk(r2[1][1], r2[0][1]);
            }
        }
    }
}
DI void p0a_store(rsrc_t wr, int jb0, char* lds, int tid) {
    const int nj = jb0 + 192 < 256 ? 3 : 2;
#pragma unroll
    for (int it = 0; it < 4; ++it) { const int e = tid + it * 512, k = e >= 1280 ? 2 : (e >= 640 ? 1 : 0), f = e - k * 640, cl = f >> 3, d = f & 7;
        if (k < nj && e < 1920) { const int jb = jb0 + 96 * k;
            st16(wr, (unsigned)(WS_UR + ((size_t)((jb & 7) * 80 + cl) * S + (2047 - (jb >> 3) * 64 - 63) + 8 * d) * 2),
                 *(const LAS u32x4*)((lds_cptr)lds + P0_OFF + k * 10240 + cl * 128 + ((d ^ ((cl >> 3) & 7)) << 4))); } }
}
DI void p0b_load(const Args& a, int b, unsigned lds0, int wid, int lane) {
    const int cb = b & 7, tb = b >> 3;
#pragma unroll
    for (int i = 0; i < 3; ++i) { const int p = wid + 8 * i;
        if (p < 20) { const int ci = p * 64 + lane, row = div10(ci);
            dma1(lds0 + P0_OFF + p * 1024, (unsigned)(row * 1280 + (ci - 10 * row) * 16), (const char*)a.wo + (size_t)tb * 128 * 1280 + cb * 160); }
    }
}
DI void p0b_finish(rsrc_t wr, int b, char* lds, int wid, int lane) {
    const int cb = b & 7, tb = b >> 3;
    const int j = lane & 15, g = lane >> 4, q = j >> 2, p = lane & 3;
    const lds_cptr brd = (lds_cptr)lds + P0_OFF + (16 * (g >> 1) + 4 * (g & 1) + q) * 160 + p * 8;
#pragma unroll
    for (int i = 0; i < 3; ++i) if (wid + 8 * i < 20) { const int f = wid + 8 * i, kq = f / 5, cbk = f % 5;
        const bf16x8 w = tr_frag(brd + kq * 32 * 160 + cbk * 32, 1280);
        st16(wr, (unsigned)(WS_WO + ((size_t)(cb * 32 + tb) * 20 + f) * 1024 + lane * 16), __builtin_bit_cast(u32x4, w)); }
}

#define PF_TILE 6
constexpr int TSLOT = 32 * 1040;
struct P1Ctx { const char* wsrc; const char* tsrc; unsigned wvoff, wlds, tvoff, tlds; };
DI P1Ctx p1_setup(const Args& a, int h, int r, unsigned lds0, int wid, int lane) {
    P1Ctx c;
    c.wsrc = (const char*)((wid >> 2) ? a.wuv : a.wuk) + (size_t)h * 256 + ((wid >> 1) & 1) * 128 + (size_t)(wid & 1) * 4 * 8 * 8192;
    c.wvoff = (unsigned)(lane >> 3) * 8192u + (unsigned)(((lane & 7) ^ (((lane >> 4) & 1) << 2)) * 16);
    c.wlds = lds0 + ((wid >> 2) * 2 + ((wid >> 1) & 1)) * 8192 + (wid & 1) * 4 * 1024;
    c.tsrc = (const char*)a.ckv + (size_t)(256 * r + 4 * wid) * 1024;
    c.tvoff = lane * 16; c.tlds = lds0 + 4 * wid * 1040;
    return c;
}
DI void p1_dma_w1(const P1Ctx& c, int ch, int slot, int i) { dma1(c.wlds + slot * TSLOT + i * 1024, c.wvoff, c.wsrc + (size_t)ch * 64 * 8192 + (size_t)i * 8 * 8192); }
template <int J> DI void p1_dma_t1(const P1Ctx& c, int t, int slot) {
    asm volatile("s_mov_b32 m0, %0\n\ts_nop 0\n\tglobal_load_lds_dwordx4 %1, %2 offset:%c3" :: "s"(c.tlds + slot * TSLOT + 16 * J), "v"(c.tvoff), "s"(c.tsrc + (size_t)t * 32 * 1024), "i"(J * 1024) : "memory");
}
DI void p1_prime_t(const P1Ctx& c) { p1_dma_t1<0>(c, 0, 3); p1_dma_t1<1>(c, 0, 3); p1_dma_t1<2>(c, 0, 3); p1_dma_t1<3>(c, 0, 3); }
DI void p1_prime_w(const P1Ctx& c) {
#pragma unroll
    for (int ch = 0; ch < 3; ++ch)
#pragma unroll
        for (int i = 0; i < 4; ++i) p1_dma_w1(c, ch, ch, i);
}
constexpr int STG_UR = 3 * TSLOT, STG_H = STG_UR + 16384;
template <int MAT>
DI void p1_body(const Args& a, const P1Ctx& c, rsrc_t wr, int b, int h, int r, char* lds, int wid, int lane, int tid, GAS u64* flags, u64 tag) {
    const int g4 = lane >> 4, q = (lane & 15) >> 2, p = lane & 3;
    const lds_cptr wrd = (lds_cptr)lds + (MAT * 2 + ((wid >> 1) & 1)) * 8192 + (8 * (g4 >> 1) + q) * 128 + (((wid & 1) ^ (q >> 1)) * 64) + (g4 & 1) * 32 + p * 8;
    const lds_cptr trd = (lds_cptr)lds + (lane & 31) * 1040 + (lane >> 5) * 16;
    const unsigned dst = (unsigned)((MAT ? WS_V : WS_K) + ((size_t)(h * 64 + 8 * r) * 8 + 2 * (wid & 3)) * 1024) + lane * 16;
    unsigned pfd = 0;
    bf16x8 Wf[32];
    f32x16 acc = f32x16{};
    const lds_cptr t0p = trd + 3 * TSLOT;
#define P1M(A_, W_) (MAT == 0 ? MFMA32(W_, A_, acc) : MFMA32(A_, W_, acc))
#pragma unroll
    for (int ch = 0; ch < 8; ++ch) {
        if (ch == 0) wait_bar<8>(); else wait_bar<4>();
        if (ch == 3 && b >= 160 && tid < 3) { const int jb = b - 160 + 96 * tid; if (jb < 256) flag_store(flags + FL_F0 + (jb & 7) * 32 + (jb >> 3), tag); }
        const lds_cptr wb = wrd + (ch % 3) * TSLOT;
        bf16x8 af[4];
#pragma unroll
        for (int s = 0; s < 4; ++s) { Wf[4 * ch + s] = tr_frag(wb + s * 2048, 512); af[s] = lds_frag(t0p + (4 * ch + s) * 32); }
        SBAR();
#pragma unroll
        for (int s = 0; s < 4; ++s) {
            if (ch >= 1) { if (ch + 2 <= 7) p1_dma_w1(c, ch + 2, (ch + 2) % 3, s);
                           else { if (s == 0) p1_dma_t1<0>(c, ch - 5, (ch + 2) % 3); if (s == 1) p1_dma_t1<1>(c, ch - 5, (ch + 2) % 3); if (s == 2) p1_dma_t1<2>(c, ch - 5, (ch + 2) % 3); if (s == 3) p1_dma_t1<3>(c, ch - 5, (ch + 2) % 3); } }
            acc = P1M(af[s], Wf[4 * ch + s]); PIN(acc);
            SBAR();
        }
    }
    f32x16 accp = acc;
    bf16x8 af[4];
    wait_bar<4>();
#pragma unroll
    for (int s = 0; s < 4; ++s) af[s] = lds_frag(trd + (8 % 3) * TSLOT + s * 32);
    SBAR();
#pragma unroll
    for (int i = 1; i < 8; ++i) {
        const int j = 7 + i;
        const lds_cptr tb = trd + (j % 3) * TSLOT, tn = trd + ((j + 1) % 3) * TSLOT;
#pragma unroll
        for (int s = 0; s < 32; ++s) {
            if (s == 28 && i < 7) {
                if (b < 160 && i == 4 && wid == 0) poll_flags(flags, lane < 32 ? FL_F0 + (b / 20) * 32 + lane : -1, tag);
                if (i + 2 <= 7) wait_bar<6>(); else wait_bar<2>();
                const unsigned lds0 = (unsigned)(uintptr_t)lds;
                if (i == 1) p0b_load(a, b, lds0, wid, lane);
                if (i == PF_TILE) {
                    const int id = (b * 8 + wid) * 60 + (lane < 59 ? lane : 59);
                    const char* p = id < 40960 ? (const char*)a.x + (size_t)id * 64 : (const char*)a.wq + (size_t)(id - 40960) * 64;
                    asm volatile("global_load_dword %0, %1, off" : "+v"(pfd) : "v"(p) : "memory"); }
                if (b < 160) {
                    if (i == 1) dma2(lds0 + STG_H + wid * 2048, lane * 16, (const char*)a.ssmk + (size_t)(4 * b) * 4096 + wid * 2048);
                    if (i == 4) dma2c(lds0 + STG_UR + wid * 2048, lane * 16, a.ws + WS_UR + (size_t)(4 * b) * 4096 + wid * 2048);
                }
                SBAR();
            }
            if (!(s & 1) && s + 4 < 32) { LGKM(2); SBAR(); }
            if (s == 0) acc = MAT == 0 ? MFMA32(Wf[0], af[0], (f32x16{})) : MFMA32(af[0], Wf[0], (f32x16{})); else acc = P1M(af[s & 3], Wf[s]);
            PIN(acc);
            if (s + 4 < 32) af[s & 3] = lds_frag(tb + (s + 4) * 32); else if (i < 7) af[s & 3] = lds_frag(tn + (s - 28) * 32);
            if (s == 2) st16(wr, dst + (i - 1) * 8192, pack8(accp, 0));
            if (s == 4) st16(wr, dst + (i - 1) * 8192 + 1024, pack8(accp, 1));
            if (i + 2 <= 7) {
                if (s == 6) p1_dma_t1<0>(c, i + 2, (j + 2) % 3);
                if (s == 11) p1_dma_t1<1>(c, i + 2, (j + 2) % 3);
                if (s == 16) p1_dma_t1<2>(c, i + 2, (j + 2) % 3);
                if (s == 21) p1_dma_t1<3>(c, i + 2, (j + 2) % 3);
            }
            SBAR();
        }
        accp = acc;
    }
    asm volatile("" : "+v"(pfd));
    st16(wr, dst + 7 * 8192, pack8(accp, 0)); st16(wr, dst + 7 * 8192 + 1024, pack8(accp, 1));
#undef P1M
}

constexpr float QSCALE = 0.08838834764831845f * 1.4426950408889634f;
constexpr float THR = 8.f;
constexpr int QCH = 49152;
constexpr int KRING = 0, VRING = 65536, SPARE = 131072, QA_OFF = VRING + 32768;
#define MX3(a, b, c) __builtin_fmaxf(__builtin_fmaxf((a), (b)), (c))

DI void p2_body(const Args& a, int b, int h, int r, int blkA, int blkB, char* lds, int wid, int lane, int tid, GAS u64* flags, u64 tag) {
    const unsigned lds0 = (unsigned)(uintptr_t)lds;
    const int r32 = lane & 31, hi = lane >> 5, rg = wid & 3, par = wid >> 2;
    const int myblk = par ? blkB : blkA;
    const char* xg = (const char*)a.x + (size_t)(128 * myblk + 32 * rg) * 1280;
    const unsigned xvoff = (unsigned)(lane >> 3) * 1280u + (unsigned)(((lane & 7) ^ ((lane >> 4) & 7)) * 16);
    const unsigned xvoff2 = (unsigned)(lane >> 3) * 1280u + 8 * 1280u + (unsigned)(((lane & 7) ^ ((4 + (lane >> 4)) & 7)) * 16);
    const unsigned xlds = lds0 + 16384 + wid * 4096;
    const char* wg = (const char*)a.wq + (size_t)h * 256 + (wid >> 2) * 128 + (size_t)(wid & 3) * 2 * 8 * 8192;
    const unsigned wvoff = (unsigned)(lane >> 3) * 8192u + (unsigned)(((lane & 7) ^ (((lane >> 4) & 1) << 2)) * 16);
    const unsigned wvoff2 = wvoff + 8 * 8192u;
    const unsigned wlds = lds0 + 16384 + 32768 + (wid >> 2) * 8192 + (wid & 3) * 2048;
    const int g4 = lane >> 4, q = (lane & 15) >> 2, p = lane & 3;
    const int hsw = (q >> 1) & 1, ysw = hi ^ ((r32 >> 1) & 7);
    const lds_cptr wrd0 = (lds_cptr)lds + 16384 + 32768 + (8 * (g4 >> 1) + q) * 128 + (g4 & 1) * 32 + p * 8 + hsw * 64;
    const lds_cptr wrd1 = (lds_cptr)lds + 16384 + 32768 + (8 * (g4 >> 1) + q) * 128 + (g4 & 1) * 32 + p * 8 + (hsw ^ 1) * 64;
    lds_cptr xrd[4];
#pragma unroll
    for (int s = 0; s < 4; ++s) xrd[s] = (lds_cptr)lds + 16384 + wid * 4096 + r32 * 128 + (((2 * s) ^ ysw) << 4);
    f32x16 acc[4] = {};
#pragma unroll
    for (int c = 0; c < 2; ++c) { const char* xp = xg + c * 128; const char* wp = wg + (size_t)c * 64 * 8192; const unsigned so_ = ((c + 2) % 3) * QCH;
        dma1(xlds + so_, xvoff, xp); dma1(xlds + so_ + 1024, xvoff2, xp); dma1(xlds + so_ + 2048, xvoff, xp + 16 * 1280); dma1(xlds + so_ + 3072, xvoff2, xp + 16 * 1280);
        dma1(wlds + so_, wvoff, wp); dma1(wlds + so_ + 1024, wvoff2, wp); }
    unsigned so = 2 * QCH;
    const char* xp = xg + 2 * 128; const char* wp = wg + (size_t)2 * 64 * 8192;
    const unsigned kl0 = lds0 + KRING + wid * 2048, tvoff = lane * 16;
    const char* Kw = a.ws + WS_K + (size_t)h * 64 * 8192 + wid * 2048;
#define WFR(B0, B1, i) tr_frag((((i) & 1) ? (B1) : (B0)) + (((i) >> 1) & 1) * 8192 + ((i) >> 2) * 2048, 512)
    bf16x8 wf[4], xf[2];
#define P2CHUNK(MODE, EXTRA, LAST) do { \
        const unsigned sn = so == 0 ? 2 * QCH : so - QCH, sx = so == 2 * QCH ? 0 : so + QCH;        \
        const unsigned xl_ = xlds + sn, wl_ = wlds + sn; const char* xp2 = xp + 16 * 1280; \
        const lds_cptr wb0 = wrd0 + so, wb1 = wrd1 + so, wn0 = wrd0 + sx, wn1 = wrd1 + sx; \
        _Pragma("unroll") for (int i = 0; i < 16; ++i) { \
            if (i == 12 && !(LAST)) { \
                if ((EXTRA) == 2 && wid == 0) poll_flags(flags, lane < 8 ? FL_F1 + h * 8 + lane : -1, tag); \
                if ((MODE) == 0) wait_bar<6>(); else wait_bar<4>(); \
                if ((EXTRA) == 1 && tid == 0) { flag_store(flags + FL_FW + (b & 7) * 32 + (b >> 3), tag); if (b < 160) flag_store(flags + FL_F4 + b, tag); else flag_store(flags + FL_F1 + h * 8 + r, tag); } \
                SBAR(); } \
            acc[i & 3] = MFMA32(wf[i & 3], xf[(i >> 2) & 1], acc[i & 3]); PIN(acc[i & 3]); \
            if (i + 4 < 16) wf[i & 3] = WFR(wb0, wb1, i + 4); else if (!(LAST)) wf[i & 3] = WFR(wn0, wn1, i - 12); \
            if ((i & 3) == 0 && i + 4 < 16) xf[((i >> 2) + 1) & 1] = lds_frag(xrd[(i >> 2) + 1] + so); \
            if (i == 12 && !(LAST)) xf[0] = lds_frag(xrd[0] + sx); \
            if ((MODE) == 0) { \
                if (i == 0) { SETM0(xl_, 0); } if (i == 1) { GLDS(xvoff, xp, 0); SETM0(xl_, 1024); } \
                if (i == 3) { GLDS(xvoff2, xp, 0); SETM0(xl_, 2048); } if (i == 5) { GLDS(xvoff, xp2, 0); SETM0(xl_, 3072); } \
                if (i == 7) { GLDS(xvoff2, xp2, 0); SETM0(wl_, 0); } if (i == 9) { GLDS(wvoff, wp, 0); SETM0(wl_, 1024); } \
                if (i == 11) { GLDS(wvoff2, wp, 0); } \
            } else { \
                const char* ku_ = Kw + (size_t)(2 * ((MODE) - 1)) * 16384; \
                if (i == 0) { SETM0(kl0, (2 * ((MODE) - 1)) * 16384); } if (i == 1) GLDSC(tvoff, ku_, 0); if (i == 4) GLDSC(tvoff, ku_, 1024); \
                if (i == 6) { SETM0(kl0, (2 * ((MODE) - 1) + 1) * 16384); } if (i == 8) GLDSC(tvoff, ku_ + 16384, 0); if (i == 11) GLDSC(tvoff, ku_ + 16384, 1024); \
            } \
            SBAR(); \
        } \
        so = sx; xp += 128; wp += (size_t)64 * 8192; } while (0)
    wait_bar<6>();
    xf[0] = lds_frag(xrd[0] + so);
#pragma unroll
    for (int i = 0; i < 4; ++i) wf[i] = WFR(wrd0 + so, wrd1 + so, i);
    SBAR();
    P2CHUNK(0, 0, false);
    P2CHUNK(0, 1, false);
#pragma unroll 1
    for (int c = 2; c < 7; ++c) P2CHUNK(0, 0, false);
    P2CHUNK(0, 2, false);
    P2CHUNK(1, 0, false);
    P2CHUNK(2, 0, true);
#undef P2CHUNK
    wait_bar<63>();
    lds_ptr qw = (lds_ptr)lds + (par ? SPARE : QA_OFF) + rg * 8192 + lane * 16;
#pragma unroll
    for (int db = 0; db < 4; ++db) {
#pragma unroll
        for (int e = 0; e < 16; ++e) acc[db][e] *= QSCALE;
        *(LAS u32x4*)(qw + (2 * db) * 1024) = pack8(acc[db], 0);
        *(LAS u32x4*)(qw + (2 * db + 1) * 1024) = pack8(acc[db], 1);
    }
#undef WFR
}

DI void p3_body(rsrc_t wr, const char* ws, int h, int blkA, int blkB, char* lds, int wid, int lane, int tid, GAS u64* flags, u64 tag) {
    const unsigned lds0 = (unsigned)(uintptr_t)lds;
    const int r32 = lane & 31, hi = lane >> 5, rg = wid & 3, par = wid >> 2;
    const unsigned tvoff = lane * 16;
    const unsigned kl0 = lds0 + KRING + wid * 2048, vl0 = lds0 + VRING + wid * 2048;
    const char* Kw = ws + WS_K + (size_t)h * 64 * 8192 + wid * 2048; const char* Vw = ws + WS_V + (size_t)h * 64 * 8192 + wid * 2048;
    const lds_cptr krd = (lds_cptr)lds + KRING + par * 8192 + lane * 16;
    const lds_cptr vrd = (lds_cptr)lds + VRING + par * 8192 + lane * 16;
    int g0 = 0;
    bf16x8 qf[8];
#define VF(i) ((((i) & 3) * 2 + ((i) >> 2)) * 1024)
#define MFMA_C(D, A, B, C) asm volatile("v_mfma_f32_32x32x16_bf16 %0, %2, %3, %1" : "=&v"(D), "+v"(C) : "v"(A), "v"(B))
#define SLOT(X, Y, PN, PP, ODD, QK, SM, PV, FIRST, DM, PFK, MASK) do { \
        unsigned long long fire = 0; \
        _Pragma("unroll") for (int i = 0; i < 16; ++i) { \
            if (i < 8 && QK && !(i & 1)) { LGKM(2); SBAR(); } \
            if (i >= 8 && PV && !(i & 1)) { LGKM(2); SBAR(); } \
            if (i < 8) { if (QK) { if (i == 0) { if (FIRST) X = MFMA32(kf[0], qf[0], zero16); else MFMA_C(X, kf[0], qf[0], negm); } else X = MFMA32(kf[i & 3], qf[i], X); PIN(X); \
                                   if (i < 4) kf[i] = lds_frag(kp + ((ODD) ? 0 : 16384) + (i + 4) * 1024); } \
                         if (PV && i >= 4) vf[i - 4] = lds_frag(vp + ((ODD) ? 0 : 16384) + VF(i - 4)); } \
            else { if (PV) { o[i & 3] = MFMA32(vf[i & 3], __builtin_bit_cast(bf16x8, PP[(i - 8) >> 2]), o[i & 3]); PIN(o[i & 3]); if (i < 12) vf[i & 3] = lds_frag(vp + ((ODD) ? 0 : 16384) + VF(i - 4)); } \
                   if (PFK && i >= 12) kf[i - 12] = lds_frag(kp + ((ODD) ? 16384 : 0) + (i - 12) * 1024); } \
            if (!(ODD) && i == 5) { kp = (lds_cptr)(uintptr_t)((unsigned)(uintptr_t)kp ^ 0x8000u); PIN(kp); } \
            if (!(ODD) && i == 13) { vp = (lds_cptr)(uintptr_t)((unsigned)(uintptr_t)vp ^ 0x8000u); PIN(vp); } \
            if (DM) { \
                if (i == 0) SETM0(dk, (ODD) ? 16384 : 0); \
                if (i == 1) GLDSC(uoff, Kw, 0); \
                if (i == 3) GLDSC(uoff, Kw, 1024); \
                if ((DM) & 2) { \
                    if (i == 4) SETM0(dv, (ODD) ? 16384 : 0); \
                    if (i == 5) GLDSC(uoffv, Vw, 0); \
                    if (i == 7) GLDSC(uoffv, Vw, 1024); \
                    if (i == 11) { uoffv += 16384u; PIN(uoffv); } } \
                if (i == 8) { uoff += (j + 5 == n) ? 16384u * (unsigned)(1 - n) : 16384u; PIN(uoff); } \
                if ((ODD) && i == 9) { dk ^= 0x8000u; dv ^= 0x8000u; PINS(dk); PINS(dv); } } \
            if (SM) { \
                if (i == 0) { Y[0] = EXP2(Y[0]); Y[1] = EXP2(Y[1]); } else if (i < 15) Y[i + 1] = EXP2(Y[i + 1]); \
                if (i == 1) l2 += Y[0]; \
                if (i >= 1) { if (i & 1) l += Y[i]; else l2 += Y[i]; PIN(l); PIN(l2); } \
                if (i & 1) { PN[i >> 3][(i >> 1) & 3] = cvtpk(Y[i - 1], Y[i]); PIN(PN[i >> 3]); } \
                if (i < 15) PIN(Y); } \
            if (QK) { \
                if (MASK && i == 9 && (MASK == 1 || j + 1 >= n - 2)) { int qq = qrel - 64 * (j + 1 - (n - 2)); PIN(qq); \
                    _Pragma("unroll") for (int r = 0; r < 16; ++r) { const int c = (r & 3) + 8 * (r >> 2); if (c > qq) X[r] = -INFINITY; } } \
                if (i == 9) { mxa = MX3(X[0], X[1], X[2]); PIN(mxa); } \
                if (i == 10) { mxb = MX3(X[3], X[4], X[5]); mxa = MX3(mxa, X[6], X[7]); PIN(mxb); PIN(mxa); } \
                if (i == 11) { mxb = MX3(mxb, X[8], X[9]); PIN(mxb); } \
                if (i == 12) { mxa = MX3(mxa, X[10], X[11]); mxb = MX3(mxb, X[12], X[13]); PIN(mxb); PIN(mxa); } \
                if (i == 13) { mxa = MX3(mxa, X[14], X[15]); PIN(mxa); } \
                if (i == 14) { mxa = MX3(mxa, mxb, X[0]); PIN(mxa); } \
                if (i == 15 && !(FIRST)) { fire = __ballot(mxa > THR); PINS(fire); } } \
            SBAR(); \
        } \
        if (__builtin_expect((fire | pend) != 0, 0) || FIRST) { \
            if (PV && pend) { _Pragma("unroll") for (int d = 0; d < 4; ++d) _Pragma("unroll") for (int r = 0; r < 16; ++r) o[d][r] *= alp; } \
            pend = 0; \
            if (QK && (FIRST || fire)) { \
                auto rr = __builtin_amdgcn_permlane32_swap(__float_as_uint(mxa), __float_as_uint(mxa), false, false); \
                const float rm = __builtin_fmaxf(__uint_as_float(rr[0]), __uint_as_float(rr[1])); \
                if (FIRST) { const float dl = __builtin_fmaxf(rm, -1e30f); m = dl; \
                    _Pragma("unroll") for (int r = 0; r < 16; ++r) { X[r] -= dl; negm[r] = -dl; } PIN(negm); } \
                else { const float dl = __builtin_fmaxf(rm, 0.f); m += dl; alp = EXP2(-dl); l *= alp; l2 *= alp; pend = 1; \
                    const float ndl = -dl, aone = hi ? 0.f : 1.f; \
                    asm volatile("s_nop 1\n\tv_mfma_f32_32x32x2_f32 %0, %2, %3, %0\n\tv_mfma_f32_32x32x2_f32 %1, %2, %3, %1\n\ts_nop 7\n\ts_nop 7\n\ts_nop 7" : "+v"(X), "+v"(negm) : "v"(aone), "v"(ndl)); } } } \
        SBAR(); \
        } while (0)
#define TBAR() wait_bar_v<4>()
    const f32x16 zero16 = {};
#pragma unroll 1
    for (int pass = 0; pass < 2; ++pass) {
        const int blk = pass ? blkB : blkA, n = 2 * (blk + 1);
        dma2c(vl0 + (g0 & 3) * 16384, tvoff, Vw); dma2c(vl0 + ((g0 + 1) & 3) * 16384, tvoff, Vw + 16384);
        if (pass == 0) wait_bar<4>();
        if (pass == 0) {
            const lds_cptr qr = (lds_cptr)lds + QA_OFF + rg * 8192 + lane * 16;
#pragma unroll
            for (int s = 0; s < 8; ++s) qf[s] = lds_frag(qr + s * 1024);
        }
        float m = -1e30f, l = 0.f, l2 = 0.f, alp = 1.f, mxa = 0.f, mxb = 0.f; unsigned long long pend = 0; f32x16 o[4] = {};
        u32x4 pk0[2] = {}, pk1[2] = {};
        f32x16 s0, s1, negm;
        bf16x8 kf[4], vf[4] = {};
        const int qrel = 32 * rg + r32 - 32 * par - 4 * hi;
        lds_cptr kp = krd + (g0 & 2) * 16384, vp = vrd + ((g0 & 2) ^ 2) * 16384;
        unsigned dk = kl0 + (g0 & 2) * 16384, dv = vl0 + ((g0 + 2) & 2) * 16384;
        unsigned uoffv = tvoff + 2 * 16384;
        unsigned uoff = tvoff + ((4 < n) ? 4 : 4 - n) * 16384;
#pragma unroll
        for (int i = 0; i < 4; ++i) kf[i] = lds_frag(kp + i * 1024);
        int j = -1;
        SLOT(s0, s1, pk1, pk0, true, true, false, false, true, false, true, 2);
        wait_bar_v<63>();
        j = 0;
        SLOT(s1, s0, pk0, pk1, false, true, true, false, false, 3, true, 2);
        TBAR();
        if (pass == 1 && tid == 0) flag_store(flags + FL_F3 + blkA * 32 + h, tag);
#pragma unroll 1
        for (j = 1; j < n - 3; j += 2) {
            SLOT(s0, s1, pk1, pk0, true, true, true, true, false, 3, true, 0);
            TBAR();
            ++j;
            SLOT(s1, s0, pk0, pk1, false, true, true, true, false, 3, true, 0);
            TBAR();
            --j;
        }
        if (n >= 4) {
            j = n - 3;
            SLOT(s0, s1, pk1, pk0, true, true, true, true, false, 3, true, 1);
            TBAR();
            j = n - 2;
            SLOT(s1, s0, pk0, pk1, false, true, true, true, false, 1, true, 1);
            wait_bar_v<2>();
        }
        j = n - 1;
        SLOT(s0, s1, pk1, pk0, true, false, true, true, false, 1, false, 0);
        wait_bar_v<2>();
        if (pass == 0) {
            const lds_cptr qr = (lds_cptr)lds + SPARE + rg * 8192 + lane * 16;
#pragma unroll
            for (int s = 0; s < 8; ++s) qf[s] = lds_frag(qr + s * 1024);
        }
        j = n;
        SLOT(s1, s0, pk0, pk1, false, false, false, true, false, false, false, 0);
        g0 += n;
        {
            l += l2;
            auto rr = __builtin_amdgcn_permlane32_swap(__float_as_uint(l), __float_as_uint(l), false, false);
            l = __uint_as_float(rr[0]) + __uint_as_float(rr[1]);
            wait_bar<63>();
            LAS float* ml = (LAS float*)((lds_ptr)lds + SPARE);
            *(LAS f32x2*)(ml + (wid * 64 + lane) * 2) = (f32x2){m, l};
            lds_ptr sendp = (lds_ptr)lds + VRING + par * 32768 + rg * 8192 + lane * 16;
            lds_cptr recvp = (lds_cptr)lds + VRING + (par ^ 1) * 32768 + rg * 8192 + lane * 16;
            f32x16 keep[2];
            if (par) {
#pragma unroll
                for (int d = 0; d < 2; ++d) { keep[d] = o[2 + d];
#pragma unroll
                    for (int g = 0; g < 4; ++g) *(LAS f32x4*)(sendp + (d * 4 + g) * 1024) = (f32x4){o[d][4 * g], o[d][4 * g + 1], o[d][4 * g + 2], o[d][4 * g + 3]}; }
            } else {
#pragma unroll
                for (int d = 0; d < 2; ++d) { keep[d] = o[d];
#pragma unroll
                    for (int g = 0; g < 4; ++g) *(LAS f32x4*)(sendp + (d * 4 + g) * 1024) = (f32x4){o[2 + d][4 * g], o[2 + d][4 * g + 1], o[2 + d][4 * g + 2], o[2 + d][4 * g + 3]}; }
            }
            wait_bar<63>();
            const f32x2 mlo = *(const LAS f32x2*)(ml + ((wid ^ 4) * 64 + lane) * 2);
            const float mm = __builtin_fmaxf(m, mlo[0]), fo = EXP2(m - mm), fx = EXP2(mlo[0] - mm);
            const float inv = __builtin_amdgcn_rcpf(l * fo + mlo[1] * fx), sc = fo * inv, sx = fx * inv;
            const unsigned cdst = (unsigned)(WS_CTX + ((size_t)(blk * 4 + rg) * 32 + h) * 8192 + (par * 4) * 1024) + lane * 16;
#pragma unroll
            for (int d = 0; d < 2; ++d) {
#pragma unroll
                for (int g = 0; g < 4; ++g) {
                    const f32x4 v4 = *(const LAS f32x4*)(recvp + (d * 4 + g) * 1024);
#pragma unroll
                    for (int e = 0; e < 4; ++e) keep[d][4 * g + e] = keep[d][4 * g + e] * sc + v4[e] * sx;
                }
                st16(wr, cdst + (2 * d) * 1024, pack8(keep[d], 0));
                st16(wr, cdst + (2 * d + 1) * 1024, pack8(keep[d], 1));
            }
            if (pass == 0) wait_bar<63>();
            else signal_flag(flags + FL_F3 + blk * 32 + h, tag, tid);
        }
    }
#undef SLOT
#undef TBAR
#undef VF
}

constexpr int CSTR = 4160;
constexpr int CCH = 4 * CSTR + 4608;
DI void p4_body(const Args& a, rsrc_t wr, int b, int h, int r, char* lds, int wid, int lane, int tid, GAS u64* flags, u64 tag) {
    const int ch = wid >> 1, c = 4 * b + ch, half = wid & 1;
    lds_ptr base = (lds_ptr)lds + ch * CCH;
    const lds_cptr ur = (lds_cptr)lds + STG_UR + ch * 4096, hs = (lds_cptr)lds + STG_H + ch * 4096;
    u32x4 w0[2], w1[2], hv[2];
#pragma unroll
    for (int it = 0; it < 2; ++it) { const int y8 = half * 128 + it * 64 + lane;
        w0[it] = *(const LAS u32x4*)(ur + 16 * y8); w1[it] = *(const LAS u32x4*)(ur + 16 * (y8 < 255 ? y8 + 1 : y8)); if (y8 == 255) w1[it] = (u32x4){0u, 0u, 0u, 0u};
        hv[it] = *(const LAS u32x4*)(hs + 16 * y8); }
    wait_bar<63>();
    const float Dc = bf2f(a.ssmD[c]);
#pragma unroll
    for (int it = 0; it < 2; ++it) {
        const int y8 = half * 128 + it * 64 + lane;
        unsigned w[6] = {w0[it][0], w0[it][1], w0[it][2], w0[it][3], w1[it][0], w1[it][1]}, o[5];
#pragma unroll
        for (int e = 0; e < 5; ++e) o[e] = __builtin_amdgcn_alignbit(w[e + 1], w[e], 16);
#pragma unroll
        for (int sg = 0; sg < 4; ++sg) { const int e = sg >> 1; u32x4 v;
            if (sg & 1) v = (u32x4){o[e], o[e + 1], o[e + 2], o[e + 3]}; else v = (u32x4){w[e], w[e + 1], w[e + 2], w[e + 3]};
            *(LAS u32x4*)(base + sg * CSTR + y8 * 16) = v; }
        *(LAS u32x4*)(base + 4 * CSTR + 512 + y8 * 16) = hv[it];
    }
    if (half == 1 && lane < 8) { const int sg = lane >> 1, y8 = 256 + (lane & 1); *(LAS u32x4*)(base + sg * CSTR + y8 * 16) = (u32x4){0u, 0u, 0u, 0u}; }
    if (half == 0 && lane < 32) *(LAS u32x4*)(base + 4 * CSTR + lane * 16) = (u32x4){0u, 0u, 0u, 0u};
    wait_bar<63>();
    const int j = lane & 15, g = lane >> 4;
    lds_cptr ard = (lds_cptr)base + ((3 - j) & 3) * CSTR + (2031 - j - ((3 - j) & 3) + 8 * g) * 2 - half * 64 - 62 * 64, ard2 = ard + 8;
    const lds_cptr brd = (lds_cptr)base + 4 * CSTR + (240 + 16 * j + 8 * g) * 2 - half * 64 - 6 * 64;
    PIN(ard2);
    f32x4 acc[8] = {};
#define P4LOAD(A, B, RC) do { _Pragma("unroll") for (int u = 0; u < 8; ++u) { const s16x4 lo = *(const LAS s16x4*)(ard + (62 - (RC) - 8 * u) * 64), hi2 = *(const LAS s16x4*)(ard2 + (62 - (RC) - 8 * u) * 64); \
            A[u] = __builtin_shufflevector(lo, hi2, 0, 1, 2, 3, 4, 5, 6, 7); B[u] = lds_frag(brd + (8 * u - (RC) + 6) * 64); } } while (0)
#define P4MM(A, B) do { _Pragma("unroll") for (int u = 0; u < 8; ++u) _Pragma("unroll") for (int v = 0; v + u < 8; ++v) { acc[u + v] = MFMA16(A[u], B[v], acc[u + v]); PIN(acc[u + v]); SBAR(); } } while (0)
    {
        bf16x8 A0[8], B0[8], A1[8], B1[8];
        P4LOAD(A0, B0, 0); SBAR(); P4LOAD(A1, B1, 2); SBAR();
        P4MM(A0, B0);
        P4LOAD(A0, B0, 4); SBAR();
        P4MM(A1, B1);
        P4LOAD(A1, B1, 6); SBAR();
        P4MM(A0, B0);
        P4MM(A1, B1);
    }
#undef P4LOAD
#undef P4MM
    wait_bar<0>();
    if (tid == 0) flag_store(flags + FL_F1 + h * 8 + r, tag);
    lds_ptr ex = base + CSTR;
    f32x4 keep[4];
    if (half) {
#pragma unroll
        for (int q = 0; q < 4; ++q) { *(LAS f32x4*)(ex + 4096 + q * 1024 + lane * 16) = acc[q]; keep[q] = acc[4 + q]; }
    } else {
#pragma unroll
        for (int q = 0; q < 4; ++q) { *(LAS f32x4*)(ex + q * 1024 + lane * 16) = acc[4 + q]; keep[q] = acc[q]; }
    }
    wait_bar<63>();
    u32x2 zp[4];
#pragma unroll
    for (int q = 0; q < 4; ++q) {
        const f32x4 o = *(const LAS f32x4*)(ex + (half ^ 1) * 4096 + q * 1024 + lane * 16);
        const f32x4 y = keep[q] + o;
        const int t = 256 * (half * 4 + q) + 16 * j + 4 * g;
        const s16x4 kvr = *(const LAS s16x4*)(base + (2047 - t - 3) * 2);
        float z[4];
#pragma unroll
        for (int r = 0; r < 4; ++r) { const float kv = bf2f((u16)kvr[3 - r]); z[r] = y[r] + kv * Dc; }
        zp[q] = (u32x2){cvtpk(z[0], z[1]), cvtpk(z[2], z[3])};
    }
#pragma unroll
    for (int q = 0; q < 4; q += 2) {
        auto s0 = __builtin_amdgcn_permlane16_swap(zp[q][0], zp[q + 1][0], false, false);
        auto s1 = __builtin_amdgcn_permlane16_swap(zp[q][1], zp[q + 1][1], false, false);
        const int t = 256 * (half * 4 + q + (g & 1)) + 16 * j + 8 * (g >> 1);
        st16(wr, (unsigned)(WS_Z + ((size_t)c * S + t) * 2), (u32x4){s0[0], s1[0], s0[1], s1[1]});
    }
}

constexpr int OCH = 40960;
DI u32x4 ld16c(rsrc_t r, unsigned off) { return __builtin_amdgcn_raw_buffer_load_b128(r, off, 0, 16); }
DI void p5_body(const Args& a, rsrc_t wr, int mt, int nt, int ks, char* lds, int wid, int lane, int tid, GAS u64* flags, u64 tag) {
    const unsigned lds0 = (unsigned)(uintptr_t)lds;
    const int rg = wid & 3, kh = wid >> 2;
    const int j = lane & 15, g = lane >> 4;
    const unsigned tv = lane * 16;
    const char* wg = a.ws + WS_WO + ((size_t)(2 * nt + ((wid >> 1) & 1)) * 32 + 8 * ks + 4 * (wid >> 2)) * 20480 + (wid & 1) * 5120;
    const unsigned wl = lds0 + (wid >> 1) * 10240 + (wid & 1) * 5120;
#define WLOAD(slot, st) do { const char* p_ = wg + (size_t)((st) >> 1) * 20480 + ((st) & 1) * 10240; \
        asm volatile("s_mov_b32 m0, %0\n\ts_nop 0\n\tglobal_load_lds_dwordx4 %1, %2 sc1\n\tglobal_load_lds_dwordx4 %1, %2 offset:1024 sc1\n\tglobal_load_lds_dwordx4 %1, %2 offset:2048 sc1\n\t" \
                     "global_load_lds_dwordx4 %1, %2 offset:3072 sc1" :: "s"(wl + (slot) * OCH), "v"(tv), "s"(p_) : "memory"); \
        dma1c(wl + (slot) * OCH + 4096, tv, p_ + 4096); } while (0)
    WLOAD(0, 0); WLOAD(1, 1);
    if (wid == 0) {
        poll_flags(flags, lane < 40 ? FL_F4 + nt * 40 + lane : (lane < 44 ? FL_F0 + (4 * (ks & 1) + lane - 40) * 32 + 2 * mt + (ks >> 1) : -1), tag);
        poll_flags(flags, lane < 8 ? FL_F3 + mt * 32 + 8 * ks + lane : -1, tag);
    }
    wait_bar<63>();
    const char* cg = a.ws + WS_CTX + ((size_t)(4 * mt + rg) * 32 + 8 * ks + 4 * kh) * 8192;
    const unsigned cv = (g >> 1) * 1024 + ((g & 1) * 32 + j) * 16;
    u32x4 cf[3][4];
#define CLOAD(slot, st) do { const char* p_ = cg + (size_t)((st) >> 1) * 8192 + ((st) & 1) * 4096; \
        asm volatile("global_load_dwordx4 %0, %4, %5 sc1\n\tglobal_load_dwordx4 %1, %4, %5 offset:256 sc1\n\tglobal_load_dwordx4 %2, %4, %5 offset:2048 sc1\n\tglobal_load_dwordx4 %3, %4, %5 offset:2304 sc1" \
                     : "=&v"(cf[slot][0]), "=&v"(cf[slot][1]), "=&v"(cf[slot][2]), "=&v"(cf[slot][3]) : "v"(cv), "s"(p_) : "memory"); } while (0)
    CLOAD(0, 0); CLOAD(1, 1);
    unsigned zr[5];
#pragma unroll
    for (int it = 0; it < 5; ++it) { const int e = tid + it * 512, cl2 = e >> 4, d = e & 15;
        zr[it] = ld4c(wr, (unsigned)(WS_Z + ((size_t)(nt * 160 + cl2) * S + mt * 128 + ks * 32 + 2 * d) * 2)); }
    u32x2 xv[3]; float gt[3];
#pragma unroll
    for (int k = 0; k < 3; ++k) { const int i = wid + 8 * k < 20 ? wid + 8 * k : wid, t = mt * 128 + ks * 32 + 16 * ((i % 10) / 5) + j, c = nt * 160 + 80 * (i / 10) + 16 * (i % 5) + 4 * g;
        xv[k] = *(const u32x2*)(a.x + (size_t)t * M + c); gt[k] = __uint_as_float(ld4c(wr, (unsigned)(WS_G + t * 4))); }
    const lds_cptr brd = (lds_cptr)lds + kh * 20480 + lane * 16;
    f32x4 acc[2][10] = {};
    bf16x8 wf[4];
#define WFI(bb, i) lds_frag((bb) + (((i) / 5) & 1) * 10240 + (((i) / 10) * 5 + (i) % 5) * 1024)
    wait_bar<4>();
#pragma unroll
    for (int k = 0; k < 3; ++k) { PIN(xv[k]); PIN(gt[k]); }
#pragma unroll
    for (int it = 0; it < 5; ++it) PIN(zr[it]);
#pragma unroll
    for (int i = 0; i < 4; ++i) wf[i] = WFI(brd, i);
    SBAR();
#pragma unroll
    for (int st = 0; st < 8; ++st) {
        asm volatile("" : "+v"(cf[st % 3][0]), "+v"(cf[st % 3][1]), "+v"(cf[st % 3][2]), "+v"(cf[st % 3][3]));
        const lds_cptr bb = brd + (st % 3) * OCH, bn = brd + ((st + 1) % 3) * OCH;
#pragma unroll
        for (int i = 0; i < 20; ++i) {
            const int ks2 = i / 10, c10 = i % 10;
            if (i == 16 && st < 7) { if (st + 2 < 8) wait_bar<9>(); else wait_bar<0>(); SBAR(); }
            acc[0][c10] = MFMA16(wf[i & 3], __builtin_bit_cast(bf16x8, cf[st % 3][2 * ks2]), acc[0][c10]); PIN(acc[0][c10]); SBAR();
            acc[1][c10] = MFMA16(wf[i & 3], __builtin_bit_cast(bf16x8, cf[st % 3][2 * ks2 + 1]), acc[1][c10]); PIN(acc[1][c10]);
            if (i + 4 < 20) wf[i & 3] = WFI(bb, i + 4); else if (st < 7) wf[i & 3] = WFI(bn, i - 16);
            if (st + 2 < 8) { if (i == 1) WLOAD((st + 2) % 3, st + 2); if (i == 10) CLOAD((st + 2) % 3, st + 2); }
            SBAR();
        }
    }
#undef WFI
#undef WLOAD
#undef CLOAD
    wait_bar<0>();
    {
        lds_ptr sp = (lds_ptr)lds + wid * 10240 + lane * 16; lds_cptr rp = (lds_cptr)lds + (wid ^ 4) * 10240 + lane * 16;
        f32x4 keep[10];
        if (kh) {
#pragma unroll
            for (int i = 0; i < 10; ++i) { *(LAS f32x4*)(sp + i * 1024) = acc[i / 5][i % 5]; keep[i] = acc[i / 5][5 + i % 5]; }
        } else {
#pragma unroll
            for (int i = 0; i < 10; ++i) { *(LAS f32x4*)(sp + i * 1024) = acc[i / 5][5 + i % 5]; keep[i] = acc[i / 5][i % 5]; }
        }
        lds_ptr zb = (lds_ptr)lds + 81920;
#pragma unroll
        for (int it = 0; it < 5; ++it) { const int e = tid + it * 512, cl2 = e >> 4, d = e & 15; *(LAS unsigned*)(zb + cl2 * 68 + d * 4) = zr[it]; }
        wait_bar<63>();
#pragma unroll
        for (int i = 0; i < 10; ++i) keep[i] += *(const LAS f32x4*)(rp + i * 1024);
        const int tile = mt * 4 + nt;
        if (rg != ks) {
            const unsigned dst = (unsigned)(WS_SL + (((size_t)tile * 4 + rg) * 4 + ks) * 20480 + kh * 10240) + lane * 16;
#pragma unroll
            for (int i = 0; i < 10; ++i) st16(wr, dst + i * 1024, __builtin_bit_cast(u32x4, keep[i]));
        } else {
            lds_ptr op = (lds_ptr)lds + 98304 + kh * 10240 + lane * 16;
#pragma unroll
            for (int i = 0; i < 10; ++i) *(LAS f32x4*)(op + i * 1024) = keep[i];
        }
        signal_flag(flags + FL_F5 + tile * 4 + ks, tag, tid);
        if (wid == 0) poll_flags(flags, (lane < 4 && lane != ks) ? FL_F5 + tile * 4 + lane : -1, tag);
        wait_bar<63>();
        u32x4 pf[3][3];
#pragma unroll
        for (int k = 0; k < 3; ++k) { const int i = wid + 8 * k < 20 ? wid + 8 * k : wid;
            const unsigned src = (unsigned)(WS_SL + ((size_t)tile * 4 + ks) * 4 * 20480) + i * 1024 + lane * 16;
#pragma unroll
            for (int f = 0; f < 3; ++f) pf[k][f] = ld16c(wr, src + (unsigned)(f + (f >= ks ? 1 : 0)) * 20480u); }
#pragma unroll
        for (int k = 0; k < 3; ++k) if (wid + 8 * k < 20) {
            const int i = wid + 8 * k, tl = 16 * ((i % 10) / 5) + j, cl = 80 * (i / 10) + 16 * (i % 5) + 4 * g;
            f32x4 av = *(const LAS f32x4*)((lds_cptr)lds + 98304 + i * 1024 + lane * 16);
#pragma unroll
            for (int f = 0; f < 3; ++f) av += __builtin_bit_cast(f32x4, pf[k][f]);
            float r4[4];
#pragma unroll
            for (int e = 0; e < 4; ++e) { const float z = bf2f(*(const LAS u16*)((lds_cptr)lds + 81920 + (cl + e) * 68 + tl * 2));
                const float xe = (e & 1) ? bhi(xv[k][e >> 1]) : blo(xv[k][e >> 1]);
                r4[e] = gt[k] * av[e] + (1.f - gt[k]) * (xe * z); }
            *(u32x2*)(a.out + (size_t)(mt * 128 + ks * 32 + tl) * M + nt * 160 + cl) = (u32x2){cvtpk(r4[0], r4[1]), cvtpk(r4[2], r4[3])};
        }
    }
}

__global__ __launch_bounds__(512, 2) void k_fused(const u16* x, const u16* ckv, const u16* wq, const u16* wuk, const u16* wuv, const u16* wo, const u16* bk, const u16* bv,
                                                  const u16* shk, const u16* ssmk, const u16* ssmD, const u16* gw, u16* out, char* ws) {
    extern __shared__ __attribute__((aligned(16))) char lds[];
    const Args a = {x, ckv, wq, wuk, wuv, wo, bk, bv, shk, ssmk, ssmD, gw, out, ws};
    const int tid = threadIdx.x, lane = tid & 63, wid = rfl(tid >> 6);
    const int b = blockIdx.x, xcd = b & 7, idx = b >> 3, h = xcd * 4 + (idx >> 3), r = idx & 7;
    const unsigned lds0 = (unsigned)(uintptr_t)lds;
    const rsrc_t wr = __builtin_amdgcn_make_buffer_rsrc(ws, 0, (int)WS_END, 0x00020000);
    GAS u64* flags = (GAS u64*)(ws + WS_F);
    const P1Ctx c1 = p1_setup(a, h, r, lds0, wid, lane);
    const u64 tag = *(const __attribute__((address_space(4))) u64*)(ws + WS_F) + 1;
    if (b >= 160) {
        u32x4 xg[3][2];
        const u32x4 g0 = *(const u32x4*)(gw + lane * 8), g1 = *(const u32x4*)(gw + 512 + (lane & 15) * 8);
#pragma unroll
        for (int k = 0; k < 3; ++k) { int jb = b - 160 + 96 * k; if (jb >= 256) jb = b - 160; const u16* xr = x + (size_t)(64 * (jb >> 3) + 8 * (jb & 7) + wid) * M;
            xg[k][0] = *(const u32x4*)(xr + lane * 8); xg[k][1] = *(const u32x4*)(xr + 512 + (lane & 15) * 8); }
        p0a_load(a, b - 160, lds0, wid, lane);
        p1_prime_t(c1);
        wait_bar<4>();
#pragma unroll
        for (int k = 0; k < 3; ++k) { PIN(xg[k][0]); PIN(xg[k][1]); }
        p0a_compute(b - 160, lds, tid);
        wait_bar<63>();
        p1_prime_w(c1);
        p0a_store(wr, b - 160, lds, tid);
#pragma unroll
        for (int k = 0; k < 3; ++k) { const int jb = b - 160 + 96 * k; float sum = 0.f, s1 = 0.f;
#pragma unroll
            for (int e = 0; e < 4; ++e) { sum += blo(xg[k][0][e]) * blo(g0[e]) + bhi(xg[k][0][e]) * bhi(g0[e]); s1 += blo(xg[k][1][e]) * blo(g1[e]) + bhi(xg[k][1][e]) * bhi(g1[e]); }
            if (lane < 16) sum += s1;
            sum = wave_sum(sum);
            if (lane == 0 && jb < 256) st4(wr, (unsigned)(WS_G + (64 * (jb >> 3) + 8 * (jb & 7) + wid) * 4), __float_as_uint(1.f / (1.f + __expf(-sum)))); }
    } else { p1_prime_t(c1); p1_prime_w(c1); }
    if (wid < 4) p1_body<0>(a, c1, wr, b, h, r, lds, wid, lane, tid, flags, tag); else p1_body<1>(a, c1, wr, b, h, r, lds, wid, lane, tid, flags, tag);
    wait_bar<63>();
    p0b_finish(wr, b, lds, wid, lane);
    if (b < 160) p4_body(a, wr, b, h, r, lds, wid, lane, tid, flags, tag);
    wait_bar<63>();
    p2_body(a, b, h, r, 15 - r, r, lds, wid, lane, tid, flags, tag);
    p3_body(wr, ws, h, 15 - r, r, lds, wid, lane, tid, flags, tag);
    {
        const int ks = xcd >> 1, mt = (xcd & 1) * 8 + (idx >> 2), nt = idx & 3;
        if (wid == 0) poll_flags(flags, lane < 16 ? FL_FW + (2 * nt + (lane >> 3)) * 32 + 8 * ks + (lane & 7) : -1, tag);
        wait_bar<0>();
        p5_body(a, wr, mt, nt, ks, lds, wid, lane, tid, flags, tag);
    }
    if (b == 0 && tid == 0) flag_store(flags, tag);
}

extern "C" void kernel_launch(void* const* d_in, const int* in_sizes, int n_in,
                              void* d_out, int out_size, void* d_ws, size_t ws_size,
                              hipStream_t stream) {
    static bool attr = false;
    if (!attr) { (void)hipFuncSetAttribute((const void*)k_fused, hipFuncAttributeMaxDynamicSharedMemorySize, LDS_BYTES); attr = true; }
    k_fused<<<256, 512, LDS_BYTES, stream>>>((const u16*)d_in[0], (const u16*)d_in[1], (const u16*)d_in[2], (const u16*)d_in[3], (const u16*)d_in[4], (const u16*)d_in[5],
                                             (const u16*)d_in[7], (const u16*)d_in[8], (const u16*)d_in[9], (const u16*)d_in[10], (const u16*)d_in[11], (const u16*)d_in[12],
                                             (u16*)d_out, (char*)d_ws);
}
```

```cpp
#include <hip/hip_runtime.h>
#include <hip/hip_bf16.h>
#include <stdint.h>

typedef unsigned short u16;
typedef unsigned long long u64;
typedef short bf16x8 __attribute__((ext_vector_type(8)));
typedef short s16x4 __attribute__((ext_vector_type(4)));
typedef float f32x16 __attribute__((ext_vector_type(16)));
typedef float f32x4 __attribute__((ext_vector_type(4)));
typedef float f32x2 __attribute__((ext_vector_type(2)));
typedef __bf16 bf16x2_t __attribute__((ext_vector_type(2)));
typedef unsigned u32x4 __attribute__((ext_vector_type(4)));
typedef unsigned u32x2 __attribute__((ext_vector_type(2)));
typedef short v4i16_t __attribute__((ext_vector_type(4)));
#define LAS __attribute__((address_space(3)))
#define GAS __attribute__((address_space(1)))
typedef LAS const char* lds_cptr;
typedef LAS char* lds_ptr;
#define DI __device__ __forceinline__
#define SBAR() __builtin_amdgcn_sched_barrier(0)
#define PIN(x) asm volatile("" : "+v"(x))
#define PINS(x) asm volatile("" : "+s"(x))
#define MFMA32(a, b, c) __builtin_amdgcn_mfma_f32_32x32x16_bf16(a, b, c, 0, 0, 0)
#define MFMA16(a, b, c) __builtin_amdgcn_mfma_f32_16x16x32_bf16(a, b, c, 0, 0, 0)
#define LGKM(n) __builtin_amdgcn_s_waitcnt(0xC07F | ((n) << 8))
#define EXP2(v) __builtin_amdgcn_exp2f(v)

constexpr int S = 2048, H = 32, D = 128, M = 640, DC = 512, HD = H * D;
constexpr int LDS_BYTES = 163840;
constexpr size_t WS_K = 0, WS_V = (size_t)S * HD * 2, WS_CTX = 2 * WS_V, WS_UR = 3 * WS_V, WS_Z = WS_UR + (size_t)S * M * 2, WS_WO = WS_Z + (size_t)S * M * 2,
                 WS_G = WS_WO + (size_t)HD * M * 2, WS_F = WS_G + 8192, WS_SL = WS_F + 32768, WS_END = WS_SL + (size_t)64 * 16 * 20480;
constexpr int FL_F0 = 16, FL_F1 = FL_F0 + 256, FL_F3 = FL_F1 + 256, FL_F4 = FL_F3 + 512, FL_FW = FL_F4 + 160, FL_F5 = FL_FW + 256;

DI float bf2f(u16 b) { return __uint_as_float(((unsigned)b) << 16); }
DI unsigned cvtpk(float lo, float hi) { f32x2 v = {lo, hi}; bf16x2_t b = __builtin_convertvector(v, bf16x2_t); return __builtin_bit_cast(unsigned, b); }
DI u16 f2bf(float f) { return (u16)cvtpk(f, f); }
DI float rb(float f) { return __uint_as_float(cvtpk(f, f) << 16); }
DI unsigned rfl(unsigned x) { return __builtin_amdgcn_readfirstlane(x); }

#define SETM0(base, imm) asm volatile("s_add_u32 m0, %0, %1" :: "s"(base), "i"(imm) : "scc")
#define GLDS(voff, sbase, imm) asm volatile("global_load_lds_dwordx4 %0, %1 offset:%c2" :: "v"(voff), "s"(sbase), "i"(imm) : "memory")
#define GLDSC(voff, sbase, imm) asm volatile("global_load_lds_dwordx4 %0, %1 offset:%c2 sc1" :: "v"(voff), "s"(sbase), "i"(imm) : "memory")
DI void dma1(unsigned ldsaddr, unsigned voff, const void* sbase) {
    asm volatile("s_mov_b32 m0, %0\n\ts_nop 0\n\tglobal_load_lds_dwordx4 %1, %2" :: "s"(ldsaddr), "v"(voff), "s"(sbase) : "memory");
}
DI void dma2(unsigned ldsaddr, unsigned voff, const void* sbase) {
    asm volatile("s_mov_b32 m0, %0\n\ts_nop 0\n\tglobal_load_lds_dwordx4 %1, %2\n\tglobal_load_lds_dwordx4 %1, %2 offset:1024" :: "s"(ldsaddr), "v"(voff), "s"(sbase) : "memory");
}
DI void dma1c(unsigned ldsaddr, unsigned voff, const void* sbase) {
    asm volatile("s_mov_b32 m0, %0\n\ts_nop 0\n\tglobal_load_lds_dwordx4 %1, %2 sc1" :: "s"(ldsaddr), "v"(voff), "s"(sbase) : "memory");
}
DI void dma2c(unsigned ldsaddr, unsigned voff, const void* sbase) {
    asm volatile("s_mov_b32 m0, %0\n\ts_nop 0\n\tglobal_load_lds_dwordx4 %1, %2 sc1\n\tglobal_load_lds_dwordx4 %1, %2 offset:1024 sc1" :: "s"(ldsaddr), "v"(voff), "s"(sbase) : "memory");
}
template <int N> DI void wait_bar_v() { asm volatile("s_waitcnt vmcnt(%0)\n\ts_barrier" :: "i"(N) : "memory"); }
template <int N> DI void wait_bar() { asm volatile("s_waitcnt vmcnt(%0) lgkmcnt(0)\n\ts_barrier" :: "i"(N) : "memory"); }
DI bf16x8 lds_frag(lds_cptr p) { return *(const LAS bf16x8*)p; }
DI s16x4 vtr(lds_cptr p) { return __builtin_bit_cast(s16x4, __builtin_amdgcn_ds_read_tr16_b64_v4i16((LAS v4i16_t*)p)); }
DI bf16x8 tr_frag(lds_cptr p, int second) { const s16x4 lo = vtr(p), hi = vtr(p + second); return __builtin_shufflevector(lo, hi, 0, 1, 2, 3, 4, 5, 6, 7); }
DI u32x4 pack8(const f32x16& x, int s) {
    u32x4 w; w[0] = cvtpk(x[8 * s + 0], x[8 * s + 1]); w[1] = cvtpk(x[8 * s + 2], x[8 * s + 3]); w[2] = cvtpk(x[8 * s + 4], x[8 * s + 5]); w[3] = cvtpk(x[8 * s + 6], x[8 * s + 7]); return w;
}
typedef __amdgpu_buffer_rsrc_t rsrc_t;
DI void st16(rsrc_t r, unsigned off, u32x4 v) { __builtin_amdgcn_raw_buffer_store_b128(v, r, off, 0, 16); }
DI void st8(rsrc_t r, unsigned off, u32x2 v) { __builtin_amdgcn_raw_buffer_store_b64(v, r, off, 0, 16); }
DI void st4(rsrc_t r, unsigned off, unsigned v) { __builtin_amdgcn_raw_buffer_store_b32(v, r, off, 0, 16); }
DI void signal_flag(GAS u64* flag, u64 tag, int tid) {
    asm volatile("s_waitcnt vmcnt(0) lgkmcnt(0)\n\ts_barrier" ::: "memory");
    if (tid == 0) __hip_atomic_store(flag, tag, __ATOMIC_RELAXED, __HIP_MEMORY_SCOPE_AGENT);
}
DI void poll_flags(GAS u64* flags, int idx, u64 tag) {
    for (unsigned spins = 0; spins < (1u << 16); ++spins) {
        const u64 v = idx >= 0 ? __hip_atomic_load(flags + idx, __ATOMIC_RELAXED, __HIP_MEMORY_SCOPE_AGENT) : tag;
        if (__all(v == tag)) break;
        __builtin_amdgcn_s_sleep(2);
    }
}
DI unsigned ld4c(rsrc_t r, unsigned off) { return __builtin_amdgcn_raw_buffer_load_b32(r, off, 0, 16); }
DI void flag_store(GAS u64* flag, u64 tag) { __hip_atomic_store(flag, tag, __ATOMIC_RELAXED, __HIP_MEMORY_SCOPE_AGENT); }

struct Args { const u16 *x, *ckv, *wq, *wuk, *wuv, *wo, *bk, *bv, *shk, *ssmk, *ssmD, *gw; u16* out; char* ws; };

constexpr int P0_OFF = 133120;
DI float blo(unsigned w) { return __uint_as_float(w << 16); }
DI float bhi(unsigned w) { return __uint_as_float(w & 0xffff0000u); }
template <int CTRL> DI float dpp_add(float v) { return v + __builtin_bit_cast(float, __builtin_amdgcn_update_dpp(0, __builtin_bit_cast(int, v), CTRL, 0xf, 0xf, true)); }
DI float wave_sum(float v) {
    v = dpp_add<0x128>(v); v = dpp_add<0x124>(v); v = dpp_add<0x122>(v); v = dpp_add<0x121>(v);
    auto a = __builtin_amdgcn_permlane16_swap(__float_as_uint(v), __float_as_uint(v), false, false); v = __uint_as_float(a[0]) + __uint_as_float(a[1]);
    auto b = __builtin_amdgcn_permlane32_swap(__float_as_uint(v), __float_as_uint(v), false, false); return __uint_as_float(b[0]) + __uint_as_float(b[1]);
}
DI int div10(int x) { return (x * 6554) >> 16; }
DI void p0a_load(const Args& a, int jb0, unsigned lds0, int wid, int lane) {
#pragma unroll
    for (int i = 0; i < 9; ++i) {
        const int p = wid + 8 * i;
        if (p < 66) {
            const int k = p >= 44 ? 2 : (p >= 22 ? 1 : 0), q = p - 22 * k;
            int jb = jb0 + 96 * k; if (jb >= 256) jb = jb0;
            const int cb = jb & 7, tb = jb >> 3;
            if (q < 11) { const int ci = q * 64 + lane, cj = ci < 670 ? ci : 669, row = div10(cj); int t = tb * 64 - 3 + row; t = t < 0 ? 0 : t;
                dma1(lds0 + p * 1024, (unsigned)(t * 1280 + (cj - 10 * row) * 16), (const char*)a.bk + cb * 160); }
            else if (q < 21) { const int ci = (q - 11) * 64 + lane, row = div10(ci);
                dma1(lds0 + p * 1024, (unsigned)(row * 1280 + (ci - 10 * row) * 16), (const char*)a.bv + (size_t)tb * 64 * 1280 + cb * 160); }
            else dma1(lds0 + p * 1024, (unsigned)((lane < 40 ? lane : 39) * 16), (const char*)a.shk + cb * 640);
        }
    }
}
DI void p0a_compute(int jb0, char* lds, int tid) {
    const int nj = jb0 + 192 < 256 ? 3 : 2;
#pragma unroll
    for (int it = 0; it < 2; ++it) {
        const int item = tid + it * 512, k = item >= 640 ? 2 : (item >= 320 ? 1 : 0), e = item - k * 320, tp = div10(e), cc = e - 10 * tp;
        if (k < nj && item < 960) {
            const int t0 = ((jb0 + 96 * k) >> 3) * 64;
            const lds_cptr in = (lds_cptr)lds + k * 22528;
            LAS char* tile = (LAS char*)lds + P0_OFF + k * 10240;
            u32x4 kk[5], vv[2], sh[4];
#pragma unroll
            for (int i = 0; i < 5; ++i) kk[i] = *(const LAS u32x4*)(in + (2 * tp + i) * 160 + cc * 16);
#pragma unroll
            for (int i = 0; i < 2; ++i) vv[i] = *(const LAS u32x4*)(in + 11264 + (2 * tp + i) * 160 + cc * 16);
#pragma unroll
            for (int i = 0; i < 4; ++i) sh[i] = *(const LAS u32x4*)(in + 21504 + cc * 64 + i * 16);
            if (t0 + 2 * tp < 3) {
#pragma unroll
                for (int i = 0; i < 3; ++i) if (t0 + 2 * tp + i - 3 < 0) kk[i] = (u32x4){0u, 0u, 0u, 0u};
            }
            const int dw = 31 - tp, pos = (((dw >> 2) ^ (cc & 7)) << 4) + (dw & 3) * 4;
#pragma unroll
            for (int qp = 0; qp < 4; ++qp) {
                f32x2 kx[5], r2[2];
#pragma unroll
                for (int i = 0; i < 5; ++i) kx[i] = (f32x2){blo(kk[i][qp]), bhi(kk[i][qp])};
                const f32x2 t0_ = {blo(sh[qp][0]), blo(sh[qp][2])}, t1_ = {bhi(sh[qp][0]), bhi(sh[qp][2])}, t2_ = {blo(sh[qp][1]), blo(sh[qp][3])}, t3_ = {bhi(sh[qp][1]), bhi(sh[qp][3])};
#pragma unroll
                for (int r = 0; r < 2; ++r)
                    r2[r] = (kx[r] * t3_ + kx[r + 1] * t2_ + kx[r + 2] * t1_ + kx[r + 3] * t0_) * (f32x2){blo(vv[r][qp]), bhi(vv[r][qp])};
                *(LAS unsigned*)(tile + (8 * cc + 2 * qp) * 128 + pos) = cvtpk(r2[1][0], r2[0][0]);
                *(LAS unsigned*)(tile + (8 * cc + 2 * qp + 1) * 128 + pos) = cvtpk(r2[1][1], r2[0][1]);
            }
        }
    }
}
DI void p0a_store(rsrc_t wr, int jb0, char* lds, int tid) {
    const int nj = jb0 + 192 < 256 ? 3 : 2;
#pragma unroll
    for (int it = 0; it < 4; ++it) { const int e = tid + it * 512, k = e >= 1280 ? 2 : (e >= 640 ? 1 : 0), f = e - k * 640, cl = f >> 3, d = f & 7;
        if (k < nj && e < 1920) { const int jb = jb0 + 96 * k;
            st16(wr, (unsigned)(WS_UR + ((size_t)((jb & 7) * 80 + cl) * S + (2047 - (jb >> 3) * 64 - 63) + 8 * d) * 2),
                 *(const LAS u32x4*)((lds_cptr)lds + P0_OFF + k * 10240 + cl * 128 + ((d ^ ((cl >> 3) & 7)) << 4))); } }
}
DI void p0b_load(const Args& a, int b, unsigned lds0, int wid, int lane) {
    const int cb = b & 7, tb = b >> 3;
#pragma unroll
    for (int i = 0; i < 3; ++i) { const int p = wid + 8 * i;
        if (p < 20) { const int ci = p * 64 + lane, row = div10(ci);
            dma1(lds0 + P0_OFF + p * 1024, (unsigned)(row * 1280 + (ci - 10 * row) * 16), (const char*)a.wo + (size_t)tb * 128 * 1280 + cb * 160); }
    }
}
DI void p0b_frag(rsrc_t wr, int b, char* lds, int wid, int lane, int i) {
    const int cb = b & 7, tb = b >> 3;
    const int j = lane & 15, g = lane >> 4, q = j >> 2, p = lane & 3;
    const lds_cptr brd = (lds_cptr)lds + P0_OFF + (16 * (g >> 1) + 4 * (g & 1) + q) * 160 + p * 8;
    if (wid + 8 * i < 20) { const int f = wid + 8 * i, kq = f / 5, cbk = f % 5;
        const bf16x8 w = tr_frag(brd + kq * 32 * 160 + cbk * 32, 1280);
        st16(wr, (unsigned)(WS_WO + ((size_t)(cb * 32 + tb) * 20 + f) * 1024 + lane * 16), __builtin_bit_cast(u32x4, w)); }
}

constexpr int PF_TILE = 6;
constexpr int TSLOT = 32 * 1040;
struct P1Ctx { const char* wsrc; const char* tsrc; unsigned wvoff, wlds, tvoff, tlds; };
DI P1Ctx p1_setup(const Args& a, int h, int r, unsigned lds0, int wid, int lane) {
    P1Ctx c;
    c.wsrc = (const char*)((wid >> 2) ? a.wuv : a.wuk) + (size_t)h * 256 + ((wid >> 1) & 1) * 128 + (size_t)(wid & 1) * 4 * 8 * 8192;
    c.wvoff = (unsigned)(lane >> 3) * 8192u + (unsigned)(((lane & 7) ^ (((lane >> 4) & 1) << 2)) * 16);
    c.wlds = lds0 + ((wid >> 2) * 2 + ((wid >> 1) & 1)) * 8192 + (wid & 1) * 4 * 1024;
    c.tsrc = (const char*)a.ckv + (size_t)(256 * r + 4 * wid) * 1024;
    c.tvoff = lane * 16; c.tlds = lds0 + 4 * wid * 1040;
    return c;
}
DI void p1_dma_w1(const P1Ctx& c, int ch, int slot, int i) { dma1(c.wlds + slot * TSLOT + i * 1024, c.wvoff, c.wsrc + (size_t)ch * 64 * 8192 + (size_t)i * 8 * 8192); }
template <int J> DI void p1_dma_t1(const P1Ctx& c, int t, int slot) {
    asm volatile("s_mov_b32 m0, %0\n\ts_nop 0\n\tglobal_load_lds_dwordx4 %1, %2 offset:%c3" :: "s"(c.tlds + slot * TSLOT + 16 * J), "v"(c.tvoff), "s"(c.tsrc + (size_t)t * 32 * 1024), "i"(J * 1024) : "memory");
}
DI void p1_prime_t(const P1Ctx& c) { p1_dma_t1<0>(c, 0, 3); p1_dma_t1<1>(c, 0, 3); p1_dma_t1<2>(c, 0, 3); p1_dma_t1<3>(c, 0, 3); }
DI void p1_prime_w(const P1Ctx& c) {
#pragma unroll
    for (int ch = 0; ch < 3; ++ch)
#pragma unroll
        for (int i = 0; i < 4; ++i) p1_dma_w1(c, ch, ch, i);
}
constexpr int STG_UR = 3 * TSLOT, STG_H = STG_UR + 16384;
template <int MAT>
DI void p1_body(const Args& a, const P1Ctx& c, rsrc_t wr, int b, int h, int r, char* lds, int wid, int lane, int tid, GAS u64* flags, u64 tag) {
    const int g4 = lane >> 4, q = (lane & 15) >> 2, p = lane & 3;
    const lds_cptr wrd = (lds_cptr)lds + (MAT * 2 + ((wid >> 1) & 1)) * 8192 + (8 * (g4 >> 1) + q) * 128 + (((wid & 1) ^ (q >> 1)) * 64) + (g4 & 1) * 32 + p * 8;
    const lds_cptr trd = (lds_cptr)lds + (lane & 31) * 1040 + (lane >> 5) * 16;
    const unsigned dst = (unsigned)((MAT ? WS_V : WS_K) + ((size_t)(h * 64 + 8 * r) * 8 + 2 * (wid & 3)) * 1024) + lane * 16;
    unsigned pfd = 0;
    bf16x8 Wf[32];
    f32x16 acc = f32x16{};
    const lds_cptr t0p = trd + 3 * TSLOT;
#define P1M(A_, W_) (MAT == 0 ? MFMA32(W_, A_, acc) : MFMA32(A_, W_, acc))
#pragma unroll
    for (int ch = 0; ch < 8; ++ch) {
        if (ch == 0) wait_bar<8>(); else wait_bar<4>();
        if (ch == 3 && b >= 160 && tid < 3) { const int jb = b - 160 + 96 * tid; if (jb < 256) flag_store(flags + FL_F0 + (jb & 7) * 32 + (jb >> 3), tag); }
        const lds_cptr wb = wrd + (ch % 3) * TSLOT;
        bf16x8 af[4];
#pragma unroll
        for (int s = 0; s < 4; ++s) { Wf[4 * ch + s] = tr_frag(wb + s * 2048, 512); af[s] = lds_frag(t0p + (4 * ch + s) * 32); }
        SBAR();
#pragma unroll
        for (int s = 0; s < 4; ++s) {
            if (ch >= 1) { if (ch + 2 <= 7) p1_dma_w1(c, ch + 2, (ch + 2) % 3, s);
                           else { if (s == 0) p1_dma_t1<0>(c, ch - 5, (ch + 2) % 3); if (s == 1) p1_dma_t1<1>(c, ch - 5, (ch + 2) % 3); if (s == 2) p1_dma_t1<2>(c, ch - 5, (ch + 2) % 3); if (s == 3) p1_dma_t1<3>(c, ch - 5, (ch + 2) % 3); } }
            acc = P1M(af[s], Wf[4 * ch + s]); PIN(acc);
            SBAR();
        }
    }
    f32x16 accp = acc;
    bf16x8 af[4];
    wait_bar<4>();
#pragma unroll
    for (int s = 0; s < 4; ++s) af[s] = lds_frag(trd + (8 % 3) * TSLOT + s * 32);
    SBAR();
#pragma unroll
    for (int i = 1; i < 8; ++i) {
        const int j = 7 + i;
        const lds_cptr tb = trd + (j % 3) * TSLOT, tn = trd + ((j + 1) % 3) * TSLOT;
#pragma unroll
        for (int s = 0; s < 32; ++s) {
            if (s == 28 && i < 7) {
                if (b < 160 && i == 4 && wid == 0) poll_flags(flags, lane < 32 ? FL_F0 + (b / 20) * 32 + lane : -1, tag);
                if (i + 2 <= 7) wait_bar<6>(); else wait_bar<2>();
                const unsigned lds0 = (unsigned)(uintptr_t)lds;
                if (i == 1) p0b_load(a, b, lds0, wid, lane);
                if (i == PF_TILE) {
                    const int id = (b * 8 + wid) * 60 + (lane < 59 ? lane : 59);
                    const char* p = id < 40960 ? (const char*)a.x + (size_t)id * 64 : (const char*)a.wq + (size_t)(id - 40960) * 64;
                    asm volatile("global_load_dword %0, %1, off" : "+v"(pfd) : "v"(p) : "memory"); }
                if (b < 160) {
                    if (i == 1) dma2(lds0 + STG_H + wid * 2048, lane * 16, (const char*)a.ssmk + (size_t)(4 * b) * 4096 + wid * 2048);
                    if (i == 4) dma2c(lds0 + STG_UR + wid * 2048, lane * 16, a.ws + WS_UR + (size_t)(4 * b) * 4096 + wid * 2048);
                }
                SBAR();
            }
            if (!(s & 1) && s + 4 < 32) { LGKM(2); SBAR(); }
            if (s == 0) acc = MAT == 0 ? MFMA32(Wf[0], af[0], (f32x16{})) : MFMA32(af[0], Wf[0], (f32x16{})); else acc = P1M(af[s & 3], Wf[s]);
            PIN(acc);
            if (s + 4 < 32) af[s & 3] = lds_frag(tb + (s + 4) * 32); else if (i < 7) af[s & 3] = lds_frag(tn + (s - 28) * 32);
            if (s == 2) st16(wr, dst + (i - 1) * 8192, pack8(accp, 0));
            if (s == 4) st16(wr, dst + (i - 1) * 8192 + 1024, pack8(accp, 1));
            if (i == 4 && (s == 8 || s == 14 || s == 20)) p0b_frag(wr, b, lds, wid, lane, (s - 8) / 6);
            if (i + 2 <= 7) {
                if (s == 6) p1_dma_t1<0>(c, i + 2, (j + 2) % 3);
                if (s == 11) p1_dma_t1<1>(c, i + 2, (j + 2) % 3);
                if (s == 16) p1_dma_t1<2>(c, i + 2, (j + 2) % 3);
                if (s == 21) p1_dma_t1<3>(c, i + 2, (j + 2) % 3);
            }
            SBAR();
        }
        accp = acc;
    }
    asm volatile("s_waitcnt vmcnt(2)" : "+v"(pfd) :: "memory");
    st16(wr, dst + 7 * 8192, pack8(accp, 0)); st16(wr, dst + 7 * 8192 + 1024, pack8(accp, 1));
#undef P1M
}

constexpr float QSCALE = 0.08838834764831845f * 1.4426950408889634f;
constexpr float THR = 8.f;
constexpr int QCH = 49152;
constexpr int KRING = 0, VRING = 65536, SPARE = 131072, QA_OFF = VRING + 32768;
#define MX3(a, b, c) __builtin_fmaxf(__builtin_fmaxf((a), (b)), (c))

DI void p2_body(const Args& a, int b, int h, int r, int blkA, int blkB, char* lds, int wid, int lane, int tid, GAS u64* flags, u64 tag) {
    const unsigned lds0 = (unsigned)(uintptr_t)lds;
    const int r32 = lane & 31, hi = lane >> 5, rg = wid & 3, par = wid >> 2;
    const int myblk = par ? blkB : blkA;
    const char* xg = (const char*)a.x + (size_t)(128 * myblk + 32 * rg) * 1280;
    const unsigned xvoff = (unsigned)(lane >> 3) * 1280u + (unsigned)(((lane & 7) ^ ((lane >> 4) & 7)) * 16);
    const unsigned xvoff2 = (unsigned)(lane >> 3) * 1280u + 8 * 1280u + (unsigned)(((lane & 7) ^ ((4 + (lane >> 4)) & 7)) * 16);
    const unsigned xlds = lds0 + 16384 + wid * 4096;
    const char* wg = (const char*)a.wq + (size_t)h * 256 + (wid >> 2) * 128 + (size_t)(wid & 3) * 2 * 8 * 8192;
    const unsigned wvoff = (unsigned)(lane >> 3) * 8192u + (unsigned)(((lane & 7) ^ (((lane >> 4) & 1) << 2)) * 16);
    const unsigned wvoff2 = wvoff + 8 * 8192u;
    const unsigned wlds = lds0 + 16384 + 32768 + (wid >> 2) * 8192 + (wid & 3) * 2048;
    const int g4 = lane >> 4, q = (lane & 15) >> 2, p = lane & 3;
    const int hsw = (q >> 1) & 1, ysw = hi ^ ((r32 >> 1) & 7);
    const lds_cptr wrd0 = (lds_cptr)lds + 16384 + 32768 + (8 * (g4 >> 1) + q) * 128 + (g4 & 1) * 32 + p * 8 + hsw * 64;
    const lds_cptr wrd1 = (lds_cptr)lds + 16384 + 32768 + (8 * (g4 >> 1) + q) * 128 + (g4 & 1) * 32 + p * 8 + (hsw ^ 1) * 64;
    lds_cptr xrd[4];
#pragma unroll
    for (int s = 0; s < 4; ++s) xrd[s] = (lds_cptr)lds + 16384 + wid * 4096 + r32 * 128 + (((2 * s) ^ ysw) << 4);
    f32x16 acc[4] = {};
#pragma unroll
    for (int c = 0; c < 2; ++c) { const char* xp = xg + c * 128; const char* wp = wg + (size_t)c * 64 * 8192; const unsigned so_ = ((c + 2) % 3) * QCH;
        dma1(xlds + so_, xvoff, xp); dma1(xlds + so_ + 1024, xvoff2, xp); dma1(xlds + so_ + 2048, xvoff, xp + 16 * 1280); dma1(xlds + so_ + 3072, xvoff2, xp + 16 * 1280);
        dma1(wlds + so_, wvoff, wp); dma1(wlds + so_ + 1024, wvoff2, wp); }
    unsigned so = 2 * QCH;
    const char* xp = xg + 2 * 128; const char* wp = wg + (size_t)2 * 64 * 8192;
    const unsigned kl0 = lds0 + KRING + wid * 2048, tvoff = lane * 16;
    const char* Kw = a.ws + WS_K + (size_t)h * 64 * 8192 + wid * 2048;
#define WFR(B0, B1, i) tr_frag((((i) & 1) ? (B1) : (B0)) + (((i) >> 1) & 1) * 8192 + ((i) >> 2) * 2048, 512)
    bf16x8 wf[4], xf[2];
#define P2CHUNK(MODE, EXTRA, LAST) do { \
        const unsigned sn = so == 0 ? 2 * QCH : so - QCH, sx = so == 2 * QCH ? 0 : so + QCH;        \
        const unsigned xl_ = xlds + sn, wl_ = wlds + sn; const char* xp2 = xp + 16 * 1280; \
        const lds_cptr wb0 = wrd0 + so, wb1 = wrd1 + so, wn0 = wrd0 + sx, wn1 = wrd1 + sx; \
        _Pragma("unroll") for (int i = 0; i < 16; ++i) { \
            if (i == 12 && !(LAST)) { \
                if ((EXTRA) == 2 && wid == 0) poll_flags(flags, lane < 8 ? FL_F1 + h * 8 + lane : -1, tag); \
                if ((MODE) == 0) wait_bar<6>(); else wait_bar<4>(); \
                if ((EXTRA) == 1 && tid == 0) { flag_store(flags + FL_FW + (b & 7) * 32 + (b >> 3), tag); if (b < 160) flag_store(flags + FL_F4 + b, tag); else flag_store(flags + FL_F1 + h * 8 + r, tag); } \
                SBAR(); } \
            acc[i & 3] = MFMA32(wf[i & 3], xf[(i >> 2) & 1], acc[i & 3]); PIN(acc[i & 3]); \
            if (i + 4 < 16) wf[i & 3] = WFR(wb0, wb1, i + 4); else if (!(LAST)) wf[i & 3] = WFR(wn0, wn1, i - 12); \
            if ((i & 3) == 0 && i + 4 < 16) xf[((i >> 2) + 1) & 1] = lds_frag(xrd[(i >> 2) + 1] + so); \
            if (i == 12 && !(LAST)) xf[0] = lds_frag(xrd[0] + sx); \
            if ((MODE) == 0) { \
                if (i == 0) { SETM0(xl_, 0); } if (i == 1) { GLDS(xvoff, xp, 0); SETM0(xl_, 1024); } \
                if (i == 3) { GLDS(xvoff2, xp, 0); SETM0(xl_, 2048); } if (i == 5) { GLDS(xvoff, xp2, 0); SETM0(xl_, 3072); } \
                if (i == 7) { GLDS(xvoff2, xp2, 0); SETM0(wl_, 0); } if (i == 9) { GLDS(wvoff, wp, 0); SETM0(wl_, 1024); } \
                if (i == 11) { GLDS(wvoff2, wp, 0); } \
            } else { \
                const char* ku_ = Kw + (size_t)(2 * ((MODE) - 1)) * 16384; \
                if (i == 0) { SETM0(kl0, (2 * ((MODE) - 1)) * 16384); } if (i == 1) GLDSC(tvoff, ku_, 0); if (i == 4) GLDSC(tvoff, ku_, 1024); \
                if (i == 6) { SETM0(kl0, (2 * ((MODE) - 1) + 1) * 16384); } if (i == 8) GLDSC(tvoff, ku_ + 16384, 0); if (i == 11) GLDSC(tvoff, ku_ + 16384, 1024); \
            } \
            SBAR(); \
        } \
        so = sx; xp += 128; wp += (size_t)64 * 8192; } while (0)
    wait_bar<6>();
    xf[0] = lds_frag(xrd[0] + so);
#pragma unroll
    for (int i = 0; i < 4; ++i) wf[i] = WFR(wrd0 + so, wrd1 + so, i);
    SBAR();
    P2CHUNK(0, 0, false);
    P2CHUNK(0, 1, false);
#pragma unroll 1
    for (int c = 2; c < 7; ++c) P2CHUNK(0, 0, false);
    P2CHUNK(0, 2, false);
    P2CHUNK(1, 0, false);
    P2CHUNK(2, 0, true);
#undef P2CHUNK
    wait_bar<63>();
    lds_ptr qw = (lds_ptr)lds + (par ? SPARE : QA_OFF) + rg * 8192 + lane * 16;
#pragma unroll
    for (int db = 0; db < 4; ++db) {
#pragma unroll
        for (int e = 0; e < 16; ++e) acc[db][e] *= QSCALE;
        *(LAS u32x4*)(qw + (2 * db) * 1024) = pack8(acc[db], 0);
        *(LAS u32x4*)(qw + (2 * db + 1) * 1024) = pack8(acc[db], 1);
    }
#undef WFR
}

DI void p3_body(rsrc_t wr, const char* ws, int h, int blkA, int blkB, char* lds, int wid, int lane, int tid, GAS u64* flags, u64 tag) {
    const unsigned lds0 = (unsigned)(uintptr_t)lds;
    const int r32 = lane & 31, hi = lane >> 5, rg = wid & 3, par = wid >> 2;
    const unsigned tvoff = lane * 16;
    const unsigned kl0 = lds0 + KRING + wid * 2048, vl0 = lds0 + VRING + wid * 2048;
    const char* Kw = ws + WS_K + (size_t)h * 64 * 8192 + wid * 2048; const char* Vw = ws + WS_V + (size_t)h * 64 * 8192 + wid * 2048;
    const lds_cptr krd = (lds_cptr)lds + KRING + par * 8192 + lane * 16;
    const lds_cptr vrd = (lds_cptr)lds + VRING + par * 8192 + lane * 16;
    int g0 = 0;
    bf16x8 qf[8];
#define VF(i) ((((i) & 3) * 2 + ((i) >> 2)) * 1024)
#define MFMA_C(D, A, B, C) asm volatile("v_mfma_f32_32x32x16_bf16 %0, %2, %3, %1" : "=&v"(D), "+v"(C) : "v"(A), "v"(B))
#define SLOT(X, Y, PN, PP, ODD, QK, SM, PV, FIRST, DM, PFK, MASK) do { \
        unsigned long long fire = 0; \
        _Pragma("unroll") for (int i = 0; i < 16; ++i) { \
            if (i < 8 && QK && !(i & 1)) { LGKM(2); SBAR(); } \
            if (i >= 8 && PV && !(i & 1)) { LGKM(2); SBAR(); } \
            if (i < 8) { if (QK) { if (i == 0) { if (FIRST) X = MFMA32(kf[0], qf[0], zero16); else MFMA_C(X, kf[0], qf[0], negm); } else X = MFMA32(kf[i & 3], qf[i], X); PIN(X); \
                                   if (i < 4) kf[i] = lds_frag(kp + ((ODD) ? 0 : 16384) + (i + 4) * 1024); } \
                         if (PV && i >= 4) vf[i - 4] = lds_frag(vp + ((ODD) ? 0 : 16384) + VF(i - 4)); } \
            else { if (PV) { o[i & 3] = MFMA32(vf[i & 3], __builtin_bit_cast(bf16x8, PP[(i - 8) >> 2]), o[i & 3]); PIN(o[i & 3]); if (i < 12) vf[i & 3] = lds_frag(vp + ((ODD) ? 0 : 16384) + VF(i - 4)); } \
                   if (PFK && i >= 12) kf[i - 12] = lds_frag(kp + ((ODD) ? 16384 : 0) + (i - 12) * 1024); } \
            if (!(ODD) && i == 5) { kp = (lds_cptr)(uintptr_t)((unsigned)(uintptr_t)kp ^ 0x8000u); PIN(kp); } \
            if (!(ODD) && i == 13) { vp = (lds_cptr)(uintptr_t)((unsigned)(uintptr_t)vp ^ 0x8000u); PIN(vp); } \
            if (DM) { \
                if (i == 0) SETM0(dk, (ODD) ? 16384 : 0); \
                if (i == 1) GLDSC(uoff, Kw, 0); \
                if (i == 3) GLDSC(uoff, Kw, 1024); \
                if ((DM) & 2) { \
                    if (i == 4) SETM0(dv, (ODD) ? 16384 : 0); \
                    if (i == 5) GLDSC(uoffv, Vw, 0); \
                    if (i == 7) GLDSC(uoffv, Vw, 1024); \
                    if (i == 11) { uoffv += 16384u; PIN(uoffv); } } \
                if (i == 8) { uoff += (j + 5 == n) ? 16384u * (unsigned)(1 - n) : 16384u; PIN(uoff); } \
                if ((ODD) && i == 9) { dk ^= 0x8000u; dv ^= 0x8000u; PINS(dk); PINS(dv); } } \
            if (SM) { \
                if (i == 0) { Y[0] = EXP2(Y[0]); Y[1] = EXP2(Y[1]); } else if (i < 15) Y[i + 1] = EXP2(Y[i + 1]); \
                if (i == 1) l2 += Y[0]; \
                if (i >= 1) { if (i & 1) l += Y[i]; else l2 += Y[i]; PIN(l); PIN(l2); } \
                if (i & 1) { PN[i >> 3][(i >> 1) & 3] = cvtpk(Y[i - 1], Y[i]); PIN(PN[i >> 3]); } \
                if (i < 15) PIN(Y); } \
            if (QK) { \
                if (MASK && i == 9 && (MASK == 1 || j + 1 >= n - 2)) { int qq = qrel - 64 * (j + 1 - (n - 2)); PIN(qq); \
                    _Pragma("unroll") for (int r = 0; r < 16; ++r) { const int c = (r & 3) + 8 * (r >> 2); if (c > qq) X[r] = -INFINITY; } } \
                if (i == 9) { mxa = MX3(X[0], X[1], X[2]); PIN(mxa); } \
                if (i == 10) { mxb = MX3(X[3], X[4], X[5]); mxa = MX3(mxa, X[6], X[7]); PIN(mxb); PIN(mxa); } \
                if (i == 11) { mxb = MX3(mxb, X[8], X[9]); PIN(mxb); } \
                if (i == 12) { mxa = MX3(mxa, X[10], X[11]); mxb = MX3(mxb, X[12], X[13]); PIN(mxb); PIN(mxa); } \
                if (i == 13) { mxa = MX3(mxa, X[14], X[15]); PIN(mxa); } \
                if (i == 14) { mxa = MX3(mxa, mxb, X[0]); PIN(mxa); } \
                if (i == 15 && !(FIRST)) { fire = __ballot(mxa > THR); PINS(fire); } } \
            SBAR(); \
        } \
        if (__builtin_expect((fire | pend) != 0, 0) || FIRST) { \
            if (PV && pend) { _Pragma("unroll") for (int d = 0; d < 4; ++d) _Pragma("unroll") for (int r = 0; r < 16; ++r) o[d][r] *= alp; } \
            pend = 0; \
            if (QK && (FIRST || fire)) { \
                auto rr = __builtin_amdgcn_permlane32_swap(__float_as_uint(mxa), __float_as_uint(mxa), false, false); \
                const float rm = __builtin_fmaxf(__uint_as_float(rr[0]), __uint_as_float(rr[1])); \
                if (FIRST) { const float dl = __builtin_fmaxf(rm, -1e30f); m = dl; \
                    _Pragma("unroll") for (int r = 0; r < 16; ++r) { X[r] -= dl; negm[r] = -dl; } PIN(negm); } \
                else { const float dl = __builtin_fmaxf(rm, 0.f); m += dl; alp = EXP2(-dl); l *= alp; l2 *= alp; pend = 1; \
                    const float ndl = -dl, aone = hi ? 0.f : 1.f; \
                    asm volatile("s_nop 1\n\tv_mfma_f32_32x32x2_f32 %0, %2, %3, %0\n\tv_mfma_f32_32x32x2_f32 %1, %2, %3, %1\n\ts_nop 7\n\ts_nop 7\n\ts_nop 7" : "+v"(X), "+v"(negm) : "v"(aone), "v"(ndl)); } } } \
        SBAR(); \
        } while (0)
#define TBAR() wait_bar_v<4>()
    const f32x16 zero16 = {};
#pragma unroll 1
    for (int pass = 0; pass < 2; ++pass) {
        const int blk = pass ? blkB : blkA, n = 2 * (blk + 1);
        dma2c(vl0 + (g0 & 3) * 16384, tvoff, Vw); dma2c(vl0 + ((g0 + 1) & 3) * 16384, tvoff, Vw + 16384);
        if (pass == 0) wait_bar<4>();
        if (pass == 0) {
            const lds_cptr qr = (lds_cptr)lds + QA_OFF + rg * 8192 + lane * 16;
#pragma unroll
            for (int s = 0; s < 8; ++s) qf[s] = lds_frag(qr + s * 1024);
        }
        float m = -1e30f, l = 0.f, l2 = 0.f, alp = 1.f, mxa = 0.f, mxb = 0.f; unsigned long long pend = 0; f32x16 o[4] = {};
        u32x4 pk0[2] = {}, pk1[2] = {};
        f32x16 s0, s1, negm;
        bf16x8 kf[4], vf[4] = {};
        const int qrel = 32 * rg + r32 - 32 * par - 4 * hi;
        lds_cptr kp = krd + (g0 & 2) * 16384, vp = vrd + ((g0 & 2) ^ 2) * 16384;
        unsigned dk = kl0 + (g0 & 2) * 16384, dv = vl0 + ((g0 + 2) & 2) * 16384;
        unsigned uoffv = tvoff + 2 * 16384;
        unsigned uoff = tvoff + ((4 < n) ? 4 : 4 - n) * 16384;
#pragma unroll
        for (int i = 0; i < 4; ++i) kf[i] = lds_frag(kp + i * 1024);
        int j = -1;
        SLOT(s0, s1, pk1, pk0, true, true, false, false, true, false, true, 2);
        wait_bar_v<63>();
        j = 0;
        SLOT(s1, s0, pk0, pk1, false, true, true, false, false, 3, true, 2);
        TBAR();
        if (pass == 1 && tid == 0) flag_store(flags + FL_F3 + blkA * 32 + h, tag);
#pragma unroll 1
        for (j = 1; j < n - 3; j += 2) {
            SLOT(s0, s1, pk1, pk0, true, true, true, true, false, 3, true, 0);
            TBAR();
            ++j;
            SLOT(s1, s0, pk0, pk1, false, true, true, true, false, 3, true, 0);
            TBAR();
            --j;
        }
        if (n >= 4) {
            j = n - 3;
            SLOT(s0, s1, pk1, pk0, true, true, true, true, false, 3, true, 1);
            TBAR();
            j = n - 2;
            SLOT(s1, s0, pk0, pk1, false, true, true, true, false, 1, true, 1);
            wait_bar_v<2>();
        }
        j = n - 1;
        SLOT(s0, s1, pk1, pk0, true, false, true, true, false, 1, false, 0);
        wait_bar_v<2>();
        if (pass == 0) {
            const lds_cptr qr = (lds_cptr)lds + SPARE + rg * 8192 + lane * 16;
#pragma unroll
            for (int s = 0; s < 8; ++s) qf[s] = lds_frag(qr + s * 1024);
        }
        j = n;
        SLOT(s1, s0, pk0, pk1, false, false, false, true, false, false, false, 0);
        g0 += n;
        {
            l += l2;
            auto rr = __builtin_amdgcn_permlane32_swap(__float_as_uint(l), __float_as_uint(l), false, false);
            l = __uint_as_float(rr[0]) + __uint_as_float(rr[1]);
            wait_bar<63>();
            LAS float* ml = (LAS float*)((lds_ptr)lds + SPARE);
            *(LAS f32x2*)(ml + (wid * 64 + lane) * 2) = (f32x2){m, l};
            lds_ptr sendp = (lds_ptr)lds + VRING + par * 32768 + rg * 8192 + lane * 16;
            lds_cptr recvp = (lds_cptr)lds + VRING + (par ^ 1) * 32768 + rg * 8192 + lane * 16;
            f32x16 keep[2];
            if (par) {
#pragma unroll
                for (int d = 0; d < 2; ++d) { keep[d] = o[2 + d];
#pragma unroll
                    for (int g = 0; g < 4; ++g) *(LAS f32x4*)(sendp + (d * 4 + g) * 1024) = (f32x4){o[d][4 * g], o[d][4 * g + 1], o[d][4 * g + 2], o[d][4 * g + 3]}; }
            } else {
#pragma unroll
                for (int d = 0; d < 2; ++d) { keep[d] = o[d];
#pragma unroll
                    for (int g = 0; g < 4; ++g) *(LAS f32x4*)(sendp + (d * 4 + g) * 1024) = (f32x4){o[2 + d][4 * g], o[2 + d][4 * g + 1], o[2 + d][4 * g + 2], o[2 + d][4 * g + 3]}; }
            }
            wait_bar<63>();
            const f32x2 mlo = *(const LAS f32x2*)(ml + ((wid ^ 4) * 64 + lane) * 2);
            const float mm = __builtin_fmaxf(m, mlo[0]), fo = EXP2(m - mm), fx = EXP2(mlo[0] - mm);
            const float inv = __builtin_amdgcn_rcpf(l * fo + mlo[1] * fx), sc = fo * inv, sx = fx * inv;
            const unsigned cdst = (unsigned)(WS_CTX + ((size_t)(blk * 4 + rg) * 32 + h) * 8192 + (par * 4) * 1024) + lane * 16;
#pragma unroll
            for (int d = 0; d < 2; ++d) {
#pragma unroll
                for (int g = 0; g < 4; ++g) {
                    const f32x4 v4 = *(const LAS f32x4*)(recvp + (d * 4 + g) * 1024);
#pragma unroll
                    for (int e = 0; e < 4; ++e) keep[d][4 * g + e] = keep[d][4 * g + e] * sc + v4[e] * sx;
                }
                st16(wr, cdst + (2 * d) * 1024, pack8(keep[d], 0));
                st16(wr, cdst + (2 * d + 1) * 1024, pack8(keep[d], 1));
            }
            if (pass == 0) wait_bar<63>();
            else signal_flag(flags + FL_F3 + blk * 32 + h, tag, tid);
        }
    }
#undef SLOT
#undef TBAR
#undef VF
}

constexpr int CSTR = 4160;
constexpr int CCH = 4 * CSTR + 4608;
DI void p4_body(const Args& a, rsrc_t wr, int b, int h, int r, char* lds, int wid, int lane, int tid, GAS u64* flags, u64 tag) {
    const int ch = wid >> 1, c = 4 * b + ch, half = wid & 1;
    lds_ptr base = (lds_ptr)lds + ch * CCH;
    const lds_cptr ur = (lds_cptr)lds + STG_UR + ch * 4096, hs = (lds_cptr)lds + STG_H + ch * 4096;
    u32x4 w0[2], w1[2], hv[2];
#pragma unroll
    for (int it = 0; it < 2; ++it) { const int y8 = half * 128 + it * 64 + lane;
        w0[it] = *(const LAS u32x4*)(ur + 16 * y8); w1[it] = *(const LAS u32x4*)(ur + 16 * (y8 < 255 ? y8 + 1 : y8)); if (y8 == 255) w1[it] = (u32x4){0u, 0u, 0u, 0u};
        hv[it] = *(const LAS u32x4*)(hs + 16 * y8); }
    wait_bar<63>();
    const float Dc = bf2f(a.ssmD[c]);
#pragma unroll
    for (int it = 0; it < 2; ++it) {
        const int y8 = half * 128 + it * 64 + lane;
        unsigned w[6] = {w0[it][0], w0[it][1], w0[it][2], w0[it][3], w1[it][0], w1[it][1]}, o[5];
#pragma unroll
        for (int e = 0; e < 5; ++e) o[e] = __builtin_amdgcn_alignbit(w[e + 1], w[e], 16);
#pragma unroll
        for (int sg = 0; sg < 4; ++sg) { const int e = sg >> 1; u32x4 v;
            if (sg & 1) v = (u32x4){o[e], o[e + 1], o[e + 2], o[e + 3]}; else v = (u32x4){w[e], w[e + 1], w[e + 2], w[e + 3]};
            *(LAS u32x4*)(base + sg * CSTR + y8 * 16) = v; }
        *(LAS u32x4*)(base + 4 * CSTR + 512 + y8 * 16) = hv[it];
    }
    if (half == 1 && lane < 8) { const int sg = lane >> 1, y8 = 256 + (lane & 1); *(LAS u32x4*)(base + sg * CSTR + y8 * 16) = (u32x4){0u, 0u, 0u, 0u}; }
    if (half == 0 && lane < 32) *(LAS u32x4*)(base + 4 * CSTR + lane * 16) = (u32x4){0u, 0u, 0u, 0u};
    wait_bar<63>();
    const int j = lane & 15, g = lane >> 4;
    lds_cptr ard = (lds_cptr)base + ((3 - j) & 3) * CSTR + (2031 - j - ((3 - j) & 3) + 8 * g) * 2 - half * 64 - 62 * 64, ard2 = ard + 8;
    const lds_cptr brd = (lds_cptr)base + 4 * CSTR + (240 + 16 * j + 8 * g) * 2 - half * 64 - 6 * 64;
    PIN(ard2);
    f32x4 acc[8];
#pragma unroll
    for (int q = 0; q < 8; ++q) { acc[q] = (f32x4){0.f, 0.f, 0.f, 0.f}; PIN(acc[q]); }
    lds_cptr ard1 = ard; PIN(ard1);
#define P4LD1(A, B, RC, u) do { const s16x4 lo = *(const LAS s16x4*)(ard1 + (62 - (RC) - 8 * (u)) * 64), hi2 = *(const LAS s16x4*)(ard2 + (62 - (RC) - 8 * (u)) * 64); \
            A[u] = __builtin_shufflevector(lo, hi2, 0, 1, 2, 3, 4, 5, 6, 7); B[u] = lds_frag(brd + (8 * (u) - (RC) + 6) * 64); } while (0)
#define P4LOAD(A, B, RC) do { _Pragma("unroll") for (int u = 0; u < 8; ++u) P4LD1(A, B, RC, u); } while (0)
#define P4MM(A, B, AN, BN, RCN, NEXT) do { int m_ = 0; _Pragma("unroll") for (int u = 0; u < 8; ++u) _Pragma("unroll") for (int v = 0; v + u < 8; ++v) { \
            acc[u + v] = MFMA16(A[u], B[v], acc[u + v]); PIN(acc[u + v]); \
            const int k_ = u * 8 - u * (u - 1) / 2 + v; (void)m_; \
            if ((NEXT) && (k_ & 3) == 1 && (k_ >> 2) < 8) P4LD1(AN, BN, RCN, (k_ >> 2)); \
            SBAR(); } } while (0)
    {
        bf16x8 A0[8], B0[8], A1[8], B1[8];
        P4LOAD(A0, B0, 0); SBAR();
        P4MM(A0, B0, A1, B1, 2, true);
        P4MM(A1, B1, A0, B0, 4, true);
        P4MM(A0, B0, A1, B1, 6, true);
        P4MM(A1, B1, A0, B0, 0, false);
    }
#undef P4LD1
#undef P4LOAD
#undef P4MM
    wait_bar<0>();
    if (tid == 0) flag_store(flags + FL_F1 + h * 8 + r, tag);
    lds_ptr ex = base + CSTR;
    f32x4 keep[4];
    if (half) {
#pragma unroll
        for (int q = 0; q < 4; ++q) { *(LAS f32x4*)(ex + 4096 + q * 1024 + lane * 16) = acc[q]; keep[q] = acc[4 + q]; }
    } else {
#pragma unroll
        for (int q = 0; q < 4; ++q) { *(LAS f32x4*)(ex + q * 1024 + lane * 16) = acc[4 + q]; keep[q] = acc[q]; }
    }
    wait_bar<63>();
    u32x2 zp[4];
#pragma unroll
    for (int q = 0; q < 4; ++q) {
        const f32x4 o = *(const LAS f32x4*)(ex + (half ^ 1) * 4096 + q * 1024 + lane * 16);
        const f32x4 y = keep[q] + o;
        const int t = 256 * (half * 4 + q) + 16 * j + 4 * g;
        const s16x4 kvr = *(const LAS s16x4*)(base + (2047 - t - 3) * 2);
        float z[4];
#pragma unroll
        for (int r = 0; r < 4; ++r) { const float kv = bf2f((u16)kvr[3 - r]); z[r] = y[r] + kv * Dc; }
        zp[q] = (u32x2){cvtpk(z[0], z[1]), cvtpk(z[2], z[3])};
    }
#pragma unroll
    for (int q = 0; q < 4; q += 2) {
        auto s0 = __builtin_amdgcn_permlane16_swap(zp[q][0], zp[q + 1][0], false, false);
        auto s1 = __builtin_amdgcn_permlane16_swap(zp[q][1], zp[q + 1][1], false, false);
        const int t = 256 * (half * 4 + q + (g & 1)) + 16 * j + 8 * (g >> 1);
        st16(wr, (unsigned)(WS_Z + ((size_t)c * S + t) * 2), (u32x4){s0[0], s1[0], s0[1], s1[1]});
    }
}

constexpr int OCH = 40960;
DI u32x4 ld16c(rsrc_t r, unsigned off) { return __builtin_amdgcn_raw_buffer_load_b128(r, off, 0, 16); }
DI void p5_body(const Args& a, rsrc_t wr, int mt, int nt, int ks, char* lds, int wid, int lane, int tid, GAS u64* flags, u64 tag) {
    const unsigned lds0 = (unsigned)(uintptr_t)lds;
    const int rg = wid & 3, kh = wid >> 2;
    const int j = lane & 15, g = lane >> 4;
    const unsigned tv = lane * 16;
    const char* wg = a.ws + WS_WO + ((size_t)(2 * nt + ((wid >> 1) & 1)) * 32 + 8 * ks + 4 * (wid >> 2)) * 20480 + (wid & 1) * 5120;
    const unsigned wl = lds0 + (wid >> 1) * 10240 + (wid & 1) * 5120;
#define WLOAD(slot, st) do { const char* p_ = wg + (size_t)((st) >> 1) * 20480 + ((st) & 1) * 10240; \
        asm volatile("s_mov_b32 m0, %0\n\ts_nop 0\n\tglobal_load_lds_dwordx4 %1, %2 sc1\n\tglobal_load_lds_dwordx4 %1, %2 offset:1024 sc1\n\tglobal_load_lds_dwordx4 %1, %2 offset:2048 sc1\n\t" \
                     "global_load_lds_dwordx4 %1, %2 offset:3072 sc1" :: "s"(wl + (slot) * OCH), "v"(tv), "s"(p_) : "memory"); \
        dma1c(wl + (slot) * OCH + 4096, tv, p_ + 4096); } while (0)
    WLOAD(0, 0); WLOAD(1, 1);
    if (wid == 0) {
        poll_flags(flags, lane < 40 ? FL_F4 + nt * 40 + lane : (lane < 44 ? FL_F0 + (4 * (ks & 1) + lane - 40) * 32 + 2 * mt + (ks >> 1) : -1), tag);
        poll_flags(flags, lane < 8 ? FL_F3 + mt * 32 + 8 * ks + lane : -1, tag);
    }
    wait_bar<63>();
    const char* cg = a.ws + WS_CTX + ((size_t)(4 * mt + rg) * 32 + 8 * ks + 4 * kh) * 8192;
    const unsigned cv = (g >> 1) * 1024 + ((g & 1) * 32 + j) * 16;
    u32x4 cf[3][4];
#define CLOAD(slot, st) do { const char* p_ = cg + (size_t)((st) >> 1) * 8192 + ((st) & 1) * 4096; \
        asm volatile("global_load_dwordx4 %0, %4, %5 sc1\n\tglobal_load_dwordx4 %1, %4, %5 offset:256 sc1\n\tglobal_load_dwordx4 %2, %4, %5 offset:2048 sc1\n\tglobal_load_dwordx4 %3, %4, %5 offset:2304 sc1" \
                     : "=&v"(cf[slot][0]), "=&v"(cf[slot][1]), "=&v"(cf[slot][2]), "=&v"(cf[slot][3]) : "v"(cv), "s"(p_) : "memory"); } while (0)
    CLOAD(0, 0); CLOAD(1, 1);
    unsigned zr[5];
#pragma unroll
    for (int it = 0; it < 5; ++it) { const int e = tid + it * 512, cl2 = e >> 4, d = e & 15;
        zr[it] = ld4c(wr, (unsigned)(WS_Z + ((size_t)(nt * 160 + cl2) * S + mt * 128 + ks * 32 + 2 * d) * 2)); }
    u32x2 xv[3]; float gt[3];
#pragma unroll
    for (int k = 0; k < 3; ++k) { const int i = wid + 8 * k < 20 ? wid + 8 * k : wid, t = mt * 128 + ks * 32 + 16 * ((i % 10) / 5) + j, c = nt * 160 + 80 * (i / 10) + 16 * (i % 5) + 4 * g;
        xv[k] = *(const u32x2*)(a.x + (size_t)t * M + c); gt[k] = __uint_as_float(ld4c(wr, (unsigned)(WS_G + t * 4))); }
    const lds_cptr brd = (lds_cptr)lds + kh * 20480 + lane * 16;
    f32x4 acc[2][10] = {};
    bf16x8 wf[4];
#define WFI(bb, i) lds_frag((bb) + (((i) / 5) & 1) * 10240 + (((i) / 10) * 5 + (i) % 5) * 1024)
    wait_bar<4>();
#pragma unroll
    for (int k = 0; k < 3; ++k) { PIN(xv[k]); PIN(gt[k]); }
#pragma unroll
    for (int it = 0; it < 5; ++it) PIN(zr[it]);
#pragma unroll
    for (int i = 0; i < 4; ++i) wf[i] = WFI(brd, i);
    SBAR();
#pragma unroll
    for (int st = 0; st < 8; ++st) {
        asm volatile("" : "+v"(cf[st % 3][0]), "+v"(cf[st % 3][1]), "+v"(cf[st % 3][2]), "+v"(cf[st % 3][3]));
        const lds_cptr bb = brd + (st % 3) * OCH, bn = brd + ((st + 1) % 3) * OCH;
#pragma unroll
        for (int i = 0; i < 20; ++i) {
            const int ks2 = i / 10, c10 = i % 10;
            if (i == 16 && st < 7) { if (st + 2 < 8) wait_bar<9>(); else wait_bar<0>(); SBAR(); }
            acc[0][c10] = MFMA16(wf[i & 3], __builtin_bit_cast(bf16x8, cf[st % 3][2 * ks2]), acc[0][c10]); PIN(acc[0][c10]); SBAR();
            acc[1][c10] = MFMA16(wf[i & 3], __builtin_bit_cast(bf16x8, cf[st % 3][2 * ks2 + 1]), acc[1][c10]); PIN(acc[1][c10]);
            if (i + 4 < 20) wf[i & 3] = WFI(bb, i + 4); else if (st < 7) wf[i & 3] = WFI(bn, i - 16);
            if (st + 2 < 8) { if (i == 1) WLOAD((st + 2) % 3, st + 2); if (i == 10) CLOAD((st + 2) % 3, st + 2); }
            SBAR();
        }
    }
#undef WFI
#undef WLOAD
#undef CLOAD
    wait_bar<0>();
    {
        lds_ptr sp = (lds_ptr)lds + wid * 10240 + lane * 16; lds_cptr rp = (lds_cptr)lds + (wid ^ 4) * 10240 + lane * 16;
        f32x4 keep[10];
        if (kh) {
#pragma unroll
            for (int i = 0; i < 10; ++i) { *(LAS f32x4*)(sp + i * 1024) = acc[i / 5][i % 5]; keep[i] = acc[i / 5][5 + i % 5]; }
        } else {
#pragma unroll
            for (int i = 0; i < 10; ++i) { *(LAS f32x4*)(sp + i * 1024) = acc[i / 5][5 + i % 5]; keep[i] = acc[i / 5][i % 5]; }
        }
        lds_ptr zb = (lds_ptr)lds + 81920;
#pragma unroll
        for (int it = 0; it < 5; ++it) { const int e = tid + it * 512, cl2 = e >> 4, d = e & 15; *(LAS unsigned*)(zb + cl2 * 68 + d * 4) = zr[it]; }
        wait_bar<63>();
#pragma unroll
        for (int i = 0; i < 10; ++i) keep[i] += *(const LAS f32x4*)(rp + i * 1024);
        const int tile = mt * 4 + nt;
        if (rg != ks) {
            const unsigned dst = (unsigned)(WS_SL + (((size_t)tile * 4 + rg) * 4 + ks) * 20480 + kh * 10240) + lane * 16;
#pragma unroll
            for (int i = 0; i < 10; ++i) st16(wr, dst + i * 1024, __builtin_bit_cast(u32x4, keep[i]));
        } else {
            lds_ptr op = (lds_ptr)lds + 98304 + kh * 10240 + lane * 16;
#pragma unroll
            for (int i = 0; i < 10; ++i) *(LAS f32x4*)(op + i * 1024) = keep[i];
        }
        signal_flag(flags + FL_F5 + tile * 4 + ks, tag, tid);
        if (wid == 0) poll_flags(flags, (lane < 4 && lane != ks) ? FL_F5 + tile * 4 + lane : -1, tag);
        wait_bar<63>();
        u32x4 pf[3][3];
#pragma unroll
        for (int k = 0; k < 3; ++k) { const int i = wid + 8 * k < 20 ? wid + 8 * k : wid;
            const unsigned src = (unsigned)(WS_SL + ((size_t)tile * 4 + ks) * 4 * 20480) + i * 1024 + lane * 16;
#pragma unroll
            for (int f = 0; f < 3; ++f) pf[k][f] = ld16c(wr, src + (unsigned)(f + (f >= ks ? 1 : 0)) * 20480u); }
#pragma unroll
        for (int k = 0; k < 3; ++k) if (wid + 8 * k < 20) {
            const int i = wid + 8 * k, tl = 16 * ((i % 10) / 5) + j, cl = 80 * (i / 10) + 16 * (i % 5) + 4 * g;
            f32x4 av = *(const LAS f32x4*)((lds_cptr)lds + 98304 + i * 1024 + lane * 16);
#pragma unroll
            for (int f = 0; f < 3; ++f) av += __builtin_bit_cast(f32x4, pf[k][f]);
            float r4[4];
#pragma unroll
            for (int e = 0; e < 4; ++e) { const float z = bf2f(*(const LAS u16*)((lds_cptr)lds + 81920 + (cl + e) * 68 + tl * 2));
                const float xe = (e & 1) ? bhi(xv[k][e >> 1]) : blo(xv[k][e >> 1]);
                r4[e] = gt[k] * av[e] + (1.f - gt[k]) * (xe * z); }
            *(u32x2*)(a.out + (size_t)(mt * 128 + ks * 32 + tl) * M + nt * 160 + cl) = (u32x2){cvtpk(r4[0], r4[1]), cvtpk(r4[2], r4[3])};
        }
    }
}

__global__ __launch_bounds__(512, 2) void k_fused(const u16* x, const u16* ckv, const u16* wq, const u16* wuk, const u16* wuv, const u16* wo, const u16* bk, const u16* bv,
                                                  const u16* shk, const u16* ssmk, const u16* ssmD, const u16* gw, u16* out, char* ws) {
    extern __shared__ __attribute__((aligned(16))) char lds[];
    const Args a = {x, ckv, wq, wuk, wuv, wo, bk, bv, shk, ssmk, ssmD, gw, out, ws};
    const int tid = threadIdx.x, lane = tid & 63, wid = rfl(tid >> 6);
    const int b = blockIdx.x, xcd = b & 7, idx = b >> 3, h = xcd * 4 + (idx >> 3), r = idx & 7;
    const unsigned lds0 = (unsigned)(uintptr_t)lds;
    const rsrc_t wr = __builtin_amdgcn_make_buffer_rsrc(ws, 0, (int)WS_END, 0x00020000);
    GAS u64* flags = (GAS u64*)(ws + WS_F);
    const P1Ctx c1 = p1_setup(a, h, r, lds0, wid, lane);
    const u64 tag = *(const __attribute__((address_space(4))) u64*)(ws + WS_F) + 1;
    if (b >= 160) {
        u32x4 xg[3][2];
        const u32x4 g0 = *(const u32x4*)(gw + lane * 8), g1 = *(const u32x4*)(gw + 512 + (lane & 15) * 8);
#pragma unroll
        for (int k = 0; k < 3; ++k) { int jb = b - 160 + 96 * k; if (jb >= 256) jb = b - 160; const u16* xr = x + (size_t)(64 * (jb >> 3) + 8 * (jb & 7) + wid) * M;
            xg[k][0] = *(const u32x4*)(xr + lane * 8); xg[k][1] = *(const u32x4*)(xr + 512 + (lane & 15) * 8); }
        p0a_load(a, b - 160, lds0, wid, lane);
        p1_prime_t(c1);
        wait_bar<4>();
#pragma unroll
        for (int k = 0; k < 3; ++k) { PIN(xg[k][0]); PIN(xg[k][1]); }
        p0a_compute(b - 160, lds, tid);
        wait_bar<63>();
        p1_prime_w(c1);
        p0a_store(wr, b - 160, lds, tid);
#pragma unroll
        for (int k = 0; k < 3; ++k) { const int jb = b - 160 + 96 * k; float sum = 0.f, s1 = 0.f;
#pragma unroll
            for (int e = 0; e < 4; ++e) { sum += blo(xg[k][0][e]) * blo(g0[e]) + bhi(xg[k][0][e]) * bhi(g0[e]); s1 += blo(xg[k][1][e]) * blo(g1[e]) + bhi(xg[k][1][e]) * bhi(g1[e]); }
            if (lane < 16) sum += s1;
            sum = wave_sum(sum);
            if (lane == 0 && jb < 256) st4(wr, (unsigned)(WS_G + (64 * (jb >> 3) + 8 * (jb & 7) + wid) * 4), __float_as_uint(1.f / (1.f + __expf(-sum)))); }
    } else { p1_prime_t(c1); p1_prime_w(c1); }
    if (wid < 4) p1_body<0>(a, c1, wr, b, h, r, lds, wid, lane, tid, flags, tag); else p1_body<1>(a, c1, wr, b, h, r, lds, wid, lane, tid, flags, tag);
    wait_bar<63>();
    if (b < 160) p4_body(a, wr, b, h, r, lds, wid, lane, tid, flags, tag);
    wait_bar<63>();
    p2_body(a, b, h, r, 15 - r, r, lds, wid, lane, tid, flags, tag);
    p3_body(wr, ws, h, 15 - r, r, lds, wid, lane, tid, flags, tag);
    {
        const int ks = xcd >> 1, mt = (xcd & 1) * 8 + (idx >> 2), nt = idx & 3;
        if (wid == 0) poll_flags(flags, lane < 16 ? FL_FW + (2 * nt + (lane >> 3)) * 32 + 8 * ks + (lane & 7) : -1, tag);
        wait_bar<0>();
        p5_body(a, wr, mt, nt, ks, lds, wid, lane, tid, flags, tag);
    }
    if (b == 0 && tid == 0) flag_store(flags, tag);
}

extern "C" void kernel_launch(void* const* d_in, const int* in_sizes, int n_in,
                              void* d_out, int out_size, void* d_ws, size_t ws_size,
                              hipStream_t stream) {
    static bool attr = false;
    if (!attr) { (void)hipFuncSetAttribute((const void*)k_fused, hipFuncAttributeMaxDynamicSharedMemorySize, LDS_BYTES); attr = true; }
    k_fused<<<256, 512, LDS_BYTES, stream>>>((const u16*)d_in[0], (const u16*)d_in[1], (const u16*)d_in[2], (const u16*)d_in[3], (const u16*)d_in[4], (const u16*)d_in[5],
                                             (const u16*)d_in[7], (const u16*)d_in[8], (const u16*)d_in[9], (const u16*)d_in[10], (const u16*)d_in[11], (const u16*)d_in[12],
                                             (u16*)d_out, (char*)d_ws);
}
```
